```python
import torch

def _softmax(x, axis=-1):
    return torch.softmax(x, dim=axis)
def _gelu(x):
    return torch.nn.functional.gelu(x, approximate="tanh")
def _silu(x):
    return torch.nn.functional.silu(x)
def _sigmoid(x):
    return torch.sigmoid(x)
def _layernorm(x, axis=-1, eps=1e-5):
    m = x.mean(dim=axis, keepdim=True)
    v = x.var(dim=axis, keepdim=True, unbiased=False)
    return (x - m) / torch.sqrt(v + eps)
def _scatter_add(tgt, idx, src, axis=0):
    return tgt.clone().index_add_(axis, idx, src)
def _segment_sum(src, seg, n):
    out = torch.zeros((n,) + src.shape[1:], dtype=src.dtype)
    return out.index_add_(0, seg, src)
def _pad(x, pw):
    flat = tuple(v for pair in reversed(pw) for v in pair)
    return torch.nn.functional.pad(x, flat)

kernel_name = 'standalone__mha_block_cached__sampling'

def setup_inputs():

    _w = (lambda a: (lambda t: t.to(torch.bfloat16) if t.is_floating_point() else t)(torch.as_tensor(a)))
    B, Sq, D = 16, 8, 1024
    H, Sk, Dh = 32, 2048, 128
    return {
        "x": _w((lambda *s: torch.randn(*s, dtype=torch.bfloat16) * 0.8)(B, Sq, D)),
        "k_cache": _w((lambda *s: torch.randn(*s, dtype=torch.bfloat16) * 0.8)(B, H, Sk, Dh)),
        "v_cache": _w((lambda *s: torch.randn(*s, dtype=torch.bfloat16) * 0.05)(B, H, Sk, Dh)),
        "wq": _w((lambda *s: torch.randn(*s, dtype=torch.bfloat16) * 0.8)(D, H * Dh) * (D ** -0.5)),
        "wo": _w((lambda *s: torch.randn(*s, dtype=torch.bfloat16) * 0.05)(H * Dh, D) * ((H * Dh) ** -0.5)),
    }

def reference(x, k_cache, v_cache, wq, wo):
    B, Sq, D = x.shape
    H, Dh = 32, 128
    q = (lambda x, *d: x.permute(*d))((x @ wq).reshape(B, Sq, H, Dh), 0, 2, 1, 3)
    scores = torch.einsum("bhqd,bhkd->bhqk", q, k_cache) * (Dh ** -0.5)
    p = _softmax(scores, axis=-1)
    o = torch.einsum("bhqk,bhkd->bhqd", p, v_cache)
    return (lambda x, *d: x.permute(*d))(o, 0, 2, 1, 3).reshape(B, Sq, H * Dh) @ wo
```

```cpp
#include <hip/hip_runtime.h>
#include <hip/hip_bf16.h>
#include <stdint.h>

#define GAS __attribute__((address_space(1)))
#define LAS __attribute__((address_space(3)))
typedef unsigned v4u __attribute__((ext_vector_type(4)));
typedef unsigned v2u __attribute__((ext_vector_type(2)));
typedef float v4f __attribute__((ext_vector_type(4)));
typedef float v2f __attribute__((ext_vector_type(2)));
typedef short v4s __attribute__((ext_vector_type(4)));
typedef __bf16 v8bf __attribute__((ext_vector_type(8)));
typedef __bf16 v2bf __attribute__((ext_vector_type(2)));

namespace {
constexpr int NB = 16, SQ = 8, DM = 1024, NH = 32, SK = 2048, DH = 128;
constexpr int NWG = 256, NTHR = 768, NATT = 3, NHELP = 8;
constexpr int PIECE = 1056;
constexpr int UNIT = 8 * PIECE;
constexpr int NR = 15;
constexpr int NUNIT = 256;
#define WIN 20
#define WQ_AUX 0
#define WO_AUX 0
constexpr int WM1 = WIN - 1, PUB_A = WM1 / 8, PUB_B = WM1 % 8;
constexpr int L_CTRL = NR * UNIT;
constexpr int L_SCR = L_CTRL + 256;
constexpr int L_MRG = L_SCR;
constexpr int MRG_REC = 8192 + 512;
constexpr int L_STG = L_SCR + 2 * MRG_REC;
constexpr int LDS_BYTES = L_SCR + 32768;
static_assert(LDS_BYTES <= 160 * 1024 && L_STG + 8192 <= LDS_BYTES, "LDS map");
enum { C_FULL = 0, C_REL = 4, C_Q1 = 8, C_Q2 = 9, C_QREAD = 10, C_MRG = 12, C_ORC = 14 };
constexpr size_t OBUF_BYTES = (size_t)2 * 4 * NH * 4096;
constexpr unsigned SENT = 0xFFFFFFFFu;
constexpr float CSC = 0.08838834764831845f * 1.4426950408889634f;
constexpr unsigned SPIN_CAP = 1u << 22;

__device__ __forceinline__ unsigned rfl(unsigned x) { return __builtin_amdgcn_readfirstlane(x); }
__device__ __forceinline__ v4f mfma(v4u a, v4u b, v4f c) {
  return __builtin_amdgcn_mfma_f32_16x16x32_bf16(__builtin_bit_cast(v8bf, a), __builtin_bit_cast(v8bf, b), c, 0, 0, 0);
}
__device__ __forceinline__ unsigned pk2(float lo, float hi) {
  v2f x = {lo, hi};
  return __builtin_bit_cast(unsigned, __builtin_convertvector(x, v2bf));
}
__device__ __forceinline__ v2u trread(LAS char* p) {
  return __builtin_bit_cast(v2u, __builtin_amdgcn_ds_read_tr16_b64_v4i16((LAS v4s*)p));
}
__device__ __forceinline__ float xg_max(float x) {
  auto s = __builtin_amdgcn_permlane16_swap(__float_as_uint(x), __float_as_uint(x), false, false);
  x = fmaxf(__uint_as_float(s[0]), __uint_as_float(s[1]));
  auto t = __builtin_amdgcn_permlane32_swap(__float_as_uint(x), __float_as_uint(x), false, false);
  return fmaxf(__uint_as_float(t[0]), __uint_as_float(t[1]));
}
__device__ __forceinline__ float xg_sum(float x) {
  auto s = __builtin_amdgcn_permlane16_swap(__float_as_uint(x), __float_as_uint(x), false, false);
  x = __uint_as_float(s[0]) + __uint_as_float(s[1]);
  auto t = __builtin_amdgcn_permlane32_swap(__float_as_uint(x), __float_as_uint(x), false, false);
  return __uint_as_float(t[0]) + __uint_as_float(t[1]);
}
#define LDS_WAIT() asm volatile("s_waitcnt lgkmcnt(0)" ::: "memory")
#define CFENCE() asm volatile("" ::: "memory")
__device__ __forceinline__ unsigned ctl_rd(unsigned addr) {
  unsigned v;
  asm volatile("ds_read_b32 %0, %1\n\ts_waitcnt lgkmcnt(0)" : "=v"(v) : "v"(addr) : "memory");
  return rfl(v);
}
__device__ __forceinline__ void ctl_wr(unsigned addr, unsigned val) {
  asm volatile("s_mov_b64 exec, 1\n\tds_write_b32 %0, %1\n\ts_mov_b64 exec, -1" ::"v"(addr), "v"(val) : "memory");
}
__device__ __forceinline__ unsigned ctl_add(unsigned addr) {
  unsigned v;
  asm volatile("s_mov_b64 exec, 1\n\tds_add_rtn_u32 %0, %1, %2\n\ts_mov_b64 exec, -1\n\ts_waitcnt lgkmcnt(0)" : "=&v"(v) : "v"(addr), "v"(1u) : "memory");
  return rfl(v);
}
template <int SLP> __device__ __forceinline__ unsigned ctl_wait(unsigned addr, unsigned need) {
  unsigned v = ctl_rd(addr);
  for (unsigned sp = 0; v < need && sp < SPIN_CAP; ++sp) {
    __builtin_amdgcn_s_sleep(SLP);
    v = ctl_rd(addr);
  }
  return v;
}

#define DMA1(A, IMM, VO) \
  "s_waitcnt vmcnt(%c[w])\n\t" A "s_nop 0\n\tbuffer_load_dwordx4 " VO ", %[rs], %[so] offen offset:" IMM " nt lds\n\t"
#define PUBL "s_mov_b64 exec, 1\n\tds_write_b32 %[fa], %[fv]\n\ts_mov_b64 exec, -1\n\t"
template <bool PUB>
__device__ __forceinline__ void dma_unit(__amdgpu_buffer_rsrc_t rs, unsigned soff, unsigned m0base, unsigned vo0, unsigned vo1, unsigned faddr, unsigned fval) {
#define PB(k) (PUB && PUB_B == (k))
#define DMA_STMT(k, A, IMM, VO) \
  if (PB(k)) asm volatile(DMA1(PUBL A, IMM, "%[vo]")::[w] "n"(WM1), [rs] "s"(rs), [so] "s"(soff), [m] "s"(m0base + (k) * 32 + ((k) >> 2) * 4096), [vo] "v"(VO), [fa] "v"(faddr), [fv] "v"(fval) : "memory"); \
  else asm volatile(DMA1(A, IMM, "%[vo]")::[w] "n"(WM1), [rs] "s"(rs), [so] "s"(soff), [m] "s"(m0base + (k) * 32 + ((k) >> 2) * 4096), [vo] "v"(VO) : "memory");
  DMA_STMT(0, "s_mov_b32 m0, %[m]\n\t", "0", vo0)
  DMA_STMT(1, "s_mov_b32 m0, %[m]\n\t", "1024", vo0)
  DMA_STMT(2, "s_mov_b32 m0, %[m]\n\t", "2048", vo0)
  DMA_STMT(3, "s_mov_b32 m0, %[m]\n\t", "3072", vo0)
  DMA_STMT(4, "s_mov_b32 m0, %[m]\n\t", "0", vo1)
  DMA_STMT(5, "s_mov_b32 m0, %[m]\n\t", "1024", vo1)
  DMA_STMT(6, "s_mov_b32 m0, %[m]\n\t", "2048", vo1)
  DMA_STMT(7, "s_mov_b32 m0, %[m]\n\t", "3072", vo1)
#undef DMA_STMT
#undef PB
}

__global__ void __launch_bounds__(NTHR, 3)
mha_block_kernel(const unsigned short* __restrict__ x, const unsigned short* __restrict__ kc, const unsigned short* __restrict__ vc,
                 const unsigned short* __restrict__ wq, const unsigned short* __restrict__ wo, unsigned short* __restrict__ out,
                 unsigned char* __restrict__ ws) {
  extern __shared__ __attribute__((aligned(16))) char smem[];
  LAS char* L = (LAS char*)smem;
  const unsigned lbase = (unsigned)(size_t)L;
  const int lane = threadIdx.x & 63, wave = rfl(threadIdx.x >> 6);
  const int g = lane >> 4, c = lane & 15;
  const int j = blockIdx.x, h = j & 31, bp = j >> 5;
  const unsigned ctl = lbase + L_CTRL;
  const __amdgpu_buffer_rsrc_t rsO = __builtin_amdgcn_make_buffer_rsrc((void*)ws, 0, (int)OBUF_BYTES, 0x00020000);

  if (wave == 0) {
    __builtin_amdgcn_s_setprio(3);
    const __amdgpu_buffer_rsrc_t rsK = __builtin_amdgcn_make_buffer_rsrc((void*)kc, 0, NB * NH * SK * DH * 2, 0x00020000);
    const __amdgpu_buffer_rsrc_t rsV = __builtin_amdgcn_make_buffer_rsrc((void*)vc, 0, NB * NH * SK * DH * 2, 0x00020000);
    const unsigned vo0 = lane * 16, vo1 = lane * 16 + 4096;
    const unsigned faddr = ctl + 4 * C_FULL;
    const unsigned sbase = (unsigned)(bp * NH + h) * (SK * DH * 2);
    dma_unit<false>(rsK, sbase, lbase, vo0, vo1, faddr, 0u);
    dma_unit<false>(rsV, sbase, lbase + UNIT, vo0, vo1, faddr, 0u);
    __builtin_amdgcn_s_barrier();
    unsigned pos = 2, rel0 = 0, rel1 = 0, rel2 = 0;
#pragma unroll 1
    for (int n = 1; n < NUNIT / 2; ++n) {
      const unsigned soff = sbase + (unsigned)(n >> 6) * (8u * NH * SK * DH * 2) + (unsigned)(n & 63) * 8192u;
#pragma unroll
      for (int kv = 0; kv < 2; ++kv) {
        const int u = 2 * n + kv;
        if (u >= NR) {
          const unsigned up = u - NR, aw = (up >> 1) % 3u, need = up + 1;
          unsigned have = aw == 0 ? rel0 : (aw == 1 ? rel1 : rel2);
          for (unsigned sp = 0; have < need && sp < SPIN_CAP; ++sp) {
            unsigned r0, r1, r2;
            asm volatile("ds_read_b32 %0, %3 offset:16\n\tds_read_b32 %1, %3 offset:20\n\tds_read_b32 %2, %3 offset:24\n\ts_waitcnt lgkmcnt(0)"
                         : "=&v"(r0), "=&v"(r1), "=&v"(r2) : "v"(ctl) : "memory");
            rel0 = rfl(r0); rel1 = rfl(r1); rel2 = rfl(r2);
            have = aw == 0 ? rel0 : (aw == 1 ? rel1 : rel2);
            if (have < need) __builtin_amdgcn_s_sleep(1);
          }
        }
        dma_unit<true>(kv ? rsV : rsK, soff, lbase + pos * UNIT, vo0, vo1, faddr, (unsigned)(u - PUB_A));
        pos = pos + 1 == NR ? 0 : pos + 1;
      }
    }
    asm volatile("s_waitcnt vmcnt(16)" ::: "memory");
    ctl_wr(faddr, NUNIT - 2);
    asm volatile("s_waitcnt vmcnt(8)" ::: "memory");
    ctl_wr(faddr, NUNIT - 1);
    asm volatile("s_waitcnt vmcnt(0)" ::: "memory");
    ctl_wr(faddr, NUNIT);
  } else if (wave <= NATT) {
    const int aw = wave - 1;
    if (aw == 0) { *(LAS unsigned*)(L + L_CTRL + 4 * lane) = 0u; LDS_WAIT(); }
    __builtin_amdgcn_s_barrier();
    ctl_wait<2>(ctl + 4 * C_Q2, 4);
    v4u qf[4];
#pragma unroll
    for (int s = 0; s < 4; ++s) {
      v4f lo = {0.f, 0.f, 0.f, 0.f}, hi = {0.f, 0.f, 0.f, 0.f};
#pragma unroll
      for (int P = 0; P < 4; ++P) {
        lo += *(LAS v4f*)(L + L_SCR + P * 8192 + g * 2048 + (s * 16 + c) * 16);
        hi += *(LAS v4f*)(L + L_SCR + P * 8192 + g * 2048 + 1024 + (s * 16 + c) * 16);
      }
      qf[s] = v4u{pk2(lo[0], lo[1]), pk2(lo[2], lo[3]), pk2(hi[0], hi[1]), pk2(hi[2], hi[3])};
    }
    LDS_WAIT();
    ctl_add(ctl + 4 * C_QREAD);
    const unsigned koff = (c & 7) * PIECE + (c >> 3) * 256 + g * 16;
    const unsigned voff = (4 * (g & 1) + (c >> 2)) * PIECE + (g >> 1) * 256 + (c & 3) * 8;
    float m = -INFINITY, l = 0.f;
    v4f acc[8];
#pragma unroll
    for (int d = 0; d < 8; ++d) acc[d] = v4f{0.f, 0.f, 0.f, 0.f};
    unsigned pos = 2 * aw, full = 0;
#pragma unroll 1
    for (int n = aw; n < NUNIT / 2; n += NATT) {
      const unsigned posv = pos + 1 == NR ? 0 : pos + 1;
      if (full < (unsigned)(2 * n + 1)) full = ctl_wait<1>(ctl + 4 * C_FULL, 2 * n + 1);
      CFENCE();
      LAS char* kb = L + pos * UNIT + koff;
      v4u kf[2][4];
#pragma unroll
      for (int t = 0; t < 2; ++t)
#pragma unroll
        for (int s = 0; s < 4; ++s) kf[t][s] = *(LAS v4u*)(kb + t * 512 + s * 64);
      LDS_WAIT();
      ctl_wr(ctl + 4 * (C_REL + aw), 2 * n + 1);
      asm volatile("" : "+v"(kf[0][0]), "+v"(kf[0][1]), "+v"(kf[0][2]), "+v"(kf[0][3]), "+v"(kf[1][0]), "+v"(kf[1][1]), "+v"(kf[1][2]), "+v"(kf[1][3])::"memory");
      v4f s0 = {0.f, 0.f, 0.f, 0.f}, s1 = {0.f, 0.f, 0.f, 0.f};
#pragma unroll
      for (int s = 0; s < 4; ++s) { s0 = mfma(kf[0][s], qf[s], s0); s1 = mfma(kf[1][s], qf[s], s1); }
      float mx = fmaxf(fmaxf(fmaxf(s0[0], s0[1]), fmaxf(s0[2], s0[3])), fmaxf(fmaxf(s1[0], s1[1]), fmaxf(s1[2], s1[3])));
      mx = xg_max(mx);
      const float mn = fmaxf(m, mx);
      const float alpha = __builtin_amdgcn_exp2f((m - mn) * CSC);
      const float mc = mn * CSC;
      m = mn;
      float p[8];
#pragma unroll
      for (int i = 0; i < 4; ++i) { p[i] = __builtin_amdgcn_exp2f(fmaf(s0[i], CSC, -mc)); p[4 + i] = __builtin_amdgcn_exp2f(fmaf(s1[i], CSC, -mc)); }
      l = fmaf(l, alpha, ((p[0] + p[1]) + (p[2] + p[3])) + ((p[4] + p[5]) + (p[6] + p[7])));
      const v4u pf = {pk2(p[0], p[1]), pk2(p[2], p[3]), pk2(p[4], p[5]), pk2(p[6], p[7])};
#pragma unroll
      for (int d = 0; d < 8; ++d) acc[d] *= alpha;
      if (full < (unsigned)(2 * n + 2)) full = ctl_wait<1>(ctl + 4 * C_FULL, 2 * n + 2);
      CFENCE();
      LAS char* vb = L + posv * UNIT + voff;
      v4u vf[8];
#pragma unroll
      for (int d = 0; d < 8; ++d) {
        const v2u a = trread(vb + d * 32), b = trread(vb + 512 + d * 32);
        vf[d] = v4u{a[0], a[1], b[0], b[1]};
      }
      LDS_WAIT();
      ctl_wr(ctl + 4 * (C_REL + aw), 2 * n + 2);
      asm volatile("" : "+v"(vf[0]), "+v"(vf[1]), "+v"(vf[2]), "+v"(vf[3]), "+v"(vf[4]), "+v"(vf[5]), "+v"(vf[6]), "+v"(vf[7])::"memory");
#pragma unroll
      for (int d = 0; d < 8; ++d) acc[d] = mfma(vf[d], pf, acc[d]);
      pos += 2 * NATT;
      if (pos >= NR) pos -= NR;
      if ((n & 63) + NATT >= 64) {
        const int ph = n >> 6;
        const bool mine = (c >> 3) == ph;
        if ((n & 63) != 63) {
          LAS char* rec = L + L_MRG + (62 - (n & 63)) * MRG_REC;
          if (mine) {
#pragma unroll
            for (int d = 0; d < 8; ++d) *(LAS v4f*)(rec + d * 1024 + lane * 16) = acc[d];
            *(LAS v2f*)(rec + 8192 + lane * 8) = v2f{m, l};
          }
          LDS_WAIT();
          ctl_add(ctl + 4 * (C_MRG + ph));
        } else {
          ctl_wait<1>(ctl + 4 * (C_MRG + ph), 2);
          CFENCE();
          LAS char* r0 = L + L_MRG;
          LAS char* r1 = L + L_MRG + MRG_REC;
          const v2f a0 = *(LAS v2f*)(r0 + 8192 + lane * 8), a1 = *(LAS v2f*)(r1 + 8192 + lane * 8);
          const float M = fmaxf(m, fmaxf(a0[0], a1[0]));
          const float w = __builtin_amdgcn_exp2f((m - M) * CSC), w0 = __builtin_amdgcn_exp2f((a0[0] - M) * CSC), w1 = __builtin_amdgcn_exp2f((a1[0] - M) * CSC);
          const float Ls = xg_sum(fmaf(w, l, fmaf(w0, a0[1], w1 * a1[1])));
          const float inv = 1.0f / Ls;
          const float f = w * inv, f0 = w0 * inv, f1 = w1 * inv;
          const int obase = ((ph * 4 + (bp >> 1)) * NH + h) * 4096 + g * 256 + ((bp & 1) * 8 + (c & 7)) * 16;
#pragma unroll
          for (int s = 0; s < 4; ++s) {
            const v4f x0 = acc[2 * s] * f + *(LAS v4f*)(r0 + (2 * s) * 1024 + lane * 16) * f0 + *(LAS v4f*)(r1 + (2 * s) * 1024 + lane * 16) * f1;
            const v4f x1 = acc[2 * s + 1] * f + *(LAS v4f*)(r0 + (2 * s + 1) * 1024 + lane * 16) * f0 + *(LAS v4f*)(r1 + (2 * s + 1) * 1024 + lane * 16) * f1;
            v4u o = {pk2(x0[0], x0[1]), pk2(x0[2], x0[3]), pk2(x1[0], x1[1]), pk2(x1[2], x1[3])};
#pragma unroll
            for (int k = 0; k < 4; ++k) o[k] = o[k] == SENT ? 0x7FFF7FFFu : o[k];
            if (mine) __builtin_amdgcn_raw_buffer_store_b128(o, rsO, obase + s * 1024, 0, 16);
          }
        }
        m = -INFINITY; l = 0.f;
#pragma unroll
        for (int d = 0; d < 8; ++d) acc[d] = v4f{0.f, 0.f, 0.f, 0.f};
      }
    }
  } else {
    const int gw = wave - 1 - NATT;
    {
      const __amdgpu_buffer_rsrc_t rsQ = __builtin_amdgcn_make_buffer_rsrc((void*)wq, 0, DM * NH * DH * 2, 0x00020000);
      const int wvo = (8 * g * (NH * DH) + 8 * c) * 2, wso = (128 * gw * (NH * DH) + h * DH) * 2;
      const int xrow = ((c < 8 ? bp : bp + 8) * SQ + (c & 7));
      const GAS char* xb = (const GAS char*)x + ((size_t)xrow * DM + 128 * gw + 8 * g) * 2;
      v4u xf[4], w[4][8];
#pragma unroll
      for (int t = 0; t < 4; ++t) xf[t] = *(const GAS v4u*)(xb + t * 64);
#pragma unroll
      for (int t = 0; t < 3; ++t)
#pragma unroll
        for (int jj = 0; jj < 8; ++jj) w[t][jj] = __builtin_amdgcn_raw_buffer_load_b128(rsQ, wvo, wso + (32 * t + jj) * (NH * DH * 2), WQ_AUX);
      __builtin_amdgcn_sched_barrier(0);
      __builtin_amdgcn_s_barrier();
      __builtin_amdgcn_sched_barrier(0);
      v4f qa[8];
#pragma unroll
      for (int d = 0; d < 8; ++d) qa[d] = v4f{0.f, 0.f, 0.f, 0.f};
#pragma unroll
      for (int t = 0; t < 4; ++t) {
#pragma unroll
        for (int dd = 0; dd < 8; ++dd) {
          v4u f;
#pragma unroll
          for (int v = 0; v < 4; ++v) f[v] = __builtin_amdgcn_perm(w[t][2 * v + 1][dd >> 1], w[t][2 * v][dd >> 1], (dd & 1) ? 0x07060302u : 0x05040100u);
          qa[dd] = mfma(f, xf[t], qa[dd]);
          if (dd & 1) __builtin_amdgcn_sched_barrier(0);
        }
        if (t == 0) {
          __builtin_amdgcn_sched_barrier(0);
#pragma unroll
          for (int jj = 0; jj < 8; ++jj) w[3][jj] = __builtin_amdgcn_raw_buffer_load_b128(rsQ, wvo, wso + (96 + jj) * (NH * DH * 2), WQ_AUX);
          __builtin_amdgcn_sched_barrier(0);
        }
      }
      LAS char* part = L + L_SCR + (gw & 3) * 8192 + lane * 16;
      if (gw < 4) {
        ctl_wait<1>(ctl + 4 * C_Q1, 4);
        CFENCE();
      }
#pragma unroll
      for (int i = 0; i < 4; ++i) {
        v4f lo = {qa[0][i], qa[1][i], qa[2][i], qa[3][i]}, hi = {qa[4][i], qa[5][i], qa[6][i], qa[7][i]};
        if (gw < 4) { lo += *(LAS v4f*)(part + i * 2048); hi += *(LAS v4f*)(part + i * 2048 + 1024); }
        *(LAS v4f*)(part + i * 2048) = lo;
        *(LAS v4f*)(part + i * 2048 + 1024) = hi;
      }
      LDS_WAIT();
      ctl_add(ctl + 4 * (gw < 4 ? C_Q2 : C_Q1));
    }
    const int n0 = 128 * (j & 7) + 16 * ((j >> 3) & 7), rg = j >> 6;
    v4u wf[16];
    {
      const __amdgpu_buffer_rsrc_t rsW = __builtin_amdgcn_make_buffer_rsrc((void*)wo, 0, DM * NH * DH * 2, 0x00020000);
      const int ovo = ((lane >> 1) * DM + 8 * (lane & 1)) * 2, oso = (512 * gw * DM + n0) * 2;
      for (int z = 0; z < gw; ++z) __builtin_amdgcn_s_sleep(6);
#pragma unroll
      for (int ks = 0; ks < 16; ++ks) {
        wf[ks] = __builtin_amdgcn_raw_buffer_load_b128(rsW, ovo, oso + 32 * ks * (DM * 2), WO_AUX);
        __builtin_amdgcn_s_sleep(48);
        __builtin_amdgcn_sched_barrier(0);
      }
      ctl_wait<8>(ctl + 4 * C_QREAD, NATT);
      CFENCE();
      LAS char* st = L + L_STG + gw * 1024;
#pragma unroll
      for (int ks = 0; ks < 16; ++ks) {
        *(LAS v4u*)(st + lane * 16) = wf[ks];
        LDS_WAIT();
        const v2u a = trread(st + (4 * g + (c >> 2)) * 32 + (c & 3) * 8), b = trread(st + 512 + (4 * g + (c >> 2)) * 32 + (c & 3) * 8);
        wf[ks] = v4u{a[0], a[1], b[0], b[1]};
        LDS_WAIT();
      }
    }
#pragma unroll 1
    for (int ph = 0; ph < 2; ++ph) {
      ctl_wait<16>(ctl + 4 * C_FULL, ph == 0 ? 152 : NUNIT - 8);
      const int fb = ((ph * 4 + rg) * NH + 4 * gw) * 4096 + lane * 16;
      v4f oa[4];
#pragma unroll
      for (int hh = 0; hh < 4; ++hh) oa[hh] = v4f{0.f, 0.f, 0.f, 0.f};
      unsigned pend = 15u;
      for (unsigned sp = 0; pend != 0u && sp < (1u << 16); ++sp) {
        v4u f[4][4];
#pragma unroll
        for (int hh = 0; hh < 4; ++hh)
          if (pend & (1u << hh)) {
#pragma unroll
            for (int s = 0; s < 4; ++s) f[hh][s] = __builtin_amdgcn_raw_buffer_load_b128(rsO, fb + hh * 4096 + s * 1024, 0, 16);
          }
#pragma unroll
        for (int hh = 0; hh < 4; ++hh)
          if (pend & (1u << hh)) {
            bool ok = true;
#pragma unroll
            for (int s = 0; s < 4; ++s)
#pragma unroll
              for (int k = 0; k < 4; ++k) ok &= f[hh][s][k] != SENT;
            if (__all(ok)) {
#pragma unroll
              for (int s = 0; s < 4; ++s) oa[hh] = mfma(wf[4 * hh + s], f[hh][s], oa[hh]);
              pend &= ~(1u << hh);
            }
          }
        if (pend) __builtin_amdgcn_s_sleep(2);
      }
      LAS char* red = L + L_STG;
      *(LAS v4f*)(red + gw * 1024 + lane * 16) = (oa[0] + oa[1]) + (oa[2] + oa[3]);
      LDS_WAIT();
      if (ctl_add(ctl + 4 * (C_ORC + ph)) == NHELP - 1) {
        CFENCE();
        v4f s = *(LAS v4f*)(red + lane * 16);
#pragma unroll
        for (int k = 1; k < NHELP; ++k) s += *(LAS v4f*)(red + k * 1024 + lane * 16);
        *(GAS v2u*)((GAS char*)out + ((size_t)(64 * ph + 16 * rg + c) * DM + n0 + 4 * g) * 2) = v2u{pk2(s[0], s[1]), pk2(s[2], s[3])};
        LDS_WAIT();
        ctl_add(ctl + 4 * (C_ORC + ph));
      }
      if (ph == 0) ctl_wait<8>(ctl + 4 * C_ORC, NHELP + 1);
    }
  }
}
}

extern "C" void kernel_launch(void* const* d_in, const int* in_sizes, int n_in, void* d_out, int out_size, void* d_ws, size_t ws_size,
                              hipStream_t stream) {
  (void)in_sizes; (void)n_in; (void)out_size; (void)ws_size;
  (void)hipFuncSetAttribute((const void*)mha_block_kernel, hipFuncAttributeMaxDynamicSharedMemorySize, LDS_BYTES);
  (void)hipMemsetAsync(d_ws, 0xFF, OBUF_BYTES, stream);
  mha_block_kernel<<<NWG, NTHR, LDS_BYTES, stream>>>((const unsigned short*)d_in[0], (const unsigned short*)d_in[1], (const unsigned short*)d_in[2],
                                                     (const unsigned short*)d_in[3], (const unsigned short*)d_in[4], (unsigned short*)d_out,
                                                     (unsigned char*)d_ws);
}
```

```cpp
#include <hip/hip_runtime.h>
#include <hip/hip_bf16.h>
#include <stdint.h>

#define GAS __attribute__((address_space(1)))
#define LAS __attribute__((address_space(3)))
typedef unsigned v4u __attribute__((ext_vector_type(4)));
typedef unsigned v2u __attribute__((ext_vector_type(2)));
typedef float v4f __attribute__((ext_vector_type(4)));
typedef float v2f __attribute__((ext_vector_type(2)));
typedef short v4s __attribute__((ext_vector_type(4)));
typedef __bf16 v8bf __attribute__((ext_vector_type(8)));
typedef __bf16 v2bf __attribute__((ext_vector_type(2)));

#define WIN 20
#define WIN0 24
#define N0 12
#define WINE 40
#define NE 4
#define NI 2
#define QI_GO 5
#define HQ_WIN 40
#define WF_SLEEP 16
#define G1_LEAD 4
#define G1_SLEEP 4
#define ABL_NOST 0
#define ABL_NOG0 0
#define Q_GAP 4
#define ABL_NOQ 0
#define X_NT 0
#define P_ST_AUX 16
#define G0_AT 160
#define ABL_NOWO 0
#define ABL_NOCLR 0
#define OA_AT 126
#define O_ST_AUX 16

namespace {
constexpr int NB = 16, SQ = 8, DM = 1024, NH = 32, SK = 2048, DH = 128;
constexpr int NWG = 256, NTHR = 768, NATT = 3, NHELP = 8;
constexpr int PIECE = 1056;
constexpr int UNIT = 8 * PIECE;
constexpr int NR = 15;
constexpr int NUNIT = 256;
constexpr int AUX_NT = 2, AUX_SC1 = 16;
constexpr int L_CTRL = NR * UNIT;
constexpr int L_SCR = L_CTRL + 256;
constexpr int L_MRG = L_SCR;
constexpr int MRG_REC = 8192 + 512;
constexpr int L_OB = L_SCR + 2 * MRG_REC;
constexpr int LDS_BYTES = 160 * 1024;
static_assert(L_SCR + 32768 <= LDS_BYTES && L_OB + 16 * PIECE <= LDS_BYTES, "LDS map");
enum { C_FULL = 0, C_REL = 4, C_Q1 = 8, C_Q2 = 9, C_QI = 10, C_HB = 11, C_MRG = 12, C_EP = 14, C_CLR = 16 };
constexpr int W_EPOCH = 0, W_CF = 65536, FSTR = 16;
constexpr int W_O = 1 << 20;
constexpr int W_P = 2 << 20;
constexpr int WS_USED = 6 << 20;
constexpr unsigned SENT = 0xFFFFFFFFu;
constexpr float CSC = 0.08838834764831845f * 1.4426950408889634f;
constexpr unsigned SPIN_CAP = 1u << 22;

__device__ __forceinline__ unsigned rfl(unsigned x) { return __builtin_amdgcn_readfirstlane(x); }
__device__ __forceinline__ v4f mfma(v4u a, v4u b, v4f c) {
  return __builtin_amdgcn_mfma_f32_16x16x32_bf16(__builtin_bit_cast(v8bf, a), __builtin_bit_cast(v8bf, b), c, 0, 0, 0);
}
__device__ __forceinline__ unsigned pk2(float lo, float hi) {
  v2f x = {lo, hi};
  return __builtin_bit_cast(unsigned, __builtin_convertvector(x, v2bf));
}
__device__ __forceinline__ v2u trread(LAS char* p) {
  return __builtin_bit_cast(v2u, __builtin_amdgcn_ds_read_tr16_b64_v4i16((LAS v4s*)p));
}
__device__ __forceinline__ float xg_max(float x) {
  auto s = __builtin_amdgcn_permlane16_swap(__float_as_uint(x), __float_as_uint(x), false, false);
  x = fmaxf(__uint_as_float(s[0]), __uint_as_float(s[1]));
  auto t = __builtin_amdgcn_permlane32_swap(__float_as_uint(x), __float_as_uint(x), false, false);
  return fmaxf(__uint_as_float(t[0]), __uint_as_float(t[1]));
}
__device__ __forceinline__ float xg_sum(float x) {
  auto s = __builtin_amdgcn_permlane16_swap(__float_as_uint(x), __float_as_uint(x), false, false);
  x = __uint_as_float(s[0]) + __uint_as_float(s[1]);
  auto t = __builtin_amdgcn_permlane32_swap(__float_as_uint(x), __float_as_uint(x), false, false);
  return __uint_as_float(t[0]) + __uint_as_float(t[1]);
}
#define STAMP(k)
#define STAMPV(k, v)
#define LDS_WAIT() asm volatile("s_waitcnt lgkmcnt(0)" ::: "memory")
#define CFENCE() asm volatile("" ::: "memory")
#define PIN8(a) asm volatile("" : "+v"(a[0]), "+v"(a[1]), "+v"(a[2]), "+v"(a[3]), "+v"(a[4]), "+v"(a[5]), "+v"(a[6]), "+v"(a[7])::"memory")
template <int N> __device__ __forceinline__ void vm_wait() { asm volatile("s_waitcnt vmcnt(%c0)" ::"n"(N) : "memory"); }
__device__ __forceinline__ unsigned ctl_rd(unsigned addr) {
  unsigned v;
  asm volatile("ds_read_b32 %0, %1\n\ts_waitcnt lgkmcnt(0)" : "=v"(v) : "v"(addr) : "memory");
  return rfl(v);
}
__device__ __forceinline__ void ctl_wr(unsigned addr, unsigned val) {
  asm volatile("s_mov_b64 exec, 1\n\tds_write_b32 %0, %1\n\ts_mov_b64 exec, -1" ::"v"(addr), "v"(val) : "memory");
}
__device__ __forceinline__ unsigned ctl_add(unsigned addr) {
  unsigned v;
  asm volatile("s_mov_b64 exec, 1\n\tds_add_rtn_u32 %0, %1, %2\n\ts_mov_b64 exec, -1\n\ts_waitcnt lgkmcnt(0)" : "=&v"(v) : "v"(addr), "v"(1u) : "memory");
  return rfl(v);
}
template <int SLP> __device__ __forceinline__ unsigned ctl_wait(unsigned addr, unsigned need) {
  unsigned v = ctl_rd(addr);
  for (unsigned sp = 0; v < need && sp < SPIN_CAP; ++sp) {
    __builtin_amdgcn_s_sleep(SLP);
    v = ctl_rd(addr);
  }
  return v;
}

#define PUBL(k) ".if %c[b] == " #k "\n\ts_mov_b64 exec, 1\n\tds_write_b32 %[fa], v23\n\ts_mov_b64 exec, -1\n\t.endif\n\t"
#define PC(k, VO, IMM) "s_waitcnt vmcnt(%c[w])\n\t" PUBL(k) "buffer_load_dwordx4 " VO ", %[rs], %[so] offen offset:" IMM " nt lds\n\t"
template <int W>
__device__ __forceinline__ void dma_unit(__amdgpu_buffer_rsrc_t rs, unsigned soff, unsigned& m0b, unsigned& u, unsigned& lim, unsigned vo0, unsigned vo1,
                                         unsigned vctl, unsigned faddr) {
  unsigned sp;
  asm volatile(
      "s_cmp_lt_u32 %[u], %[lim]\n\t"
      "s_cbranch_scc1 2f\n\t"
      "s_mov_b32 %[sp], 0\n"
      "1:\n\t"
      "ds_read_b32 v20, %[vc] offset:16\n\tds_read_b32 v21, %[vc] offset:20\n\tds_read_b32 v22, %[vc] offset:24\n\t"
      "s_waitcnt lgkmcnt(0)\n\t"
      "v_min3_u32 v20, v20, v21, v22\n\t"
      "s_add_u32 %[sp], %[sp], 1\n\t"
      "v_readfirstlane_b32 %[lim], v20\n\t"
      "s_add_u32 %[lim], %[lim], %c[nr]\n\t"
      "s_cmp_lt_u32 %[u], %[lim]\n\t"
      "s_cbranch_scc1 2f\n\t"
      "s_sleep 1\n\t"
      "s_cmp_lt_u32 %[sp], %c[cap]\n\t"
      "s_cbranch_scc1 1b\n"
      "2:\n\t"
      "s_mov_b32 m0, %[m0b]\n\t"
      "v_mov_b32 v23, %[u]\n\t"
      "v_subrev_u32 v23, %c[a], v23\n\t"
      PC(0, "%[vo0]", "0")
      "ds_read_b32 v20, %[vc] offset:16\n\tds_read_b32 v21, %[vc] offset:20\n\tds_read_b32 v22, %[vc] offset:24\n\t"
      "s_add_u32 m0, m0, 32\n\t"
      PC(1, "%[vo0]", "1024")
      "s_add_u32 m0, m0, 32\n\t"
      PC(2, "%[vo0]", "2048")
      "s_add_u32 m0, m0, 32\n\t"
      PC(3, "%[vo0]", "3072")
      "s_add_u32 m0, m0, 0x1020\n\t"
      PC(4, "%[vo1]", "0")
      "s_waitcnt lgkmcnt(0)\n\t"
      "v_min3_u32 v20, v20, v21, v22\n\t"
      "s_add_u32 m0, m0, 32\n\t"
      PC(5, "%[vo1]", "1024")
      "v_readfirstlane_b32 %[lim], v20\n\t"
      "s_add_u32 %[m0b], %[m0b], %c[unit]\n\t"
      "s_add_u32 m0, m0, 32\n\t"
      PC(6, "%[vo1]", "2048")
      "s_add_u32 %[lim], %[lim], %c[nr]\n\t"
      "s_cmp_lt_u32 %[m0b], %c[ring]\n\t"
      "s_cselect_b32 %[m0b], %[m0b], 0\n\t"
      "s_add_u32 %[u], %[u], 1\n\t"
      "s_add_u32 m0, m0, 32\n\t"
      PC(7, "%[vo1]", "3072")
      : [m0b] "+s"(m0b), [u] "+s"(u), [lim] "+s"(lim), [sp] "=&s"(sp)
      : [w] "n"(W - 1), [a] "n"((W - 1) / 8), [b] "n"((W - 1) % 8), [nr] "n"(NR), [cap] "n"(SPIN_CAP), [unit] "n"(UNIT), [ring] "n"(NR * UNIT), [rs] "s"(rs),
        [so] "s"(soff), [vo0] "v"(vo0), [vo1] "v"(vo1), [vc] "v"(vctl), [fa] "v"(faddr)
      : "memory", "scc", "v20", "v21", "v22", "v23");
}

__global__ void __launch_bounds__(NTHR, 3)
mha_block_kernel(const unsigned short* __restrict__ x, const unsigned short* __restrict__ kc, const unsigned short* __restrict__ vc,
                 const unsigned short* __restrict__ wq, const unsigned short* __restrict__ wo, unsigned short* __restrict__ out,
                 unsigned char* __restrict__ ws) {
  extern __shared__ __attribute__((aligned(16))) char smem[];
  LAS char* L = (LAS char*)smem;
  const unsigned lbase = (unsigned)(size_t)L;
  const int lane = threadIdx.x & 63, wave = rfl(threadIdx.x >> 6);
  const int g = lane >> 4, c = lane & 15;
  const int j = blockIdx.x, h = j & 31, bp = j >> 5;
  const unsigned ctl = lbase + L_CTRL;
  const __amdgpu_buffer_rsrc_t rsO = __builtin_amdgcn_make_buffer_rsrc((void*)ws, 0, WS_USED, 0x00020000);

  if (wave == 0) {
    __builtin_amdgcn_s_setprio(3);
    __builtin_amdgcn_s_waitcnt(0x0F70);
    static_assert(WIN <= 24 && WIN > 16 && WIN0 >= WIN && WIN0 <= 40, "publish ladder below; published counts never decrease");
    const unsigned vo0 = lane * 16, vo1 = lane * 16 + 4096;
    const unsigned sbase = (unsigned)(bp * NH + h) * (SK * DH * 2);
    const __amdgpu_buffer_rsrc_t rsK = __builtin_amdgcn_make_buffer_rsrc((void*)kc, 0, NB * NH * SK * DH * 2, 0x00020000);
    const __amdgpu_buffer_rsrc_t rsV = __builtin_amdgcn_make_buffer_rsrc((void*)vc, 0, NB * NH * SK * DH * 2, 0x00020000);
    unsigned m0b = 0, u = 0, lim = NR;
#pragma unroll 1
    for (int n = 0; n < NI; ++n) {
      dma_unit<48>(rsK, sbase + (unsigned)n * 8192u, m0b, u, lim, vo0, vo1, ctl, ctl + 252);
      lim = NR;
      dma_unit<48>(rsV, sbase + (unsigned)n * 8192u, m0b, u, lim, vo0, vo1, ctl, ctl + 252);
      lim = NR;
      if (n == 0) __builtin_amdgcn_s_barrier();
    }
    vm_wait<0>(); ctl_wr(ctl + 4 * C_FULL, 2 * NI);
    ctl_wait<1>(ctl + 4 * C_QI, QI_GO);
#pragma unroll 1
    for (int n = NI; n < N0; ++n) {
      dma_unit<WIN0>(rsK, sbase + (unsigned)n * 8192u, m0b, u, lim, vo0, vo1, ctl, 2 * (n - NI) < (WIN0 - 1) / 8 ? ctl + 252 : ctl + 4 * C_FULL);
      dma_unit<WIN0>(rsV, sbase + (unsigned)n * 8192u, m0b, u, lim, vo0, vo1, ctl, 2 * (n - NI) + 1 < (WIN0 - 1) / 8 ? ctl + 252 : ctl + 4 * C_FULL);
    }
#pragma unroll 1
    for (int n = N0; n < 128 - NE; ++n) {
      const unsigned soff = sbase + (unsigned)(n >> 6) * (8u * NH * SK * DH * 2) + (unsigned)(n & 63) * 8192u;
      dma_unit<WIN>(rsK, soff, m0b, u, lim, vo0, vo1, ctl, ctl + 4 * C_FULL);
      dma_unit<WIN>(rsV, soff, m0b, u, lim, vo0, vo1, ctl, ctl + 4 * C_FULL);
    }
#pragma unroll 1
    for (int n = 128 - NE; n < 128; ++n) {
      const unsigned soff = sbase + (8u * NH * SK * DH * 2) + (unsigned)(n & 63) * 8192u;
      const int k = 2 * (n - (128 - NE));
      dma_unit<WINE>(rsK, soff, m0b, u, lim, vo0, vo1, ctl, k < (WINE - 1) / 8 - (WIN - 1) / 8 ? ctl + 252 : ctl + 4 * C_FULL);
      dma_unit<WINE>(rsV, soff, m0b, u, lim, vo0, vo1, ctl, k + 1 < (WINE - 1) / 8 - (WIN - 1) / 8 ? ctl + 252 : ctl + 4 * C_FULL);
    }
    if (WINE > 48) { vm_wait<48>(); ctl_wr(ctl + 4 * C_FULL, NUNIT - 6); }
    if (WINE > 40) { vm_wait<40>(); ctl_wr(ctl + 4 * C_FULL, NUNIT - 5); }
    if (WINE > 32) { vm_wait<32>(); ctl_wr(ctl + 4 * C_FULL, NUNIT - 4); }
    if (WINE > 24) { vm_wait<24>(); ctl_wr(ctl + 4 * C_FULL, NUNIT - 3); }
    vm_wait<16>(); ctl_wr(ctl + 4 * C_FULL, NUNIT - 2);
    vm_wait<8>(); ctl_wr(ctl + 4 * C_FULL, NUNIT - 1);
    vm_wait<0>(); ctl_wr(ctl + 4 * C_FULL, NUNIT);
  } else if ((wave & 3) == 0 || wave == 1) {
    const int aw = wave == 1 ? 2 : (wave >> 2) - 1;
    if (aw == 0) { *(LAS unsigned*)(L + L_CTRL + 4 * lane) = 0u; LDS_WAIT(); }
    __builtin_amdgcn_s_barrier();
    const unsigned koff = (c & 7) * PIECE + (c >> 3) * 256 + g * 16;
    const unsigned voff = (4 * (g & 1) + (c >> 2)) * PIECE + (g >> 1) * 256 + (c & 3) * 16;
    const int obase = W_O + ((bp >> 1) * 8 + (j & 7)) * 16384 + (h >> 3) * 4096 + g * 256 + ((bp & 1) * 8 + (c & 7)) * 16;
    {
      const unsigned long long tag = __hip_atomic_load((GAS unsigned long long*)(ws + W_EPOCH), __ATOMIC_RELAXED, __HIP_MEMORY_SCOPE_AGENT) + 1ull;
      const v4u sv = {SENT, SENT, SENT, SENT};
      if (aw == 0) {
#pragma unroll
        for (int s = 0; s < 4; ++s) __builtin_amdgcn_raw_buffer_store_b128(sv, rsO, obase + (c >> 3) * (4 * 8 * 16384), s * 1024, O_ST_AUX);
        if (lane == 0) *(LAS v2u*)(L + L_CTRL + 4 * C_EP) = v2u{(unsigned)tag, (unsigned)(tag >> 32)};
      } else {
#pragma unroll
        for (int k = 0; k < 8; ++k) __builtin_amdgcn_raw_buffer_store_b128(sv, rsO, lane * 16, W_P + (aw - 1) * (2 << 20) + ((j >> 3) * 64 + k * 8 + (j & 7)) * 1024, P_ST_AUX);
      }
      vm_wait<0>();
      if (ctl_add(ctl + 4 * C_CLR) == NATT - 1 && lane == 0) __hip_atomic_store((GAS unsigned long long*)(ws + W_CF) + FSTR * j, tag, __ATOMIC_RELAXED, __HIP_MEMORY_SCOPE_AGENT);
    }
    v4u oA[4];
#pragma unroll
    for (int s = 0; s < 4; ++s) oA[s] = v4u{0u, 0u, 0u, 0u};
    v4u kf[8], vf[8];
    unsigned full = ctl_wait<1>(ctl + 4 * C_FULL, 2 * aw + 2);
    {
      CFENCE();
      LAS char* kb = L + (2 * aw) * UNIT + koff;
      LAS char* vb = L + (2 * aw + 1) * UNIT + voff;
#pragma unroll
      for (int i = 0; i < 8; ++i) kf[i] = *(LAS v4u*)(kb + (i >> 2) * 512 + (i & 3) * 64);
#pragma unroll
      for (int d = 0; d < 8; ++d) {
        const v2u a = trread(vb + (d >> 1) * 64 + (d & 1) * 8), b = trread(vb + 512 + (d >> 1) * 64 + (d & 1) * 8);
        vf[d] = v4u{a[0], a[1], b[0], b[1]};
      }
      LDS_WAIT();
      ctl_wr(ctl + 4 * (C_REL + aw), 2 * aw + 2 * NATT);
      PIN8(kf); PIN8(vf);
      if (aw == 0) STAMP(14);
      ctl_wait<1>(ctl + 4 * C_Q2, 4);
      CFENCE();
    }
    v4u qf[4];
#pragma unroll
    for (int s = 0; s < 4; ++s) {
      LAS char* qp = L + L_SCR + g * 2048 + (s * 16 + c) * 16;
      const v4f lo = (*(LAS v4f*)(qp) + *(LAS v4f*)(qp + 8192)) + (*(LAS v4f*)(qp + 16384) + *(LAS v4f*)(qp + 24576));
      const v4f hi = (*(LAS v4f*)(qp + 1024) + *(LAS v4f*)(qp + 9216)) + (*(LAS v4f*)(qp + 17408) + *(LAS v4f*)(qp + 25600));
      qf[s] = v4u{pk2(lo[0], lo[1]), pk2(lo[2], lo[3]), pk2(hi[0], hi[1]), pk2(hi[2], hi[3])};
      if (s & 1) asm volatile("" : "+v"(qf[s - 1]), "+v"(qf[s])::"memory");
    }
    if (aw == 0) STAMP(4);
    float m = -INFINITY, l = 0.f;
    v4f acc[8];
#pragma unroll
    for (int d = 0; d < 8; ++d) acc[d] = v4f{0.f, 0.f, 0.f, 0.f};
    unsigned pos = 2 * aw;
#pragma unroll 1
    for (int n = aw; n < 128; n += NATT) {
      const unsigned uk = 2 * n;
      const unsigned posv = pos + 1 == NR ? 0 : pos + 1;
      if (n >= NATT) {
        if (full < uk + 1) full = ctl_wait<1>(ctl + 4 * C_FULL, uk + 1);
        CFENCE();
        LAS char* kb = L + pos * UNIT + koff;
#pragma unroll
        for (int i = 0; i < 8; ++i) kf[i] = *(LAS v4u*)(kb + (i >> 2) * 512 + (i & 3) * 64);
        LDS_WAIT();
        ctl_wr(ctl + 4 * (C_REL + aw), uk + 1);
        PIN8(kf);
      }
      v4f s0 = {0.f, 0.f, 0.f, 0.f}, s1 = {0.f, 0.f, 0.f, 0.f};
#pragma unroll
      for (int s = 0; s < 4; ++s) { s0 = mfma(kf[s], qf[s], s0); s1 = mfma(kf[4 + s], qf[s], s1); }
      float mx = fmaxf(fmaxf(fmaxf(s0[0], s0[1]), fmaxf(s0[2], s0[3])), fmaxf(fmaxf(s1[0], s1[1]), fmaxf(s1[2], s1[3])));
      mx = xg_max(mx);
      const bool lastt = (n & 63) == 63;
      const int ph = n >> 6;
      LAS char* r0 = L + L_MRG;
      LAS char* r1 = L + L_MRG + MRG_REC;
      v2f a0 = {-INFINITY, 0.f}, a1 = {-INFINITY, 0.f};
      if (lastt) {
        ctl_wait<0>(ctl + 4 * (C_MRG + ph), 2);
        CFENCE();
        a0 = *(LAS v2f*)(r0 + 8192 + lane * 8);
        a1 = *(LAS v2f*)(r1 + 8192 + lane * 8);
        mx = fmaxf(mx, fmaxf(a0[0], a1[0]));
      }
      const float mn = fmaxf(m, mx);
      const float alpha = __builtin_amdgcn_exp2f((m - mn) * CSC);
      const float mc = mn * CSC;
      m = mn;
      float p[8];
#pragma unroll
      for (int i = 0; i < 4; ++i) { p[i] = __builtin_amdgcn_exp2f(fmaf(s0[i], CSC, -mc)); p[4 + i] = __builtin_amdgcn_exp2f(fmaf(s1[i], CSC, -mc)); }
      l = fmaf(l, alpha, ((p[0] + p[1]) + (p[2] + p[3])) + ((p[4] + p[5]) + (p[6] + p[7])));
      v4u pf = {pk2(p[0], p[1]), pk2(p[2], p[3]), pk2(p[4], p[5]), pk2(p[6], p[7])};
#pragma unroll
      for (int d = 0; d < 8; ++d) acc[d] *= alpha;
      if (lastt) {
        const float w0 = __builtin_amdgcn_exp2f((a0[0] - mn) * CSC), w1 = __builtin_amdgcn_exp2f((a1[0] - mn) * CSC);
        l = 1.0f / xg_sum(fmaf(w0, a0[1], fmaf(w1, a1[1], l)));
#pragma unroll
        for (int d = 0; d < 8; ++d) acc[d] += *(LAS v4f*)(r0 + d * 1024 + lane * 16) * w0 + *(LAS v4f*)(r1 + d * 1024 + lane * 16) * w1;
      }
      asm volatile("" : "+v"(pf), "+v"(l));
      PIN8(acc);
      if (n >= NATT) {
        if (full < uk + 2) full = ctl_wait<0>(ctl + 4 * C_FULL, uk + 2);
        CFENCE();
        LAS char* vb = L + posv * UNIT + voff;
#pragma unroll
        for (int d = 0; d < 8; ++d) {
          const v2u a = trread(vb + (d >> 1) * 64 + (d & 1) * 8), b = trread(vb + 512 + (d >> 1) * 64 + (d & 1) * 8);
          vf[d] = v4u{a[0], a[1], b[0], b[1]};
        }
        LDS_WAIT();
        ctl_wr(ctl + 4 * (C_REL + aw), uk + 2 * NATT);
        PIN8(vf);
      }
#pragma unroll
      for (int d = 0; d < 8; ++d) acc[d] = mfma(vf[d], pf, acc[d]);
      pos += 2 * NATT;
      if (pos >= NR) pos -= NR;
      if (n == OA_AT && c < 8) {
#pragma unroll
        for (int s = 0; s < 4; ++s) __builtin_amdgcn_raw_buffer_store_b128(oA[s], rsO, obase + s * 1024, 0, O_ST_AUX);
      }
      if ((n & 63) + NATT >= 64) {
        const bool mine = (c >> 3) == ph;
        if (!lastt) {
          LAS char* rec = L + L_MRG + (62 - (n & 63)) * MRG_REC;
          if (mine) {
#pragma unroll
            for (int d = 0; d < 8; ++d) *(LAS v4f*)(rec + d * 1024 + lane * 16) = acc[d];
            *(LAS v2f*)(rec + 8192 + lane * 8) = v2f{m, l};
          }
          LDS_WAIT();
          ctl_add(ctl + 4 * (C_MRG + ph));
        } else {
#pragma unroll
          for (int s = 0; s < 4; ++s) {
            const v4f x0 = acc[2 * s] * l, x1 = acc[2 * s + 1] * l;
            v4u o = {pk2(x0[0], x0[1]), pk2(x0[2], x0[3]), pk2(x1[0], x1[1]), pk2(x1[2], x1[3])};
#pragma unroll
            for (int k = 0; k < 4; ++k) o[k] = o[k] == SENT ? 0x7FFF7FFFu : o[k];
            if (ph == 0) oA[s] = o;
            else if (mine) __builtin_amdgcn_raw_buffer_store_b128(o, rsO, obase + s * 1024, 4 * 8 * 16384, O_ST_AUX);
          }
          STAMP(5 + ph);
        }
        m = -INFINITY; l = 0.f;
#pragma unroll
        for (int d = 0; d < 8; ++d) acc[d] = v4f{0.f, 0.f, 0.f, 0.f};
      }
    }
  } else {
    const int gw = wave - 2 - (wave >> 2);
    {
      const __amdgpu_buffer_rsrc_t rsQ = __builtin_amdgcn_make_buffer_rsrc((void*)wq, 0, ABL_NOQ ? 0 : DM * NH * DH * 2, 0x00020000);
      const int wvo = (8 * g * (NH * DH) + 8 * c) * 2, wso = (128 * gw * (NH * DH) + h * DH) * 2;
      const int xrow = ((c < 8 ? bp : bp + 8) * SQ + (c & 7));
      const GAS char* xb = (const GAS char*)x + ((size_t)xrow * DM + 128 * gw + 8 * g) * 2;
#define WQ_ROW(t, jj) ((32 * (((t) + (bp >> 1)) & 3) + ((jj) ^ (4 * (bp & 1)))) * (NH * DH * 2))
      v4u xf[4], w[4][8];
      __builtin_amdgcn_s_barrier();
      __builtin_amdgcn_sched_barrier(0);
#pragma unroll
      for (int t = 0; t < 3; ++t)
#pragma unroll
        for (int jj = 0; jj < 8; ++jj) {
          if (t == 0 && jj == 4) { vm_wait<0>(); __builtin_amdgcn_s_sleep(Q_GAP); }
          w[t][jj] = __builtin_amdgcn_raw_buffer_load_b128(rsQ, wvo, wso + WQ_ROW(t, jj), AUX_NT);
          if (t == 0 && jj == 3) {
#pragma unroll
            for (int tt = 0; tt < 4; ++tt) xf[tt] = X_NT ? __builtin_nontemporal_load((const GAS v4u*)(xb + ((tt + (bp >> 1)) & 3) * 64)) : *(const GAS v4u*)(xb + ((tt + (bp >> 1)) & 3) * 64);
          }
          __builtin_amdgcn_sched_barrier(0);
        }
      ctl_add(ctl + 4 * C_QI);
      if (bp & 1) {
#pragma unroll
        for (int t = 0; t < 4; ++t) xf[t] = v4u{xf[t][2], xf[t][3], xf[t][0], xf[t][1]};
      }
      v4f qa[8];
#pragma unroll
      for (int d = 0; d < 8; ++d) qa[d] = v4f{0.f, 0.f, 0.f, 0.f};
#pragma unroll
      for (int t = 0; t < 4; ++t) {
#pragma unroll
        for (int dd = 0; dd < 8; ++dd) {
          v4u f;
#pragma unroll
          for (int v = 0; v < 4; ++v) f[v] = __builtin_amdgcn_perm(w[t][2 * v + 1][dd >> 1], w[t][2 * v][dd >> 1], (dd & 1) ? 0x07060302u : 0x05040100u);
          qa[dd] = mfma(f, xf[t], qa[dd]);
          if (dd & 1) __builtin_amdgcn_sched_barrier(0);
        }
        if (t == 0) {
#pragma unroll
          for (int jj = 0; jj < 8; ++jj) {
            w[3][jj] = __builtin_amdgcn_raw_buffer_load_b128(rsQ, wvo, wso + WQ_ROW(3, jj), AUX_NT);
            __builtin_amdgcn_sched_barrier(0);
          }
        }
      }
      STAMP(16 + gw);
      LAS char* part = L + L_SCR + (gw & 3) * 8192 + lane * 16;
      if (gw >= 4) {
        ctl_wait<0>(ctl + 4 * C_Q1, 4);
        CFENCE();
      }
#pragma unroll
      for (int i = 0; i < 4; ++i) {
        v4f lo = {qa[0][i], qa[1][i], qa[2][i], qa[3][i]}, hi = {qa[4][i], qa[5][i], qa[6][i], qa[7][i]};
        if (gw >= 4) { lo += *(LAS v4f*)(part + i * 2048); hi += *(LAS v4f*)(part + i * 2048 + 1024); }
        *(LAS v4f*)(part + i * 2048) = lo;
        *(LAS v4f*)(part + i * 2048 + 1024) = hi;
      }
      LDS_WAIT();
      ctl_add(ctl + 4 * (gw >= 4 ? C_Q2 : C_Q1));
      if (gw == 0) STAMP(7);
      if (gw == 7) STAMP(8);
    }
    const int xc = j & 7, idx = j >> 3, ct = idx & 7, rg = idx >> 3;
    unsigned hb = 0;
#define HBAR(SLP) do { hb += NHELP; ctl_add(ctl + 4 * C_HB); ctl_wait<SLP>(ctl + 4 * C_HB, hb); CFENCE(); } while (0)
    const int pdst = W_P + (idx * 64 + gw * 8 + xc) * 1024;
    v4u wf[16];
    {
      const __amdgpu_buffer_rsrc_t rsW = __builtin_amdgcn_make_buffer_rsrc((void*)wo, 0, ABL_NOWO ? 0 : DM * NH * DH * 2, 0x00020000);
      ctl_wait<32>(ctl + 4 * C_FULL, 40 + 2 * gw);
#pragma unroll
      for (int ks = 0; ks < 16; ++ks) {
        wf[ks] = __builtin_amdgcn_raw_buffer_load_b128(rsW, g * 2048 + c * 16, (((ks >> 2) * 8 + xc) * 128 + (ks & 3) * 32 + 4 * gw) * 2048 + ct * 256, AUX_NT);
        __builtin_amdgcn_s_sleep(WF_SLEEP);
        __builtin_amdgcn_sched_barrier(0);
      }
      LAS char* sw = L + L_OB + gw * PIECE + lane * 16;
      LAS char* sr = L + L_OB + 2 * g * PIECE + (c >> 2) * 256 + gw * 32 + (c & 3) * 8;
#pragma unroll
      for (int r = 0; r < 8; ++r) {
        *(LAS v4u*)(sw) = wf[2 * r];
        *(LAS v4u*)(sw + 8 * PIECE) = wf[2 * r + 1];
        LDS_WAIT();
        HBAR(2);
        const v2u a0 = trread(sr), b0 = trread(sr + PIECE), a1 = trread(sr + 8 * PIECE), b1 = trread(sr + 9 * PIECE);
        wf[2 * r] = v4u{a0[0], a0[1], b0[0], b0[1]};
        wf[2 * r + 1] = v4u{a1[0], a1[1], b1[0], b1[1]};
        LDS_WAIT();
        asm volatile("" : "+v"(wf[2 * r]), "+v"(wf[2 * r + 1])::"memory");
        HBAR(2);
      }
    }
    const v2u tagv = *(LAS v2u*)(L + L_CTRL + 4 * C_EP);
    const unsigned long long tag = ((unsigned long long)tagv[1] << 32) | tagv[0];
    if (gw == 7) STAMP(9);
    ctl_wait<16>(ctl + 4 * C_FULL, NUNIT - G1_LEAD);
    if (gw == 7) STAMP(10);
    {
      const int src = lane < 8 ? idx * 8 + lane : (2 * rg + (lane & 1)) * 32 + (gw >> 1) * 8 + xc;
      GAS unsigned long long* fl = (GAS unsigned long long*)(ws + W_CF) + FSTR * src;
      for (unsigned sp = 0; sp < (1u << 16); ++sp) {
        if (__all(__hip_atomic_load(fl, __ATOMIC_RELAXED, __HIP_MEMORY_SCOPE_AGENT) == tag)) break;
        __builtin_amdgcn_s_sleep(32);
      }
      __builtin_amdgcn_fence(__ATOMIC_ACQUIRE, "wavefront");
    }
    if (gw == 7) STAMP(11);
#pragma unroll 1
    for (int ph = 0; ph < 2; ++ph) {
      v4u f0, f1;
      for (unsigned sp = 0; sp < (1u << 16); ++sp) {
        CFENCE();
        f0 = __builtin_amdgcn_raw_buffer_load_b128(rsO, lane * 16, W_O + ((ph * 4 + rg) * 8 + xc) * 16384 + gw * 2048, AUX_SC1);
        f1 = __builtin_amdgcn_raw_buffer_load_b128(rsO, lane * 16, W_O + ((ph * 4 + rg) * 8 + xc) * 16384 + gw * 2048 + 1024, AUX_SC1);
        bool ok = true;
#pragma unroll
        for (int k = 0; k < 4; ++k) ok &= (f0[k] != SENT) & (f1[k] != SENT);
        if (__all(ok)) break;
        __builtin_amdgcn_s_sleep(G1_SLEEP);
      }
      *(LAS v4u*)(L + L_OB + gw * 2048 + lane * 16) = f0;
      *(LAS v4u*)(L + L_OB + gw * 2048 + 1024 + lane * 16) = f1;
      LDS_WAIT();
      HBAR(0);
      v4f oa[4];
#pragma unroll
      for (int k = 0; k < 4; ++k) oa[k] = v4f{0.f, 0.f, 0.f, 0.f};
#pragma unroll
      for (int f = 0; f < 16; ++f) oa[f & 3] = mfma(wf[f], *(LAS v4u*)(L + L_OB + f * 1024 + lane * 16), oa[f & 3]);
      v4u pt = __builtin_bit_cast(v4u, (oa[0] + oa[1]) + (oa[2] + oa[3]));
#pragma unroll
      for (int k = 0; k < 4; ++k) pt[k] = pt[k] == SENT ? 0x7FFFFFFFu : pt[k];
      __builtin_amdgcn_raw_buffer_store_b128(pt, rsO, lane * 16, pdst + ph * (2 << 20), P_ST_AUX);
      if (gw == 7) STAMP(24 + ph);
      if (ph == 0) HBAR(0);
    }
    if (gw < 2) {
      v4u q[8];
      for (unsigned sp = 0; sp < (1u << 16); ++sp) {
        CFENCE();
        bool ok = true;
#pragma unroll
        for (int k = 0; k < 8; ++k) q[k] = __builtin_amdgcn_raw_buffer_load_b128(rsO, lane * 16, W_P + gw * (2 << 20) + (idx * 64 + xc * 8 + k) * 1024, AUX_SC1);
#pragma unroll
        for (int k = 0; k < 8; ++k) ok &= (q[k][0] != SENT) & (q[k][1] != SENT) & (q[k][2] != SENT) & (q[k][3] != SENT);
        if (__all(ok)) break;
        __builtin_amdgcn_s_sleep(1);
      }
      v4f s = __builtin_bit_cast(v4f, q[0]);
#pragma unroll
      for (int k = 1; k < 8; ++k) s += __builtin_bit_cast(v4f, q[k]);
      *(GAS v2u*)((GAS char*)out + ((size_t)(64 * gw + 16 * rg + c) * DM + 128 * ct + 16 * xc + 4 * g) * 2) = v2u{pk2(s[0], s[1]), pk2(s[2], s[3])};
      STAMP(12 + gw);
    } else if (gw == 2 && j == 0) {
      GAS unsigned long long* fl = (GAS unsigned long long*)(ws + W_CF) + FSTR * lane;
      for (unsigned sp = 0; sp < (1u << 16); ++sp) {
        bool ok = true;
#pragma unroll
        for (int k = 0; k < 4; ++k) ok &= __hip_atomic_load(fl + FSTR * 64 * k, __ATOMIC_RELAXED, __HIP_MEMORY_SCOPE_AGENT) == tag;
        if (__all(ok)) break;
        __builtin_amdgcn_s_sleep(32);
      }
      if (lane == 0) __hip_atomic_store((GAS unsigned long long*)(ws + W_EPOCH), tag, __ATOMIC_RELAXED, __HIP_MEMORY_SCOPE_AGENT);
    }
  }
}
}

extern "C" void kernel_launch(void* const* d_in, const int* in_sizes, int n_in, void* d_out, int out_size, void* d_ws, size_t ws_size,
                              hipStream_t stream) {
  (void)in_sizes; (void)n_in; (void)out_size; (void)ws_size;
  (void)hipFuncSetAttribute((const void*)mha_block_kernel, hipFuncAttributeMaxDynamicSharedMemorySize, LDS_BYTES);
  mha_block_kernel<<<NWG, NTHR, LDS_BYTES, stream>>>((const unsigned short*)d_in[0], (const unsigned short*)d_in[1], (const unsigned short*)d_in[2],
                                                     (const unsigned short*)d_in[3], (const unsigned short*)d_in[4], (unsigned short*)d_out,
                                                     (unsigned char*)d_ws);
}
```

```cpp
#define DIAG_POL 1
#define DIAG_LO 0
#define DIAG_HI 4
#include <hip/hip_runtime.h>
#include <hip/hip_bf16.h>
#include <stdint.h>

#define GAS __attribute__((address_space(1)))
#define LAS __attribute__((address_space(3)))
typedef unsigned v4u __attribute__((ext_vector_type(4)));
typedef unsigned v2u __attribute__((ext_vector_type(2)));
typedef float v4f __attribute__((ext_vector_type(4)));
typedef float v2f __attribute__((ext_vector_type(2)));
typedef short v4s __attribute__((ext_vector_type(4)));
typedef __bf16 v8bf __attribute__((ext_vector_type(8)));
typedef __bf16 v2bf __attribute__((ext_vector_type(2)));

#define WIN 20
#define WIN0 24
#define N0 12
#define WINE 40
#define NE 4
#define NI 2
#define QI_GO 5
#define HQ_WIN 40
#define WF_SLEEP 16
#define G1_LEAD 4
#define G1_SLEEP 4
#define ABL_NOST 0
#define ABL_NOG0 0
#define Q_GAP 4
#define ABL_NOQ 0
#define X_NT 0
#define P_ST_AUX 16
#define G0_AT 160
#define ABL_NOWO 0
#define ABL_NOCLR 0
#define OA_AT 126
#define O_ST_AUX 16

namespace {
constexpr int NB = 16, SQ = 8, DM = 1024, NH = 32, SK = 2048, DH = 128;
constexpr int NWG = 256, NTHR = 768, NATT = 3, NHELP = 8;
constexpr int PIECE = 1056;
constexpr int UNIT = 8 * PIECE;
constexpr int NR = 15;
constexpr int NUNIT = 256;
constexpr int AUX_NT = 2, AUX_SC1 = 16;
constexpr int L_CTRL = NR * UNIT;
constexpr int L_SCR = L_CTRL + 256;
constexpr int L_MRG = L_SCR;
constexpr int MRG_REC = 8192 + 512;
constexpr int L_OB = L_SCR + 2 * MRG_REC;
constexpr int LDS_BYTES = 160 * 1024;
static_assert(L_SCR + 32768 <= LDS_BYTES && L_OB + 16 * PIECE <= LDS_BYTES, "LDS map");
enum { C_FULL = 0, C_REL = 4, C_Q1 = 8, C_Q2 = 9, C_QI = 10, C_HB = 11, C_MRG = 12, C_EP = 14, C_CLR = 16 };
constexpr int W_EPOCH = 0, W_CF = 65536, FSTR = 16;
constexpr int W_O = 1 << 20;
constexpr int W_P = 2 << 20;
constexpr int WS_USED = 6 << 20;
constexpr unsigned SENT = 0xFFFFFFFFu;
constexpr float CSC = 0.08838834764831845f * 1.4426950408889634f;
constexpr unsigned SPIN_CAP = 1u << 22;

__device__ __forceinline__ unsigned rfl(unsigned x) { return __builtin_amdgcn_readfirstlane(x); }
__device__ __forceinline__ v4f mfma(v4u a, v4u b, v4f c) {
  return __builtin_amdgcn_mfma_f32_16x16x32_bf16(__builtin_bit_cast(v8bf, a), __builtin_bit_cast(v8bf, b), c, 0, 0, 0);
}
__device__ __forceinline__ unsigned pk2(float lo, float hi) {
  v2f x = {lo, hi};
  return __builtin_bit_cast(unsigned, __builtin_convertvector(x, v2bf));
}
__device__ __forceinline__ v2u trread(LAS char* p) {
  return __builtin_bit_cast(v2u, __builtin_amdgcn_ds_read_tr16_b64_v4i16((LAS v4s*)p));
}
__device__ __forceinline__ float xg_max(float x) {
  auto s = __builtin_amdgcn_permlane16_swap(__float_as_uint(x), __float_as_uint(x), false, false);
  x = fmaxf(__uint_as_float(s[0]), __uint_as_float(s[1]));
  auto t = __builtin_amdgcn_permlane32_swap(__float_as_uint(x), __float_as_uint(x), false, false);
  return fmaxf(__uint_as_float(t[0]), __uint_as_float(t[1]));
}
__device__ __forceinline__ float xg_sum(float x) {
  auto s = __builtin_amdgcn_permlane16_swap(__float_as_uint(x), __float_as_uint(x), false, false);
  x = __uint_as_float(s[0]) + __uint_as_float(s[1]);
  auto t = __builtin_amdgcn_permlane32_swap(__float_as_uint(x), __float_as_uint(x), false, false);
  return __uint_as_float(t[0]) + __uint_as_float(t[1]);
}
#define STAMP(k)
#define STAMPV(k, v)
#define LDS_WAIT() asm volatile("s_waitcnt lgkmcnt(0)" ::: "memory")
#define CFENCE() asm volatile("" ::: "memory")
#define PIN8(a) asm volatile("" : "+v"(a[0]), "+v"(a[1]), "+v"(a[2]), "+v"(a[3]), "+v"(a[4]), "+v"(a[5]), "+v"(a[6]), "+v"(a[7])::"memory")
template <int N> __device__ __forceinline__ void vm_wait() { asm volatile("s_waitcnt vmcnt(%c0)" ::"n"(N) : "memory"); }
__device__ __forceinline__ unsigned ctl_rd(unsigned addr) {
  unsigned v;
  asm volatile("ds_read_b32 %0, %1\n\ts_waitcnt lgkmcnt(0)" : "=v"(v) : "v"(addr) : "memory");
  return rfl(v);
}
__device__ __forceinline__ void ctl_wr(unsigned addr, unsigned val) {
  asm volatile("s_mov_b64 exec, 1\n\tds_write_b32 %0, %1\n\ts_mov_b64 exec, -1" ::"v"(addr), "v"(val) : "memory");
}
__device__ __forceinline__ unsigned ctl_add(unsigned addr) {
  unsigned v;
  asm volatile("s_mov_b64 exec, 1\n\tds_add_rtn_u32 %0, %1, %2\n\ts_mov_b64 exec, -1\n\ts_waitcnt lgkmcnt(0)" : "=&v"(v) : "v"(addr), "v"(1u) : "memory");
  return rfl(v);
}
template <int SLP> __device__ __forceinline__ unsigned ctl_wait(unsigned addr, unsigned need) {
  unsigned v = ctl_rd(addr);
  for (unsigned sp = 0; v < need && sp < SPIN_CAP; ++sp) {
    __builtin_amdgcn_s_sleep(SLP);
    v = ctl_rd(addr);
  }
  return v;
}

#define PUBL(k) ".if %c[b] == " #k "\n\ts_mov_b64 exec, 1\n\tds_write_b32 %[fa], v23\n\ts_mov_b64 exec, -1\n\t.endif\n\t"
#define PC(k, VO, IMM) "s_waitcnt vmcnt(%c[w])\n\t" PUBL(k) ".if %c[nt]\n\tbuffer_load_dwordx4 " VO ", %[rs], %[so] offen offset:" IMM " nt lds\n\t.else\n\tbuffer_load_dwordx4 " VO ", %[rs], %[so] offen offset:" IMM " lds\n\t.endif\n\t"
template <int W, int NTF = 1>
__device__ __forceinline__ void dma_unit(__amdgpu_buffer_rsrc_t rs, unsigned soff, unsigned& m0b, unsigned& u, unsigned& lim, unsigned vo0, unsigned vo1,
                                         unsigned vctl, unsigned faddr) {
  unsigned sp;
  asm volatile(
      "s_cmp_lt_u32 %[u], %[lim]\n\t"
      "s_cbranch_scc1 2f\n\t"
      "s_mov_b32 %[sp], 0\n"
      "1:\n\t"
      "ds_read_b32 v20, %[vc] offset:16\n\tds_read_b32 v21, %[vc] offset:20\n\tds_read_b32 v22, %[vc] offset:24\n\t"
      "s_waitcnt lgkmcnt(0)\n\t"
      "v_min3_u32 v20, v20, v21, v22\n\t"
      "s_add_u32 %[sp], %[sp], 1\n\t"
      "v_readfirstlane_b32 %[lim], v20\n\t"
      "s_add_u32 %[lim], %[lim], %c[nr]\n\t"
      "s_cmp_lt_u32 %[u], %[lim]\n\t"
      "s_cbranch_scc1 2f\n\t"
      "s_sleep 1\n\t"
      "s_cmp_lt_u32 %[sp], %c[cap]\n\t"
      "s_cbranch_scc1 1b\n"
      "2:\n\t"
      "s_mov_b32 m0, %[m0b]\n\t"
      "v_mov_b32 v23, %[u]\n\t"
      "v_subrev_u32 v23, %c[a], v23\n\t"
      PC(0, "%[vo0]", "0")
      "ds_read_b32 v20, %[vc] offset:16\n\tds_read_b32 v21, %[vc] offset:20\n\tds_read_b32 v22, %[vc] offset:24\n\t"
      "s_add_u32 m0, m0, 32\n\t"
      PC(1, "%[vo0]", "1024")
      "s_add_u32 m0, m0, 32\n\t"
      PC(2, "%[vo0]", "2048")
      "s_add_u32 m0, m0, 32\n\t"
      PC(3, "%[vo0]", "3072")
      "s_add_u32 m0, m0, 0x1020\n\t"
      PC(4, "%[vo1]", "0")
      "s_waitcnt lgkmcnt(0)\n\t"
      "v_min3_u32 v20, v20, v21, v22\n\t"
      "s_add_u32 m0, m0, 32\n\t"
      PC(5, "%[vo1]", "1024")
      "v_readfirstlane_b32 %[lim], v20\n\t"
      "s_add_u32 %[m0b], %[m0b], %c[unit]\n\t"
      "s_add_u32 m0, m0, 32\n\t"
      PC(6, "%[vo1]", "2048")
      "s_add_u32 %[lim], %[lim], %c[nr]\n\t"
      "s_cmp_lt_u32 %[m0b], %c[ring]\n\t"
      "s_cselect_b32 %[m0b], %[m0b], 0\n\t"
      "s_add_u32 %[u], %[u], 1\n\t"
      "s_add_u32 m0, m0, 32\n\t"
      PC(7, "%[vo1]", "3072")
      : [m0b] "+s"(m0b), [u] "+s"(u), [lim] "+s"(lim), [sp] "=&s"(sp)
      : [nt] "n"(NTF), [w] "n"(W - 1), [a] "n"((W - 1) / 8), [b] "n"((W - 1) % 8), [nr] "n"(NR), [cap] "n"(SPIN_CAP), [unit] "n"(UNIT), [ring] "n"(NR * UNIT), [rs] "s"(rs),
        [so] "s"(soff), [vo0] "v"(vo0), [vo1] "v"(vo1), [vc] "v"(vctl), [fa] "v"(faddr)
      : "memory", "scc", "v20", "v21", "v22", "v23");
}

__global__ void __launch_bounds__(NTHR, 3)
mha_block_kernel(const unsigned short* __restrict__ x, const unsigned short* __restrict__ kc, const unsigned short* __restrict__ vc,
                 const unsigned short* __restrict__ wq, const unsigned short* __restrict__ wo, unsigned short* __restrict__ out,
                 unsigned char* __restrict__ ws) {
  extern __shared__ __attribute__((aligned(16))) char smem[];
  LAS char* L = (LAS char*)smem;
  const unsigned lbase = (unsigned)(size_t)L;
  const int lane = threadIdx.x & 63, wave = rfl(threadIdx.x >> 6);
  const int g = lane >> 4, c = lane & 15;
  const int j = blockIdx.x, h = j & 31, bp = j >> 5;
  const unsigned ctl = lbase + L_CTRL;
  const __amdgpu_buffer_rsrc_t rsO = __builtin_amdgcn_make_buffer_rsrc((void*)ws, 0, WS_USED, 0x00020000);

  if (wave == 0) {
    __builtin_amdgcn_s_setprio(3);
    __builtin_amdgcn_s_waitcnt(0x0F70);
    static_assert(WIN <= 24 && WIN > 16 && WIN0 >= WIN && WIN0 <= 40, "publish ladder below; published counts never decrease");
    const unsigned vo0 = lane * 16, vo1 = lane * 16 + 4096;
    const unsigned sbase = (unsigned)(bp * NH + h) * (SK * DH * 2);
    const __amdgpu_buffer_rsrc_t rsK = __builtin_amdgcn_make_buffer_rsrc((void*)kc, 0, NB * NH * SK * DH * 2, 0x00020000);
    const __amdgpu_buffer_rsrc_t rsV = __builtin_amdgcn_make_buffer_rsrc((void*)vc, 0, NB * NH * SK * DH * 2, 0x00020000);
    unsigned m0b = 0, u = 0, lim = NR;
#pragma unroll 1
    for (int n = 0; n < NI; ++n) {
      dma_unit<48>(rsK, sbase + (unsigned)n * 8192u, m0b, u, lim, vo0, vo1, ctl, ctl + 252);
      lim = NR;
      dma_unit<48>(rsV, sbase + (unsigned)n * 8192u, m0b, u, lim, vo0, vo1, ctl, ctl + 252);
      lim = NR;
      if (n == 0) __builtin_amdgcn_s_barrier();
    }
    vm_wait<0>(); ctl_wr(ctl + 4 * C_FULL, 2 * NI);
    ctl_wait<1>(ctl + 4 * C_QI, QI_GO);
#pragma unroll 1
    for (int n = NI; n < N0; ++n) {
      dma_unit<WIN0>(rsK, sbase + (unsigned)n * 8192u, m0b, u, lim, vo0, vo1, ctl, 2 * (n - NI) < (WIN0 - 1) / 8 ? ctl + 252 : ctl + 4 * C_FULL);
      dma_unit<WIN0>(rsV, sbase + (unsigned)n * 8192u, m0b, u, lim, vo0, vo1, ctl, 2 * (n - NI) + 1 < (WIN0 - 1) / 8 ? ctl + 252 : ctl + 4 * C_FULL);
    }
#pragma unroll 1
    for (int n = N0; n < 128 - NE; ++n) {
      const unsigned soff = sbase + (unsigned)(n >> 6) * (8u * NH * SK * DH * 2) + (unsigned)(n & 63) * 8192u;
      if (DIAG_POL == 1 && n < 64 && bp >= DIAG_LO && bp < DIAG_HI || DIAG_POL == 2 && n >= 64 && bp >= DIAG_LO && bp < DIAG_HI) dma_unit<WIN, 0>(rsK, soff, m0b, u, lim, vo0, vo1, ctl, ctl + 4 * C_FULL);
      else dma_unit<WIN>(rsK, soff, m0b, u, lim, vo0, vo1, ctl, ctl + 4 * C_FULL);
      if (DIAG_POL == 3 && n < 64 && bp >= DIAG_LO && bp < DIAG_HI || DIAG_POL == 4 && n >= 64 && bp >= DIAG_LO && bp < DIAG_HI) dma_unit<WIN, 0>(rsV, soff, m0b, u, lim, vo0, vo1, ctl, ctl + 4 * C_FULL);
      else dma_unit<WIN>(rsV, soff, m0b, u, lim, vo0, vo1, ctl, ctl + 4 * C_FULL);
    }
#pragma unroll 1
    for (int n = 128 - NE; n < 128; ++n) {
      const unsigned soff = sbase + (8u * NH * SK * DH * 2) + (unsigned)(n & 63) * 8192u;
      const int k = 2 * (n - (128 - NE));
      dma_unit<WINE>(rsK, soff, m0b, u, lim, vo0, vo1, ctl, k < (WINE - 1) / 8 - (WIN - 1) / 8 ? ctl + 252 : ctl + 4 * C_FULL);
      dma_unit<WINE>(rsV, soff, m0b, u, lim, vo0, vo1, ctl, k + 1 < (WINE - 1) / 8 - (WIN - 1) / 8 ? ctl + 252 : ctl + 4 * C_FULL);
    }
    if (WINE > 48) { vm_wait<48>(); ctl_wr(ctl + 4 * C_FULL, NUNIT - 6); }
    if (WINE > 40) { vm_wait<40>(); ctl_wr(ctl + 4 * C_FULL, NUNIT - 5); }
    if (WINE > 32) { vm_wait<32>(); ctl_wr(ctl + 4 * C_FULL, NUNIT - 4); }
    if (WINE > 24) { vm_wait<24>(); ctl_wr(ctl + 4 * C_FULL, NUNIT - 3); }
    vm_wait<16>(); ctl_wr(ctl + 4 * C_FULL, NUNIT - 2);
    vm_wait<8>(); ctl_wr(ctl + 4 * C_FULL, NUNIT - 1);
    vm_wait<0>(); ctl_wr(ctl + 4 * C_FULL, NUNIT);
  } else if ((wave & 3) == 0 || wave == 1) {
    const int aw = wave == 1 ? 2 : (wave >> 2) - 1;
    if (aw == 0) { *(LAS unsigned*)(L + L_CTRL + 4 * lane) = 0u; LDS_WAIT(); }
    __builtin_amdgcn_s_barrier();
    const unsigned koff = (c & 7) * PIECE + (c >> 3) * 256 + g * 16;
    const unsigned voff = (4 * (g & 1) + (c >> 2)) * PIECE + (g >> 1) * 256 + (c & 3) * 16;
    const int obase = W_O + ((bp >> 1) * 8 + (j & 7)) * 16384 + (h >> 3) * 4096 + g * 256 + ((bp & 1) * 8 + (c & 7)) * 16;
    {
      const unsigned long long tag = __hip_atomic_load((GAS unsigned long long*)(ws + W_EPOCH), __ATOMIC_RELAXED, __HIP_MEMORY_SCOPE_AGENT) + 1ull;
      const v4u sv = {SENT, SENT, SENT, SENT};
      if (aw == 0) {
#pragma unroll
        for (int s = 0; s < 4; ++s) __builtin_amdgcn_raw_buffer_store_b128(sv, rsO, obase + (c >> 3) * (4 * 8 * 16384), s * 1024, O_ST_AUX);
        if (lane == 0) *(LAS v2u*)(L + L_CTRL + 4 * C_EP) = v2u{(unsigned)tag, (unsigned)(tag >> 32)};
      } else {
#pragma unroll
        for (int k = 0; k < 8; ++k) __builtin_amdgcn_raw_buffer_store_b128(sv, rsO, lane * 16, W_P + (aw - 1) * (2 << 20) + ((j >> 3) * 64 + k * 8 + (j & 7)) * 1024, P_ST_AUX);
      }
      vm_wait<0>();
      if (ctl_add(ctl + 4 * C_CLR) == NATT - 1 && lane == 0) __hip_atomic_store((GAS unsigned long long*)(ws + W_CF) + FSTR * j, tag, __ATOMIC_RELAXED, __HIP_MEMORY_SCOPE_AGENT);
    }
    v4u oA[4];
#pragma unroll
    for (int s = 0; s < 4; ++s) oA[s] = v4u{0u, 0u, 0u, 0u};
    v4u kf[8], vf[8];
    unsigned full = ctl_wait<1>(ctl + 4 * C_FULL, 2 * aw + 2);
    {
      CFENCE();
      LAS char* kb = L + (2 * aw) * UNIT + koff;
      LAS char* vb = L + (2 * aw + 1) * UNIT + voff;
#pragma unroll
      for (int i = 0; i < 8; ++i) kf[i] = *(LAS v4u*)(kb + (i >> 2) * 512 + (i & 3) * 64);
#pragma unroll
      for (int d = 0; d < 8; ++d) {
        const v2u a = trread(vb + (d >> 1) * 64 + (d & 1) * 8), b = trread(vb + 512 + (d >> 1) * 64 + (d & 1) * 8);
        vf[d] = v4u{a[0], a[1], b[0], b[1]};
      }
      LDS_WAIT();
      ctl_wr(ctl + 4 * (C_REL + aw), 2 * aw + 2 * NATT);
      PIN8(kf); PIN8(vf);
      if (aw == 0) STAMP(14);
      ctl_wait<1>(ctl + 4 * C_Q2, 4);
      CFENCE();
    }
    v4u qf[4];
#pragma unroll
    for (int s = 0; s < 4; ++s) {
      LAS char* qp = L + L_SCR + g * 2048 + (s * 16 + c) * 16;
      const v4f lo = (*(LAS v4f*)(qp) + *(LAS v4f*)(qp + 8192)) + (*(LAS v4f*)(qp + 16384) + *(LAS v4f*)(qp + 24576));
      const v4f hi = (*(LAS v4f*)(qp + 1024) + *(LAS v4f*)(qp + 9216)) + (*(LAS v4f*)(qp + 17408) + *(LAS v4f*)(qp + 25600));
      qf[s] = v4u{pk2(lo[0], lo[1]), pk2(lo[2], lo[3]), pk2(hi[0], hi[1]), pk2(hi[2], hi[3])};
      if (s & 1) asm volatile("" : "+v"(qf[s - 1]), "+v"(qf[s])::"memory");
    }
    if (aw == 0) STAMP(4);
    float m = -INFINITY, l = 0.f;
    v4f acc[8];
#pragma unroll
    for (int d = 0; d < 8; ++d) acc[d] = v4f{0.f, 0.f, 0.f, 0.f};
    unsigned pos = 2 * aw;
#pragma unroll 1
    for (int n = aw; n < 128; n += NATT) {
      const unsigned uk = 2 * n;
      const unsigned posv = pos + 1 == NR ? 0 : pos + 1;
      if (n >= NATT) {
        if (full < uk + 1) full = ctl_wait<1>(ctl + 4 * C_FULL, uk + 1);
        CFENCE();
        LAS char* kb = L + pos * UNIT + koff;
#pragma unroll
        for (int i = 0; i < 8; ++i) kf[i] = *(LAS v4u*)(kb + (i >> 2) * 512 + (i & 3) * 64);
        LDS_WAIT();
        ctl_wr(ctl + 4 * (C_REL + aw), uk + 1);
        PIN8(kf);
      }
      v4f s0 = {0.f, 0.f, 0.f, 0.f}, s1 = {0.f, 0.f, 0.f, 0.f};
#pragma unroll
      for (int s = 0; s < 4; ++s) { s0 = mfma(kf[s], qf[s], s0); s1 = mfma(kf[4 + s], qf[s], s1); }
      float mx = fmaxf(fmaxf(fmaxf(s0[0], s0[1]), fmaxf(s0[2], s0[3])), fmaxf(fmaxf(s1[0], s1[1]), fmaxf(s1[2], s1[3])));
      mx = xg_max(mx);
      const bool lastt = (n & 63) == 63;
      const int ph = n >> 6;
      LAS char* r0 = L + L_MRG;
      LAS char* r1 = L + L_MRG + MRG_REC;
      v2f a0 = {-INFINITY, 0.f}, a1 = {-INFINITY, 0.f};
      if (lastt) {
        ctl_wait<0>(ctl + 4 * (C_MRG + ph), 2);
        CFENCE();
        a0 = *(LAS v2f*)(r0 + 8192 + lane * 8);
        a1 = *(LAS v2f*)(r1 + 8192 + lane * 8);
        mx = fmaxf(mx, fmaxf(a0[0], a1[0]));
      }
      const float mn = fmaxf(m, mx);
      const float alpha = __builtin_amdgcn_exp2f((m - mn) * CSC);
      const float mc = mn * CSC;
      m = mn;
      float p[8];
#pragma unroll
      for (int i = 0; i < 4; ++i) { p[i] = __builtin_amdgcn_exp2f(fmaf(s0[i], CSC, -mc)); p[4 + i] = __builtin_amdgcn_exp2f(fmaf(s1[i], CSC, -mc)); }
      l = fmaf(l, alpha, ((p[0] + p[1]) + (p[2] + p[3])) + ((p[4] + p[5]) + (p[6] + p[7])));
      v4u pf = {pk2(p[0], p[1]), pk2(p[2], p[3]), pk2(p[4], p[5]), pk2(p[6], p[7])};
#pragma unroll
      for (int d = 0; d < 8; ++d) acc[d] *= alpha;
      if (lastt) {
        const float w0 = __builtin_amdgcn_exp2f((a0[0] - mn) * CSC), w1 = __builtin_amdgcn_exp2f((a1[0] - mn) * CSC);
        l = 1.0f / xg_sum(fmaf(w0, a0[1], fmaf(w1, a1[1], l)));
#pragma unroll
        for (int d = 0; d < 8; ++d) acc[d] += *(LAS v4f*)(r0 + d * 1024 + lane * 16) * w0 + *(LAS v4f*)(r1 + d * 1024 + lane * 16) * w1;
      }
      asm volatile("" : "+v"(pf), "+v"(l));
      PIN8(acc);
      if (n >= NATT) {
        if (full < uk + 2) full = ctl_wait<0>(ctl + 4 * C_FULL, uk + 2);
        CFENCE();
        LAS char* vb = L + posv * UNIT + voff;
#pragma unroll
        for (int d = 0; d < 8; ++d) {
          const v2u a = trread(vb + (d >> 1) * 64 + (d & 1) * 8), b = trread(vb + 512 + (d >> 1) * 64 + (d & 1) * 8);
          vf[d] = v4u{a[0], a[1], b[0], b[1]};
        }
        LDS_WAIT();
        ctl_wr(ctl + 4 * (C_REL + aw), uk + 2 * NATT);
        PIN8(vf);
      }
#pragma unroll
      for (int d = 0; d < 8; ++d) acc[d] = mfma(vf[d], pf, acc[d]);
      pos += 2 * NATT;
      if (pos >= NR) pos -= NR;
      if (n == OA_AT && c < 8) {
#pragma unroll
        for (int s = 0; s < 4; ++s) __builtin_amdgcn_raw_buffer_store_b128(oA[s], rsO, obase + s * 1024, 0, O_ST_AUX);
      }
      if ((n & 63) + NATT >= 64) {
        const bool mine = (c >> 3) == ph;
        if (!lastt) {
          LAS char* rec = L + L_MRG + (62 - (n & 63)) * MRG_REC;
          if (mine) {
#pragma unroll
            for (int d = 0; d < 8; ++d) *(LAS v4f*)(rec + d * 1024 + lane * 16) = acc[d];
            *(LAS v2f*)(rec + 8192 + lane * 8) = v2f{m, l};
          }
          LDS_WAIT();
          ctl_add(ctl + 4 * (C_MRG + ph));
        } else {
#pragma unroll
          for (int s = 0; s < 4; ++s) {
            const v4f x0 = acc[2 * s] * l, x1 = acc[2 * s + 1] * l;
            v4u o = {pk2(x0[0], x0[1]), pk2(x0[2], x0[3]), pk2(x1[0], x1[1]), pk2(x1[2], x1[3])};
#pragma unroll
            for (int k = 0; k < 4; ++k) o[k] = o[k] == SENT ? 0x7FFF7FFFu : o[k];
            if (ph == 0) oA[s] = o;
            else if (mine) __builtin_amdgcn_raw_buffer_store_b128(o, rsO, obase + s * 1024, 4 * 8 * 16384, O_ST_AUX);
          }
          STAMP(5 + ph);
        }
        m = -INFINITY; l = 0.f;
#pragma unroll
        for (int d = 0; d < 8; ++d) acc[d] = v4f{0.f, 0.f, 0.f, 0.f};
      }
    }
  } else {
    const int gw = wave - 2 - (wave >> 2);
    {
      const __amdgpu_buffer_rsrc_t rsQ = __builtin_amdgcn_make_buffer_rsrc((void*)wq, 0, ABL_NOQ ? 0 : DM * NH * DH * 2, 0x00020000);
      const int wvo = (8 * g * (NH * DH) + 8 * c) * 2, wso = (128 * gw * (NH * DH) + h * DH) * 2;
      const int xrow = ((c < 8 ? bp : bp + 8) * SQ + (c & 7));
      const GAS char* xb = (const GAS char*)x + ((size_t)xrow * DM + 128 * gw + 8 * g) * 2;
#define WQ_ROW(t, jj) ((32 * (((t) + (bp >> 1)) & 3) + ((jj) ^ (4 * (bp & 1)))) * (NH * DH * 2))
      v4u xf[4], w[4][8];
      __builtin_amdgcn_s_barrier();
      __builtin_amdgcn_sched_barrier(0);
#pragma unroll
      for (int t = 0; t < 3; ++t)
#pragma unroll
        for (int jj = 0; jj < 8; ++jj) {
          if (t == 0 && jj == 4) { vm_wait<0>(); __builtin_amdgcn_s_sleep(Q_GAP); }
          w[t][jj] = __builtin_amdgcn_raw_buffer_load_b128(rsQ, wvo, wso + WQ_ROW(t, jj), AUX_NT);
          if (t == 0 && jj == 3) {
#pragma unroll
            for (int tt = 0; tt < 4; ++tt) xf[tt] = X_NT ? __builtin_nontemporal_load((const GAS v4u*)(xb + ((tt + (bp >> 1)) & 3) * 64)) : *(const GAS v4u*)(xb + ((tt + (bp >> 1)) & 3) * 64);
          }
          __builtin_amdgcn_sched_barrier(0);
        }
      ctl_add(ctl + 4 * C_QI);
      if (bp & 1) {
#pragma unroll
        for (int t = 0; t < 4; ++t) xf[t] = v4u{xf[t][2], xf[t][3], xf[t][0], xf[t][1]};
      }
      v4f qa[8];
#pragma unroll
      for (int d = 0; d < 8; ++d) qa[d] = v4f{0.f, 0.f, 0.f, 0.f};
#pragma unroll
      for (int t = 0; t < 4; ++t) {
#pragma unroll
        for (int dd = 0; dd < 8; ++dd) {
          v4u f;
#pragma unroll
          for (int v = 0; v < 4; ++v) f[v] = __builtin_amdgcn_perm(w[t][2 * v + 1][dd >> 1], w[t][2 * v][dd >> 1], (dd & 1) ? 0x07060302u : 0x05040100u);
          qa[dd] = mfma(f, xf[t], qa[dd]);
          if (dd & 1) __builtin_amdgcn_sched_barrier(0);
        }
        if (t == 0) {
#pragma unroll
          for (int jj = 0; jj < 8; ++jj) {
            w[3][jj] = __builtin_amdgcn_raw_buffer_load_b128(rsQ, wvo, wso + WQ_ROW(3, jj), AUX_NT);
            __builtin_amdgcn_sched_barrier(0);
          }
        }
      }
      STAMP(16 + gw);
      LAS char* part = L + L_SCR + (gw & 3) * 8192 + lane * 16;
      if (gw >= 4) {
        ctl_wait<0>(ctl + 4 * C_Q1, 4);
        CFENCE();
      }
#pragma unroll
      for (int i = 0; i < 4; ++i) {
        v4f lo = {qa[0][i], qa[1][i], qa[2][i], qa[3][i]}, hi = {qa[4][i], qa[5][i], qa[6][i], qa[7][i]};
        if (gw >= 4) { lo += *(LAS v4f*)(part + i * 2048); hi += *(LAS v4f*)(part + i * 2048 + 1024); }
        *(LAS v4f*)(part + i * 2048) = lo;
        *(LAS v4f*)(part + i * 2048 + 1024) = hi;
      }
      LDS_WAIT();
      ctl_add(ctl + 4 * (gw >= 4 ? C_Q2 : C_Q1));
      if (gw == 0) STAMP(7);
      if (gw == 7) STAMP(8);
    }
    const int xc = j & 7, idx = j >> 3, ct = idx & 7, rg = idx >> 3;
    unsigned hb = 0;
#define HBAR(SLP) do { hb += NHELP; ctl_add(ctl + 4 * C_HB); ctl_wait<SLP>(ctl + 4 * C_HB, hb); CFENCE(); } while (0)
    const int pdst = W_P + (idx * 64 + gw * 8 + xc) * 1024;
    v4u wf[16];
    {
      const __amdgpu_buffer_rsrc_t rsW = __builtin_amdgcn_make_buffer_rsrc((void*)wo, 0, ABL_NOWO ? 0 : DM * NH * DH * 2, 0x00020000);
      ctl_wait<32>(ctl + 4 * C_FULL, 40 + 2 * gw);
#pragma unroll
      for (int ks = 0; ks < 16; ++ks) {
        wf[ks] = __builtin_amdgcn_raw_buffer_load_b128(rsW, g * 2048 + c * 16, (((ks >> 2) * 8 + xc) * 128 + (ks & 3) * 32 + 4 * gw) * 2048 + ct * 256, AUX_NT);
        __builtin_amdgcn_s_sleep(WF_SLEEP);
        __builtin_amdgcn_sched_barrier(0);
      }
      LAS char* sw = L + L_OB + gw * PIECE + lane * 16;
      LAS char* sr = L + L_OB + 2 * g * PIECE + (c >> 2) * 256 + gw * 32 + (c & 3) * 8;
#pragma unroll
      for (int r = 0; r < 8; ++r) {
        *(LAS v4u*)(sw) = wf[2 * r];
        *(LAS v4u*)(sw + 8 * PIECE) = wf[2 * r + 1];
        LDS_WAIT();
        HBAR(2);
        const v2u a0 = trread(sr), b0 = trread(sr + PIECE), a1 = trread(sr + 8 * PIECE), b1 = trread(sr + 9 * PIECE);
        wf[2 * r] = v4u{a0[0], a0[1], b0[0], b0[1]};
        wf[2 * r + 1] = v4u{a1[0], a1[1], b1[0], b1[1]};
        LDS_WAIT();
        asm volatile("" : "+v"(wf[2 * r]), "+v"(wf[2 * r + 1])::"memory");
        HBAR(2);
      }
    }
    const v2u tagv = *(LAS v2u*)(L + L_CTRL + 4 * C_EP);
    const unsigned long long tag = ((unsigned long long)tagv[1] << 32) | tagv[0];
    if (gw == 7) STAMP(9);
    ctl_wait<16>(ctl + 4 * C_FULL, NUNIT - G1_LEAD);
    if (gw == 7) STAMP(10);
    {
      const int src = lane < 8 ? idx * 8 + lane : (2 * rg + (lane & 1)) * 32 + (gw >> 1) * 8 + xc;
      GAS unsigned long long* fl = (GAS unsigned long long*)(ws + W_CF) + FSTR * src;
      for (unsigned sp = 0; sp < (1u << 16); ++sp) {
        if (__all(__hip_atomic_load(fl, __ATOMIC_RELAXED, __HIP_MEMORY_SCOPE_AGENT) == tag)) break;
        __builtin_amdgcn_s_sleep(32);
      }
      __builtin_amdgcn_fence(__ATOMIC_ACQUIRE, "wavefront");
    }
    if (gw == 7) STAMP(11);
#pragma unroll 1
    for (int ph = 0; ph < 2; ++ph) {
      v4u f0, f1;
      for (unsigned sp = 0; sp < (1u << 16); ++sp) {
        CFENCE();
        f0 = __builtin_amdgcn_raw_buffer_load_b128(rsO, lane * 16, W_O + ((ph * 4 + rg) * 8 + xc) * 16384 + gw * 2048, AUX_SC1);
        f1 = __builtin_amdgcn_raw_buffer_load_b128(rsO, lane * 16, W_O + ((ph * 4 + rg) * 8 + xc) * 16384 + gw * 2048 + 1024, AUX_SC1);
        bool ok = true;
#pragma unroll
        for (int k = 0; k < 4; ++k) ok &= (f0[k] != SENT) & (f1[k] != SENT);
        if (__all(ok)) break;
        __builtin_amdgcn_s_sleep(G1_SLEEP);
      }
      *(LAS v4u*)(L + L_OB + gw * 2048 + lane * 16) = f0;
      *(LAS v4u*)(L + L_OB + gw * 2048 + 1024 + lane * 16) = f1;
      LDS_WAIT();
      HBAR(0);
      v4f oa[4];
#pragma unroll
      for (int k = 0; k < 4; ++k) oa[k] = v4f{0.f, 0.f, 0.f, 0.f};
#pragma unroll
      for (int f = 0; f < 16; ++f) oa[f & 3] = mfma(wf[f], *(LAS v4u*)(L + L_OB + f * 1024 + lane * 16), oa[f & 3]);
      v4u pt = __builtin_bit_cast(v4u, (oa[0] + oa[1]) + (oa[2] + oa[3]));
#pragma unroll
      for (int k = 0; k < 4; ++k) pt[k] = pt[k] == SENT ? 0x7FFFFFFFu : pt[k];
      __builtin_amdgcn_raw_buffer_store_b128(pt, rsO, lane * 16, pdst + ph * (2 << 20), P_ST_AUX);
      if (gw == 7) STAMP(24 + ph);
      if (ph == 0) HBAR(0);
    }
    if (gw < 2) {
      v4u q[8];
      for (unsigned sp = 0; sp < (1u << 16); ++sp) {
        CFENCE();
        bool ok = true;
#pragma unroll
        for (int k = 0; k < 8; ++k) q[k] = __builtin_amdgcn_raw_buffer_load_b128(rsO, lane * 16, W_P + gw * (2 << 20) + (idx * 64 + xc * 8 + k) * 1024, AUX_SC1);
#pragma unroll
        for (int k = 0; k < 8; ++k) ok &= (q[k][0] != SENT) & (q[k][1] != SENT) & (q[k][2] != SENT) & (q[k][3] != SENT);
        if (__all(ok)) break;
        __builtin_amdgcn_s_sleep(1);
      }
      v4f s = __builtin_bit_cast(v4f, q[0]);
#pragma unroll
      for (int k = 1; k < 8; ++k) s += __builtin_bit_cast(v4f, q[k]);
      *(GAS v2u*)((GAS char*)out + ((size_t)(64 * gw + 16 * rg + c) * DM + 128 * ct + 16 * xc + 4 * g) * 2) = v2u{pk2(s[0], s[1]), pk2(s[2], s[3])};
      STAMP(12 + gw);
    } else if (gw == 2 && j == 0) {
      GAS unsigned long long* fl = (GAS unsigned long long*)(ws + W_CF) + FSTR * lane;
      for (unsigned sp = 0; sp < (1u << 16); ++sp) {
        bool ok = true;
#pragma unroll
        for (int k = 0; k < 4; ++k) ok &= __hip_atomic_load(fl + FSTR * 64 * k, __ATOMIC_RELAXED, __HIP_MEMORY_SCOPE_AGENT) == tag;
        if (__all(ok)) break;
        __builtin_amdgcn_s_sleep(32);
      }
      if (lane == 0) __hip_atomic_store((GAS unsigned long long*)(ws + W_EPOCH), tag, __ATOMIC_RELAXED, __HIP_MEMORY_SCOPE_AGENT);
    }
  }
}
}

extern "C" void kernel_launch(void* const* d_in, const int* in_sizes, int n_in, void* d_out, int out_size, void* d_ws, size_t ws_size,
                              hipStream_t stream) {
  (void)in_sizes; (void)n_in; (void)out_size; (void)ws_size;
  (void)hipFuncSetAttribute((const void*)mha_block_kernel, hipFuncAttributeMaxDynamicSharedMemorySize, LDS_BYTES);
  mha_block_kernel<<<NWG, NTHR, LDS_BYTES, stream>>>((const unsigned short*)d_in[0], (const unsigned short*)d_in[1], (const unsigned short*)d_in[2],
                                                     (const unsigned short*)d_in[3], (const unsigned short*)d_in[4], (unsigned short*)d_out,
                                                     (unsigned char*)d_ws);
}
```

```cpp
#define WIN 18
#define WIN0 20
#define WINE 24
#include <hip/hip_runtime.h>
#include <hip/hip_bf16.h>
#include <stdint.h>
#include <stdio.h>

#define GAS __attribute__((address_space(1)))
#define LAS __attribute__((address_space(3)))
typedef unsigned v4u __attribute__((ext_vector_type(4)));
typedef unsigned v2u __attribute__((ext_vector_type(2)));
typedef float v4f __attribute__((ext_vector_type(4)));
typedef float v2f __attribute__((ext_vector_type(2)));
typedef short v4s __attribute__((ext_vector_type(4)));
typedef __bf16 v8bf __attribute__((ext_vector_type(8)));
typedef __bf16 v2bf __attribute__((ext_vector_type(2)));

#define N0 12
#define NE 4
#define NI 2
#define QI_GO 5
#define HQ_WIN 40
#define WF_SLEEP 16
#define G1_LEAD 4
#define G1_SLEEP 4
#define ABL_NOST 0
#define ABL_NOG0 0
#define Q_GAP 4
#define ABL_NOQ 0
#define X_NT 0
#define P_ST_AUX 16
#define G0_AT 160
#define ABL_NOWO 0
#define ABL_NOCLR 0
#define OA_AT 126
#define O_ST_AUX 16

namespace {
constexpr int NB = 16, SQ = 8, DM = 1024, NH = 32, SK = 2048, DH = 128;
constexpr int NWG = 256, NTHR = 768, NATT = 3, NHELP = 8;
constexpr int PIECE = 1056;
constexpr int UNIT = 8 * PIECE;
constexpr int NR = 15;
constexpr int NUNIT = 256;
constexpr int AUX_NT = 2, AUX_SC1 = 16;
constexpr int L_CTRL = NR * UNIT;
constexpr int L_SCR = L_CTRL + 256;
constexpr int L_MRG = L_SCR;
constexpr int MRG_REC = 8192 + 512;
constexpr int L_OB = L_SCR + 2 * MRG_REC;
constexpr int LDS_BYTES = 160 * 1024;
static_assert(L_SCR + 32768 <= LDS_BYTES && L_OB + 16 * PIECE <= LDS_BYTES, "LDS map");
enum { C_FULL = 0, C_REL = 4, C_Q1 = 8, C_Q2 = 9, C_QI = 10, C_HB = 11, C_MRG = 12, C_EP = 14, C_CLR = 16 };
constexpr int W_EPOCH = 0, W_CF = 65536, FSTR = 16;
constexpr int W_O = 1 << 20;
constexpr int W_P = 2 << 20;
constexpr int WS_USED = 6 << 20;
constexpr unsigned SENT = 0xFFFFFFFFu;
constexpr float CSC = 0.08838834764831845f * 1.4426950408889634f;
constexpr unsigned SPIN_CAP = 1u << 22;

__device__ __forceinline__ unsigned rfl(unsigned x) { return __builtin_amdgcn_readfirstlane(x); }
__device__ __forceinline__ v4f mfma(v4u a, v4u b, v4f c) {
  return __builtin_amdgcn_mfma_f32_16x16x32_bf16(__builtin_bit_cast(v8bf, a), __builtin_bit_cast(v8bf, b), c, 0, 0, 0);
}
__device__ __forceinline__ unsigned pk2(float lo, float hi) {
  v2f x = {lo, hi};
  return __builtin_bit_cast(unsigned, __builtin_convertvector(x, v2bf));
}
__device__ __forceinline__ v2u trread(LAS char* p) {
  return __builtin_bit_cast(v2u, __builtin_amdgcn_ds_read_tr16_b64_v4i16((LAS v4s*)p));
}
__device__ __forceinline__ float xg_max(float x) {
  auto s = __builtin_amdgcn_permlane16_swap(__float_as_uint(x), __float_as_uint(x), false, false);
  x = fmaxf(__uint_as_float(s[0]), __uint_as_float(s[1]));
  auto t = __builtin_amdgcn_permlane32_swap(__float_as_uint(x), __float_as_uint(x), false, false);
  return fmaxf(__uint_as_float(t[0]), __uint_as_float(t[1]));
}
__device__ __forceinline__ float xg_sum(float x) {
  auto s = __builtin_amdgcn_permlane16_swap(__float_as_uint(x), __float_as_uint(x), false, false);
  x = __uint_as_float(s[0]) + __uint_as_float(s[1]);
  auto t = __builtin_amdgcn_permlane32_swap(__float_as_uint(x), __float_as_uint(x), false, false);
  return __uint_as_float(t[0]) + __uint_as_float(t[1]);
}
#define STAMP(k)
#define STAMPV(k, v)
#define LDS_WAIT() asm volatile("s_waitcnt lgkmcnt(0)" ::: "memory")
#define CFENCE() asm volatile("" ::: "memory")
#define PIN8(a) asm volatile("" : "+v"(a[0]), "+v"(a[1]), "+v"(a[2]), "+v"(a[3]), "+v"(a[4]), "+v"(a[5]), "+v"(a[6]), "+v"(a[7])::"memory")
template <int N> __device__ __forceinline__ void vm_wait() { asm volatile("s_waitcnt vmcnt(%c0)" ::"n"(N) : "memory"); }
__device__ __forceinline__ unsigned ctl_rd(unsigned addr) {
  unsigned v;
  asm volatile("ds_read_b32 %0, %1\n\ts_waitcnt lgkmcnt(0)" : "=v"(v) : "v"(addr) : "memory");
  return rfl(v);
}
__device__ __forceinline__ void ctl_wr(unsigned addr, unsigned val) {
  asm volatile("s_mov_b64 exec, 1\n\tds_write_b32 %0, %1\n\ts_mov_b64 exec, -1" ::"v"(addr), "v"(val) : "memory");
}
__device__ __forceinline__ unsigned ctl_add(unsigned addr) {
  unsigned v;
  asm volatile("s_mov_b64 exec, 1\n\tds_add_rtn_u32 %0, %1, %2\n\ts_mov_b64 exec, -1\n\ts_waitcnt lgkmcnt(0)" : "=&v"(v) : "v"(addr), "v"(1u) : "memory");
  return rfl(v);
}
template <int SLP> __device__ __forceinline__ unsigned ctl_wait(unsigned addr, unsigned need) {
  unsigned v = ctl_rd(addr);
  for (unsigned sp = 0; v < need && sp < SPIN_CAP; ++sp) {
    __builtin_amdgcn_s_sleep(SLP);
    v = ctl_rd(addr);
  }
  return v;
}

#define PUBL(k) ".if %c[b] == " #k "\n\ts_mov_b64 exec, 1\n\tds_write_b32 %[fa], v23\n\ts_mov_b64 exec, -1\n\t.endif\n\t"
#define PC(k, VO, IMM) "s_waitcnt vmcnt(%c[w])\n\t" PUBL(k) ".if %c[nt]\n\tbuffer_load_dwordx4 " VO ", %[rs], %[so] offen offset:" IMM " nt lds\n\t.else\n\tbuffer_load_dwordx4 " VO ", %[rs], %[so] offen offset:" IMM " lds\n\t.endif\n\t"
template <int W, int NTF = 1>
__device__ __forceinline__ void dma_unit(__amdgpu_buffer_rsrc_t rs, unsigned soff, unsigned& m0b, unsigned& u, unsigned& lim, unsigned vo0, unsigned vo1,
                                         unsigned vctl, unsigned faddr) {
  unsigned sp;
  asm volatile(
      "s_cmp_lt_u32 %[u], %[lim]\n\t"
      "s_cbranch_scc1 2f\n\t"
      "s_mov_b32 %[sp], 0\n"
      "1:\n\t"
      "ds_read_b32 v20, %[vc] offset:16\n\tds_read_b32 v21, %[vc] offset:20\n\tds_read_b32 v22, %[vc] offset:24\n\t"
      "s_waitcnt lgkmcnt(0)\n\t"
      "v_min3_u32 v20, v20, v21, v22\n\t"
      "s_add_u32 %[sp], %[sp], 1\n\t"
      "v_readfirstlane_b32 %[lim], v20\n\t"
      "s_add_u32 %[lim], %[lim], %c[nr]\n\t"
      "s_cmp_lt_u32 %[u], %[lim]\n\t"
      "s_cbranch_scc1 2f\n\t"
      "s_sleep 1\n\t"
      "s_cmp_lt_u32 %[sp], %c[cap]\n\t"
      "s_cbranch_scc1 1b\n"
      "2:\n\t"
      "s_mov_b32 m0, %[m0b]\n\t"
      "v_mov_b32 v23, %[u]\n\t"
      "v_subrev_u32 v23, %c[a], v23\n\t"
      PC(0, "%[vo0]", "0")
      "ds_read_b32 v20, %[vc] offset:16\n\tds_read_b32 v21, %[vc] offset:20\n\tds_read_b32 v22, %[vc] offset:24\n\t"
      "s_add_u32 m0, m0, 32\n\t"
      PC(1, "%[vo0]", "1024")
      "s_add_u32 m0, m0, 32\n\t"
      PC(2, "%[vo0]", "2048")
      "s_add_u32 m0, m0, 32\n\t"
      PC(3, "%[vo0]", "3072")
      "s_add_u32 m0, m0, 0x1020\n\t"
      PC(4, "%[vo1]", "0")
      "s_waitcnt lgkmcnt(0)\n\t"
      "v_min3_u32 v20, v20, v21, v22\n\t"
      "s_add_u32 m0, m0, 32\n\t"
      PC(5, "%[vo1]", "1024")
      "v_readfirstlane_b32 %[lim], v20\n\t"
      "s_add_u32 %[m0b], %[m0b], %c[unit]\n\t"
      "s_add_u32 m0, m0, 32\n\t"
      PC(6, "%[vo1]", "2048")
      "s_add_u32 %[lim], %[lim], %c[nr]\n\t"
      "s_cmp_lt_u32 %[m0b], %c[ring]\n\t"
      "s_cselect_b32 %[m0b], %[m0b], 0\n\t"
      "s_add_u32 %[u], %[u], 1\n\t"
      "s_add_u32 m0, m0, 32\n\t"
      PC(7, "%[vo1]", "3072")
      : [m0b] "+s"(m0b), [u] "+s"(u), [lim] "+s"(lim), [sp] "=&s"(sp)
      : [nt] "n"(NTF), [w] "n"(W - 1), [a] "n"((W - 1) / 8), [b] "n"((W - 1) % 8), [nr] "n"(NR), [cap] "n"(SPIN_CAP), [unit] "n"(UNIT), [ring] "n"(NR * UNIT), [rs] "s"(rs),
        [so] "s"(soff), [vo0] "v"(vo0), [vo1] "v"(vo1), [vc] "v"(vctl), [fa] "v"(faddr)
      : "memory", "scc", "v20", "v21", "v22", "v23");
}

__global__ void __launch_bounds__(NTHR, 3)
mha_block_kernel(const unsigned short* __restrict__ x, const unsigned short* __restrict__ kc, const unsigned short* __restrict__ vc,
                 const unsigned short* __restrict__ wq, const unsigned short* __restrict__ wo, unsigned short* __restrict__ out,
                 unsigned char* __restrict__ ws) {
  extern __shared__ __attribute__((aligned(16))) char smem[];
  LAS char* L = (LAS char*)smem;
  const unsigned lbase = (unsigned)(size_t)L;
  const int lane = threadIdx.x & 63, wave = rfl(threadIdx.x >> 6);
  const int g = lane >> 4, c = lane & 15;
  const int j = blockIdx.x, h = j & 31, bp = j >> 5;
  const unsigned ctl = lbase + L_CTRL;
  const __amdgpu_buffer_rsrc_t rsO = __builtin_amdgcn_make_buffer_rsrc((void*)ws, 0, WS_USED, 0x00020000);

  if (wave == 0) {
    __builtin_amdgcn_s_setprio(3);
    __builtin_amdgcn_s_waitcnt(0x0F70);
    static_assert(WIN <= 24 && WIN > 16 && WIN0 >= WIN && WIN0 <= 40, "publish ladder below; published counts never decrease");
    const unsigned vo0 = lane * 16, vo1 = lane * 16 + 4096;
    const unsigned sbase = (unsigned)(bp * NH + h) * (SK * DH * 2);
    const __amdgpu_buffer_rsrc_t rsK = __builtin_amdgcn_make_buffer_rsrc((void*)kc, 0, NB * NH * SK * DH * 2, 0x00020000);
    const __amdgpu_buffer_rsrc_t rsV = __builtin_amdgcn_make_buffer_rsrc((void*)vc, 0, NB * NH * SK * DH * 2, 0x00020000);
    unsigned m0b = 0, u = 0, lim = NR;
#pragma unroll 1
    for (int n = 0; n < NI; ++n) {
      dma_unit<48>(rsK, sbase + (unsigned)n * 8192u, m0b, u, lim, vo0, vo1, ctl, ctl + 252);
      lim = NR;
      dma_unit<48>(rsV, sbase + (unsigned)n * 8192u, m0b, u, lim, vo0, vo1, ctl, ctl + 252);
      lim = NR;
      if (n == 0) __builtin_amdgcn_s_barrier();
    }
    vm_wait<0>(); ctl_wr(ctl + 4 * C_FULL, 2 * NI);
    ctl_wait<1>(ctl + 4 * C_QI, QI_GO);
#pragma unroll 1
    for (int n = NI; n < N0; ++n) {
      dma_unit<WIN0>(rsK, sbase + (unsigned)n * 8192u, m0b, u, lim, vo0, vo1, ctl, 2 * (n - NI) < (WIN0 - 1) / 8 ? ctl + 252 : ctl + 4 * C_FULL);
      dma_unit<WIN0>(rsV, sbase + (unsigned)n * 8192u, m0b, u, lim, vo0, vo1, ctl, 2 * (n - NI) + 1 < (WIN0 - 1) / 8 ? ctl + 252 : ctl + 4 * C_FULL);
    }
#pragma unroll 1
    for (int n = N0; n < 128 - NE; ++n) {
      const unsigned soff = sbase + (unsigned)(n >> 6) * (8u * NH * SK * DH * 2) + (unsigned)(n & 63) * 8192u;
      dma_unit<WIN>(rsK, soff, m0b, u, lim, vo0, vo1, ctl, ctl + 4 * C_FULL);
      dma_unit<WIN>(rsV, soff, m0b, u, lim, vo0, vo1, ctl, ctl + 4 * C_FULL);
    }
#pragma unroll 1
    for (int n = 128 - NE; n < 128; ++n) {
      const unsigned soff = sbase + (8u * NH * SK * DH * 2) + (unsigned)(n & 63) * 8192u;
      const int k = 2 * (n - (128 - NE));
      dma_unit<WINE>(rsK, soff, m0b, u, lim, vo0, vo1, ctl, k < (WINE - 1) / 8 - (WIN - 1) / 8 ? ctl + 252 : ctl + 4 * C_FULL);
      dma_unit<WINE>(rsV, soff, m0b, u, lim, vo0, vo1, ctl, k + 1 < (WINE - 1) / 8 - (WIN - 1) / 8 ? ctl + 252 : ctl + 4 * C_FULL);
    }
    if (WINE > 48) { vm_wait<48>(); ctl_wr(ctl + 4 * C_FULL, NUNIT - 6); }
    if (WINE > 40) { vm_wait<40>(); ctl_wr(ctl + 4 * C_FULL, NUNIT - 5); }
    if (WINE > 32) { vm_wait<32>(); ctl_wr(ctl + 4 * C_FULL, NUNIT - 4); }
    if (WINE > 24) { vm_wait<24>(); ctl_wr(ctl + 4 * C_FULL, NUNIT - 3); }
    vm_wait<16>(); ctl_wr(ctl + 4 * C_FULL, NUNIT - 2);
    vm_wait<8>(); ctl_wr(ctl + 4 * C_FULL, NUNIT - 1);
    vm_wait<0>(); ctl_wr(ctl + 4 * C_FULL, NUNIT);
  } else if ((wave & 3) == 0 || wave == 1) {
    const int aw = wave == 1 ? 2 : (wave >> 2) - 1;
    if (aw == 0) { *(LAS unsigned*)(L + L_CTRL + 4 * lane) = 0u; LDS_WAIT(); }
    __builtin_amdgcn_s_barrier();
    const unsigned koff = (c & 7) * PIECE + (c >> 3) * 256 + g * 16;
    const unsigned voff = (4 * (g & 1) + (c >> 2)) * PIECE + (g >> 1) * 256 + (c & 3) * 16;
    const int obase = W_O + ((bp >> 1) * 8 + (j & 7)) * 16384 + (h >> 3) * 4096 + g * 256 + ((bp & 1) * 8 + (c & 7)) * 16;
    {
      const unsigned long long tag = __hip_atomic_load((GAS unsigned long long*)(ws + W_EPOCH), __ATOMIC_RELAXED, __HIP_MEMORY_SCOPE_AGENT) + 1ull;
      const v4u sv = {SENT, SENT, SENT, SENT};
      if (aw == 0) {
#pragma unroll
        for (int s = 0; s < 4; ++s) __builtin_amdgcn_raw_buffer_store_b128(sv, rsO, obase + (c >> 3) * (4 * 8 * 16384), s * 1024, O_ST_AUX);
        if (lane == 0) *(LAS v2u*)(L + L_CTRL + 4 * C_EP) = v2u{(unsigned)tag, (unsigned)(tag >> 32)};
      } else {
#pragma unroll
        for (int k = 0; k < 8; ++k) __builtin_amdgcn_raw_buffer_store_b128(sv, rsO, lane * 16, W_P + (aw - 1) * (2 << 20) + ((j >> 3) * 64 + k * 8 + (j & 7)) * 1024, P_ST_AUX);
      }
      vm_wait<0>();
      if (ctl_add(ctl + 4 * C_CLR) == NATT - 1 && lane == 0) __hip_atomic_store((GAS unsigned long long*)(ws + W_CF) + FSTR * j, tag, __ATOMIC_RELAXED, __HIP_MEMORY_SCOPE_AGENT);
    }
    v4u oA[4];
#pragma unroll
    for (int s = 0; s < 4; ++s) oA[s] = v4u{0u, 0u, 0u, 0u};
    v4u kf[8], vf[8];
    unsigned full = ctl_wait<1>(ctl + 4 * C_FULL, 2 * aw + 2);
    {
      CFENCE();
      LAS char* kb = L + (2 * aw) * UNIT + koff;
      LAS char* vb = L + (2 * aw + 1) * UNIT + voff;
#pragma unroll
      for (int i = 0; i < 8; ++i) kf[i] = *(LAS v4u*)(kb + (i >> 2) * 512 + (i & 3) * 64);
#pragma unroll
      for (int d = 0; d < 8; ++d) {
        const v2u a = trread(vb + (d >> 1) * 64 + (d & 1) * 8), b = trread(vb + 512 + (d >> 1) * 64 + (d & 1) * 8);
        vf[d] = v4u{a[0], a[1], b[0], b[1]};
      }
      LDS_WAIT();
      ctl_wr(ctl + 4 * (C_REL + aw), 2 * aw + 2 * NATT);
      PIN8(kf); PIN8(vf);
      if (aw == 0) STAMP(14);
      ctl_wait<1>(ctl + 4 * C_Q2, 4);
      CFENCE();
    }
    v4u qf[4];
#pragma unroll
    for (int s = 0; s < 4; ++s) {
      LAS char* qp = L + L_SCR + g * 2048 + (s * 16 + c) * 16;
      const v4f lo = (*(LAS v4f*)(qp) + *(LAS v4f*)(qp + 8192)) + (*(LAS v4f*)(qp + 16384) + *(LAS v4f*)(qp + 24576));
      const v4f hi = (*(LAS v4f*)(qp + 1024) + *(LAS v4f*)(qp + 9216)) + (*(LAS v4f*)(qp + 17408) + *(LAS v4f*)(qp + 25600));
      qf[s] = v4u{pk2(lo[0], lo[1]), pk2(lo[2], lo[3]), pk2(hi[0], hi[1]), pk2(hi[2], hi[3])};
      if (s & 1) asm volatile("" : "+v"(qf[s - 1]), "+v"(qf[s])::"memory");
    }
    if (aw == 0) STAMP(4);
    float m = -INFINITY, l = 0.f;
    v4f acc[8];
#pragma unroll
    for (int d = 0; d < 8; ++d) acc[d] = v4f{0.f, 0.f, 0.f, 0.f};
    unsigned pos = 2 * aw;
#pragma unroll 1
    for (int n = aw; n < 128; n += NATT) {
      const unsigned uk = 2 * n;
      const unsigned posv = pos + 1 == NR ? 0 : pos + 1;
      if (n >= NATT) {
        if (full < uk + 1) full = ctl_wait<1>(ctl + 4 * C_FULL, uk + 1);
        CFENCE();
        LAS char* kb = L + pos * UNIT + koff;
#pragma unroll
        for (int i = 0; i < 8; ++i) kf[i] = *(LAS v4u*)(kb + (i >> 2) * 512 + (i & 3) * 64);
        LDS_WAIT();
        ctl_wr(ctl + 4 * (C_REL + aw), uk + 1);
        PIN8(kf);
      }
      v4f s0 = {0.f, 0.f, 0.f, 0.f}, s1 = {0.f, 0.f, 0.f, 0.f};
#pragma unroll
      for (int s = 0; s < 4; ++s) { s0 = mfma(kf[s], qf[s], s0); s1 = mfma(kf[4 + s], qf[s], s1); }
      float mx = fmaxf(fmaxf(fmaxf(s0[0], s0[1]), fmaxf(s0[2], s0[3])), fmaxf(fmaxf(s1[0], s1[1]), fmaxf(s1[2], s1[3])));
      mx = xg_max(mx);
      const bool lastt = (n & 63) == 63;
      const int ph = n >> 6;
      LAS char* r0 = L + L_MRG;
      LAS char* r1 = L + L_MRG + MRG_REC;
      v2f a0 = {-INFINITY, 0.f}, a1 = {-INFINITY, 0.f};
      if (lastt) {
        ctl_wait<0>(ctl + 4 * (C_MRG + ph), 2);
        CFENCE();
        a0 = *(LAS v2f*)(r0 + 8192 + lane * 8);
        a1 = *(LAS v2f*)(r1 + 8192 + lane * 8);
        mx = fmaxf(mx, fmaxf(a0[0], a1[0]));
      }
      const float mn = fmaxf(m, mx);
      const float alpha = __builtin_amdgcn_exp2f((m - mn) * CSC);
      const float mc = mn * CSC;
      m = mn;
      float p[8];
#pragma unroll
      for (int i = 0; i < 4; ++i) { p[i] = __builtin_amdgcn_exp2f(fmaf(s0[i], CSC, -mc)); p[4 + i] = __builtin_amdgcn_exp2f(fmaf(s1[i], CSC, -mc)); }
      l = fmaf(l, alpha, ((p[0] + p[1]) + (p[2] + p[3])) + ((p[4] + p[5]) + (p[6] + p[7])));
      v4u pf = {pk2(p[0], p[1]), pk2(p[2], p[3]), pk2(p[4], p[5]), pk2(p[6], p[7])};
#pragma unroll
      for (int d = 0; d < 8; ++d) acc[d] *= alpha;
      if (lastt) {
        const float w0 = __builtin_amdgcn_exp2f((a0[0] - mn) * CSC), w1 = __builtin_amdgcn_exp2f((a1[0] - mn) * CSC);
        l = 1.0f / xg_sum(fmaf(w0, a0[1], fmaf(w1, a1[1], l)));
#pragma unroll
        for (int d = 0; d < 8; ++d) acc[d] += *(LAS v4f*)(r0 + d * 1024 + lane * 16) * w0 + *(LAS v4f*)(r1 + d * 1024 + lane * 16) * w1;
      }
      asm volatile("" : "+v"(pf), "+v"(l));
      PIN8(acc);
      if (n >= NATT) {
        if (full < uk + 2) full = ctl_wait<0>(ctl + 4 * C_FULL, uk + 2);
        CFENCE();
        LAS char* vb = L + posv * UNIT + voff;
#pragma unroll
        for (int d = 0; d < 8; ++d) {
          const v2u a = trread(vb + (d >> 1) * 64 + (d & 1) * 8), b = trread(vb + 512 + (d >> 1) * 64 + (d & 1) * 8);
          vf[d] = v4u{a[0], a[1], b[0], b[1]};
        }
        LDS_WAIT();
        ctl_wr(ctl + 4 * (C_REL + aw), uk + 2 * NATT);
        PIN8(vf);
      }
#pragma unroll
      for (int d = 0; d < 8; ++d) acc[d] = mfma(vf[d], pf, acc[d]);
      pos += 2 * NATT;
      if (pos >= NR) pos -= NR;
      if (n == OA_AT && c < 8) {
#pragma unroll
        for (int s = 0; s < 4; ++s) __builtin_amdgcn_raw_buffer_store_b128(oA[s], rsO, obase + s * 1024, 0, O_ST_AUX);
      }
      if ((n & 63) + NATT >= 64) {
        const bool mine = (c >> 3) == ph;
        if (!lastt) {
          LAS char* rec = L + L_MRG + (62 - (n & 63)) * MRG_REC;
          if (mine) {
#pragma unroll
            for (int d = 0; d < 8; ++d) *(LAS v4f*)(rec + d * 1024 + lane * 16) = acc[d];
            *(LAS v2f*)(rec + 8192 + lane * 8) = v2f{m, l};
          }
          LDS_WAIT();
          ctl_add(ctl + 4 * (C_MRG + ph));
        } else {
#pragma unroll
          for (int s = 0; s < 4; ++s) {
            const v4f x0 = acc[2 * s] * l, x1 = acc[2 * s + 1] * l;
            v4u o = {pk2(x0[0], x0[1]), pk2(x0[2], x0[3]), pk2(x1[0], x1[1]), pk2(x1[2], x1[3])};
#pragma unroll
            for (int k = 0; k < 4; ++k) o[k] = o[k] == SENT ? 0x7FFF7FFFu : o[k];
            if (ph == 0) oA[s] = o;
            else if (mine) __builtin_amdgcn_raw_buffer_store_b128(o, rsO, obase + s * 1024, 4 * 8 * 16384, O_ST_AUX);
          }
          STAMP(5 + ph);
        }
        m = -INFINITY; l = 0.f;
#pragma unroll
        for (int d = 0; d < 8; ++d) acc[d] = v4f{0.f, 0.f, 0.f, 0.f};
      }
    }
  } else {
    const int gw = wave - 2 - (wave >> 2);
    {
      const __amdgpu_buffer_rsrc_t rsQ = __builtin_amdgcn_make_buffer_rsrc((void*)wq, 0, ABL_NOQ ? 0 : DM * NH * DH * 2, 0x00020000);
      const int wvo = (8 * g * (NH * DH) + 8 * c) * 2, wso = (128 * gw * (NH * DH) + h * DH) * 2;
      const int xrow = ((c < 8 ? bp : bp + 8) * SQ + (c & 7));
      const GAS char* xb = (const GAS char*)x + ((size_t)xrow * DM + 128 * gw + 8 * g) * 2;
#define WQ_ROW(t, jj) ((32 * (((t) + (bp >> 1)) & 3) + ((jj) ^ (4 * (bp & 1)))) * (NH * DH * 2))
      v4u xf[4], w[4][8];
      __builtin_amdgcn_s_barrier();
      __builtin_amdgcn_sched_barrier(0);
#pragma unroll
      for (int t = 0; t < 3; ++t)
#pragma unroll
        for (int jj = 0; jj < 8; ++jj) {
          if (t == 0 && jj == 4) { vm_wait<0>(); __builtin_amdgcn_s_sleep(Q_GAP); }
          w[t][jj] = __builtin_amdgcn_raw_buffer_load_b128(rsQ, wvo, wso + WQ_ROW(t, jj), AUX_NT);
          if (t == 0 && jj == 3) {
#pragma unroll
            for (int tt = 0; tt < 4; ++tt) xf[tt] = X_NT ? __builtin_nontemporal_load((const GAS v4u*)(xb + ((tt + (bp >> 1)) & 3) * 64)) : *(const GAS v4u*)(xb + ((tt + (bp >> 1)) & 3) * 64);
          }
          __builtin_amdgcn_sched_barrier(0);
        }
      ctl_add(ctl + 4 * C_QI);
      if (bp & 1) {
#pragma unroll
        for (int t = 0; t < 4; ++t) xf[t] = v4u{xf[t][2], xf[t][3], xf[t][0], xf[t][1]};
      }
      v4f qa[8];
#pragma unroll
      for (int d = 0; d < 8; ++d) qa[d] = v4f{0.f, 0.f, 0.f, 0.f};
#pragma unroll
      for (int t = 0; t < 4; ++t) {
#pragma unroll
        for (int dd = 0; dd < 8; ++dd) {
          v4u f;
#pragma unroll
          for (int v = 0; v < 4; ++v) f[v] = __builtin_amdgcn_perm(w[t][2 * v + 1][dd >> 1], w[t][2 * v][dd >> 1], (dd & 1) ? 0x07060302u : 0x05040100u);
          qa[dd] = mfma(f, xf[t], qa[dd]);
          if (dd & 1) __builtin_amdgcn_sched_barrier(0);
        }
        if (t == 0) {
#pragma unroll
          for (int jj = 0; jj < 8; ++jj) {
            w[3][jj] = __builtin_amdgcn_raw_buffer_load_b128(rsQ, wvo, wso + WQ_ROW(3, jj), AUX_NT);
            __builtin_amdgcn_sched_barrier(0);
          }
        }
      }
      STAMP(16 + gw);
      LAS char* part = L + L_SCR + (gw & 3) * 8192 + lane * 16;
      if (gw >= 4) {
        ctl_wait<0>(ctl + 4 * C_Q1, 4);
        CFENCE();
      }
#pragma unroll
      for (int i = 0; i < 4; ++i) {
        v4f lo = {qa[0][i], qa[1][i], qa[2][i], qa[3][i]}, hi = {qa[4][i], qa[5][i], qa[6][i], qa[7][i]};
        if (gw >= 4) { lo += *(LAS v4f*)(part + i * 2048); hi += *(LAS v4f*)(part + i * 2048 + 1024); }
        *(LAS v4f*)(part + i * 2048) = lo;
        *(LAS v4f*)(part + i * 2048 + 1024) = hi;
      }
      LDS_WAIT();
      ctl_add(ctl + 4 * (gw >= 4 ? C_Q2 : C_Q1));
      if (gw == 0) STAMP(7);
      if (gw == 7) STAMP(8);
    }
    const int xc = j & 7, idx = j >> 3, ct = idx & 7, rg = idx >> 3;
    unsigned hb = 0;
#define HBAR(SLP) do { hb += NHELP; ctl_add(ctl + 4 * C_HB); ctl_wait<SLP>(ctl + 4 * C_HB, hb); CFENCE(); } while (0)
    const int pdst = W_P + (idx * 64 + gw * 8 + xc) * 1024;
    v4u wf[16];
    {
      const __amdgpu_buffer_rsrc_t rsW = __builtin_amdgcn_make_buffer_rsrc((void*)wo, 0, ABL_NOWO ? 0 : DM * NH * DH * 2, 0x00020000);
      ctl_wait<32>(ctl + 4 * C_FULL, 40 + 2 * gw);
#pragma unroll
      for (int ks = 0; ks < 16; ++ks) {
        wf[ks] = __builtin_amdgcn_raw_buffer_load_b128(rsW, g * 2048 + c * 16, (((ks >> 2) * 8 + xc) * 128 + (ks & 3) * 32 + 4 * gw) * 2048 + ct * 256, AUX_NT);
        __builtin_amdgcn_s_sleep(WF_SLEEP);
        __builtin_amdgcn_sched_barrier(0);
      }
      LAS char* sw = L + L_OB + gw * PIECE + lane * 16;
      LAS char* sr = L + L_OB + 2 * g * PIECE + (c >> 2) * 256 + gw * 32 + (c & 3) * 8;
#pragma unroll
      for (int r = 0; r < 8; ++r) {
        *(LAS v4u*)(sw) = wf[2 * r];
        *(LAS v4u*)(sw + 8 * PIECE) = wf[2 * r + 1];
        LDS_WAIT();
        HBAR(2);
        const v2u a0 = trread(sr), b0 = trread(sr + PIECE), a1 = trread(sr + 8 * PIECE), b1 = trread(sr + 9 * PIECE);
        wf[2 * r] = v4u{a0[0], a0[1], b0[0], b0[1]};
        wf[2 * r + 1] = v4u{a1[0], a1[1], b1[0], b1[1]};
        LDS_WAIT();
        asm volatile("" : "+v"(wf[2 * r]), "+v"(wf[2 * r + 1])::"memory");
        HBAR(2);
      }
    }
    const v2u tagv = *(LAS v2u*)(L + L_CTRL + 4 * C_EP);
    const unsigned long long tag = ((unsigned long long)tagv[1] << 32) | tagv[0];
    if (gw == 7) STAMP(9);
    ctl_wait<16>(ctl + 4 * C_FULL, NUNIT - G1_LEAD);
    if (gw == 7) STAMP(10);
    {
      const int src = lane < 8 ? idx * 8 + lane : (2 * rg + (lane & 1)) * 32 + (gw >> 1) * 8 + xc;
      GAS unsigned long long* fl = (GAS unsigned long long*)(ws + W_CF) + FSTR * src;
      for (unsigned sp = 0; sp < (1u << 16); ++sp) {
        if (__all(__hip_atomic_load(fl, __ATOMIC_RELAXED, __HIP_MEMORY_SCOPE_AGENT) == tag)) break;
        __builtin_amdgcn_s_sleep(32);
      }
      __builtin_amdgcn_fence(__ATOMIC_ACQUIRE, "wavefront");
    }
    if (gw == 7) STAMP(11);
#pragma unroll 1
    for (int ph = 0; ph < 2; ++ph) {
      v4u f0, f1;
      for (unsigned sp = 0; sp < (1u << 16); ++sp) {
        CFENCE();
        f0 = __builtin_amdgcn_raw_buffer_load_b128(rsO, lane * 16, W_O + ((ph * 4 + rg) * 8 + xc) * 16384 + gw * 2048, AUX_SC1);
        f1 = __builtin_amdgcn_raw_buffer_load_b128(rsO, lane * 16, W_O + ((ph * 4 + rg) * 8 + xc) * 16384 + gw * 2048 + 1024, AUX_SC1);
        bool ok = true;
#pragma unroll
        for (int k = 0; k < 4; ++k) ok &= (f0[k] != SENT) & (f1[k] != SENT);
        if (__all(ok)) break;
        __builtin_amdgcn_s_sleep(G1_SLEEP);
      }
      *(LAS v4u*)(L + L_OB + gw * 2048 + lane * 16) = f0;
      *(LAS v4u*)(L + L_OB + gw * 2048 + 1024 + lane * 16) = f1;
      LDS_WAIT();
      HBAR(0);
      v4f oa[4];
#pragma unroll
      for (int k = 0; k < 4; ++k) oa[k] = v4f{0.f, 0.f, 0.f, 0.f};
#pragma unroll
      for (int f = 0; f < 16; ++f) oa[f & 3] = mfma(wf[f], *(LAS v4u*)(L + L_OB + f * 1024 + lane * 16), oa[f & 3]);
      v4u pt = __builtin_bit_cast(v4u, (oa[0] + oa[1]) + (oa[2] + oa[3]));
#pragma unroll
      for (int k = 0; k < 4; ++k) pt[k] = pt[k] == SENT ? 0x7FFFFFFFu : pt[k];
      __builtin_amdgcn_raw_buffer_store_b128(pt, rsO, lane * 16, pdst + ph * (2 << 20), P_ST_AUX);
      if (gw == 7) STAMP(24 + ph);
      if (ph == 0) HBAR(0);
    }
    if (gw < 2) {
      v4u q[8];
      for (unsigned sp = 0; sp < (1u << 16); ++sp) {
        CFENCE();
        bool ok = true;
#pragma unroll
        for (int k = 0; k < 8; ++k) q[k] = __builtin_amdgcn_raw_buffer_load_b128(rsO, lane * 16, W_P + gw * (2 << 20) + (idx * 64 + xc * 8 + k) * 1024, AUX_SC1);
#pragma unroll
        for (int k = 0; k < 8; ++k) ok &= (q[k][0] != SENT) & (q[k][1] != SENT) & (q[k][2] != SENT) & (q[k][3] != SENT);
        if (__all(ok)) break;
        __builtin_amdgcn_s_sleep(1);
      }
      v4f s = __builtin_bit_cast(v4f, q[0]);
#pragma unroll
      for (int k = 1; k < 8; ++k) s += __builtin_bit_cast(v4f, q[k]);
      *(GAS v2u*)((GAS char*)out + ((size_t)(64 * gw + 16 * rg + c) * DM + 128 * ct + 16 * xc + 4 * g) * 2) = v2u{pk2(s[0], s[1]), pk2(s[2], s[3])};
      STAMP(12 + gw);
    } else if (gw == 2 && j == 0) {
      GAS unsigned long long* fl = (GAS unsigned long long*)(ws + W_CF) + FSTR * lane;
      for (unsigned sp = 0; sp < (1u << 16); ++sp) {
        bool ok = true;
#pragma unroll
        for (int k = 0; k < 4; ++k) ok &= __hip_atomic_load(fl + FSTR * 64 * k, __ATOMIC_RELAXED, __HIP_MEMORY_SCOPE_AGENT) == tag;
        if (__all(ok)) break;
        __builtin_amdgcn_s_sleep(32);
      }
      if (lane == 0) __hip_atomic_store((GAS unsigned long long*)(ws + W_EPOCH), tag, __ATOMIC_RELAXED, __HIP_MEMORY_SCOPE_AGENT);
    }
  }
}
}

extern "C" void kernel_launch(void* const* d_in, const int* in_sizes, int n_in, void* d_out, int out_size, void* d_ws, size_t ws_size,
                              hipStream_t stream) {
  fprintf(stderr, "PTRS x %p k %p v %p wq %p wo %p out %p ws %p\n", d_in[0], d_in[1], d_in[2], d_in[3], d_in[4], d_out, d_ws);
  (void)hipFuncSetAttribute((const void*)mha_block_kernel, hipFuncAttributeMaxDynamicSharedMemorySize, LDS_BYTES);
  mha_block_kernel<<<NWG, NTHR, LDS_BYTES, stream>>>((const unsigned short*)d_in[0], (const unsigned short*)d_in[1], (const unsigned short*)d_in[2],
                                                     (const unsigned short*)d_in[3], (const unsigned short*)d_in[4], (unsigned short*)d_out,
                                                     (unsigned char*)d_ws);
}
```

```cpp
#define STAMPS 1
#include <stdio.h>
#include <hip/hip_runtime.h>
#include <hip/hip_bf16.h>
#include <stdint.h>

#define GAS __attribute__((address_space(1)))
#define LAS __attribute__((address_space(3)))
typedef unsigned v4u __attribute__((ext_vector_type(4)));
typedef unsigned v2u __attribute__((ext_vector_type(2)));
typedef float v4f __attribute__((ext_vector_type(4)));
typedef float v2f __attribute__((ext_vector_type(2)));
typedef short v4s __attribute__((ext_vector_type(4)));
typedef __bf16 v8bf __attribute__((ext_vector_type(8)));
typedef __bf16 v2bf __attribute__((ext_vector_type(2)));

#define WIN 20
#define WIN0 24
#define N0 12
#define WINE 40
#define NE 4
#define NI 2
#define QI_GO 5
#define HQ_WIN 40
#define WF_SLEEP 16
#define G1_LEAD 4
#define G1_SLEEP 4
#define ABL_NOST 0
#define ABL_NOG0 0
#define Q_GAP 4
#define ABL_NOQ 0
#define X_NT 0
#define P_ST_AUX 16
#define G0_AT 160
#define ABL_NOWO 0
#define ABL_NOCLR 0
#define OA_AT 126
#define O_ST_AUX 16

namespace {
constexpr int NB = 16, SQ = 8, DM = 1024, NH = 32, SK = 2048, DH = 128;
constexpr int NWG = 256, NTHR = 768, NATT = 3, NHELP = 8;
constexpr int PIECE = 1056;
constexpr int UNIT = 8 * PIECE;
constexpr int NR = 15;
constexpr int NUNIT = 256;
constexpr int AUX_NT = 2, AUX_SC1 = 16;
constexpr int L_CTRL = NR * UNIT;
constexpr int L_SCR = L_CTRL + 256;
constexpr int L_MRG = L_SCR;
constexpr int MRG_REC = 8192 + 512;
constexpr int L_OB = L_SCR + 2 * MRG_REC;
constexpr int LDS_BYTES = 160 * 1024;
static_assert(L_SCR + 32768 <= LDS_BYTES && L_OB + 16 * PIECE <= LDS_BYTES, "LDS map");
enum { C_FULL = 0, C_REL = 4, C_Q1 = 8, C_Q2 = 9, C_QI = 10, C_HB = 11, C_MRG = 12, C_EP = 14, C_CLR = 16 };
constexpr int W_EPOCH = 0, W_CF = 65536, FSTR = 16;
constexpr int W_O = 1 << 20;
constexpr int W_P = 2 << 20;
constexpr int WS_USED = 6 << 20;
constexpr unsigned SENT = 0xFFFFFFFFu;
constexpr float CSC = 0.08838834764831845f * 1.4426950408889634f;
constexpr unsigned SPIN_CAP = 1u << 22;

__device__ __forceinline__ unsigned rfl(unsigned x) { return __builtin_amdgcn_readfirstlane(x); }
__device__ __forceinline__ v4f mfma(v4u a, v4u b, v4f c) {
  return __builtin_amdgcn_mfma_f32_16x16x32_bf16(__builtin_bit_cast(v8bf, a), __builtin_bit_cast(v8bf, b), c, 0, 0, 0);
}
__device__ __forceinline__ unsigned pk2(float lo, float hi) {
  v2f x = {lo, hi};
  return __builtin_bit_cast(unsigned, __builtin_convertvector(x, v2bf));
}
__device__ __forceinline__ v2u trread(LAS char* p) {
  return __builtin_bit_cast(v2u, __builtin_amdgcn_ds_read_tr16_b64_v4i16((LAS v4s*)p));
}
__device__ __forceinline__ float xg_max(float x) {
  auto s = __builtin_amdgcn_permlane16_swap(__float_as_uint(x), __float_as_uint(x), false, false);
  x = fmaxf(__uint_as_float(s[0]), __uint_as_float(s[1]));
  auto t = __builtin_amdgcn_permlane32_swap(__float_as_uint(x), __float_as_uint(x), false, false);
  return fmaxf(__uint_as_float(t[0]), __uint_as_float(t[1]));
}
__device__ __forceinline__ float xg_sum(float x) {
  auto s = __builtin_amdgcn_permlane16_swap(__float_as_uint(x), __float_as_uint(x), false, false);
  x = __uint_as_float(s[0]) + __uint_as_float(s[1]);
  auto t = __builtin_amdgcn_permlane32_swap(__float_as_uint(x), __float_as_uint(x), false, false);
  return __uint_as_float(t[0]) + __uint_as_float(t[1]);
}
#define STAMP(k) do { if (lane == 0) ((GAS unsigned long long*)(ws + (16u << 20)))[blockIdx.x * 64 + (k)] = __builtin_amdgcn_s_memrealtime(); } while (0)
#define STAMPV(k, v) do { if (lane == 0) ((GAS unsigned long long*)(ws + (16u << 20)))[blockIdx.x * 64 + (k)] = (v); } while (0)
#define LDS_WAIT() asm volatile("s_waitcnt lgkmcnt(0)" ::: "memory")
#define CFENCE() asm volatile("" ::: "memory")
#define PIN8(a) asm volatile("" : "+v"(a[0]), "+v"(a[1]), "+v"(a[2]), "+v"(a[3]), "+v"(a[4]), "+v"(a[5]), "+v"(a[6]), "+v"(a[7])::"memory")
template <int N> __device__ __forceinline__ void vm_wait() { asm volatile("s_waitcnt vmcnt(%c0)" ::"n"(N) : "memory"); }
__device__ __forceinline__ unsigned ctl_rd(unsigned addr) {
  unsigned v;
  asm volatile("ds_read_b32 %0, %1\n\ts_waitcnt lgkmcnt(0)" : "=v"(v) : "v"(addr) : "memory");
  return rfl(v);
}
__device__ __forceinline__ void ctl_wr(unsigned addr, unsigned val) {
  asm volatile("s_mov_b64 exec, 1\n\tds_write_b32 %0, %1\n\ts_mov_b64 exec, -1" ::"v"(addr), "v"(val) : "memory");
}
__device__ __forceinline__ unsigned ctl_add(unsigned addr) {
  unsigned v;
  asm volatile("s_mov_b64 exec, 1\n\tds_add_rtn_u32 %0, %1, %2\n\ts_mov_b64 exec, -1\n\ts_waitcnt lgkmcnt(0)" : "=&v"(v) : "v"(addr), "v"(1u) : "memory");
  return rfl(v);
}
template <int SLP> __device__ __forceinline__ unsigned ctl_wait(unsigned addr, unsigned need) {
  unsigned v = ctl_rd(addr);
  for (unsigned sp = 0; v < need && sp < SPIN_CAP; ++sp) {
    __builtin_amdgcn_s_sleep(SLP);
    v = ctl_rd(addr);
  }
  return v;
}

#define PUBL(k) ".if %c[b] == " #k "\n\ts_mov_b64 exec, 1\n\tds_write_b32 %[fa], v23\n\ts_mov_b64 exec, -1\n\t.endif\n\t"
#define PC(k, VO, IMM) "s_waitcnt vmcnt(%c[w])\n\t" PUBL(k) ".if %c[nt]\n\tbuffer_load_dwordx4 " VO ", %[rs], %[so] offen offset:" IMM " nt lds\n\t.else\n\tbuffer_load_dwordx4 " VO ", %[rs], %[so] offen offset:" IMM " lds\n\t.endif\n\t"
template <int W, int NTF = 1>
__device__ __forceinline__ void dma_unit(__amdgpu_buffer_rsrc_t rs, unsigned soff, unsigned& m0b, unsigned& u, unsigned& lim, unsigned vo0, unsigned vo1,
                                         unsigned vctl, unsigned faddr) {
  unsigned sp;
  asm volatile(
      "s_cmp_lt_u32 %[u], %[lim]\n\t"
      "s_cbranch_scc1 2f\n\t"
      "s_mov_b32 %[sp], 0\n"
      "1:\n\t"
      "ds_read_b32 v20, %[vc] offset:16\n\tds_read_b32 v21, %[vc] offset:20\n\tds_read_b32 v22, %[vc] offset:24\n\t"
      "s_waitcnt lgkmcnt(0)\n\t"
      "v_min3_u32 v20, v20, v21, v22\n\t"
      "s_add_u32 %[sp], %[sp], 1\n\t"
      "v_readfirstlane_b32 %[lim], v20\n\t"
      "s_add_u32 %[lim], %[lim], %c[nr]\n\t"
      "s_cmp_lt_u32 %[u], %[lim]\n\t"
      "s_cbranch_scc1 2f\n\t"
      "s_sleep 1\n\t"
      "s_cmp_lt_u32 %[sp], %c[cap]\n\t"
      "s_cbranch_scc1 1b\n"
      "2:\n\t"
      "s_mov_b32 m0, %[m0b]\n\t"
      "v_mov_b32 v23, %[u]\n\t"
      "v_subrev_u32 v23, %c[a], v23\n\t"
      PC(0, "%[vo0]", "0")
      "ds_read_b32 v20, %[vc] offset:16\n\tds_read_b32 v21, %[vc] offset:20\n\tds_read_b32 v22, %[vc] offset:24\n\t"
      "s_add_u32 m0, m0, 32\n\t"
      PC(1, "%[vo0]", "1024")
      "s_add_u32 m0, m0, 32\n\t"
      PC(2, "%[vo0]", "2048")
      "s_add_u32 m0, m0, 32\n\t"
      PC(3, "%[vo0]", "3072")
      "s_add_u32 m0, m0, 0x1020\n\t"
      PC(4, "%[vo1]", "0")
      "s_waitcnt lgkmcnt(0)\n\t"
      "v_min3_u32 v20, v20, v21, v22\n\t"
      "s_add_u32 m0, m0, 32\n\t"
      PC(5, "%[vo1]", "1024")
      "v_readfirstlane_b32 %[lim], v20\n\t"
      "s_add_u32 %[m0b], %[m0b], %c[unit]\n\t"
      "s_add_u32 m0, m0, 32\n\t"
      PC(6, "%[vo1]", "2048")
      "s_add_u32 %[lim], %[lim], %c[nr]\n\t"
      "s_cmp_lt_u32 %[m0b], %c[ring]\n\t"
      "s_cselect_b32 %[m0b], %[m0b], 0\n\t"
      "s_add_u32 %[u], %[u], 1\n\t"
      "s_add_u32 m0, m0, 32\n\t"
      PC(7, "%[vo1]", "3072")
      : [m0b] "+s"(m0b), [u] "+s"(u), [lim] "+s"(lim), [sp] "=&s"(sp)
      : [nt] "n"(NTF), [w] "n"(W - 1), [a] "n"((W - 1) / 8), [b] "n"((W - 1) % 8), [nr] "n"(NR), [cap] "n"(SPIN_CAP), [unit] "n"(UNIT), [ring] "n"(NR * UNIT), [rs] "s"(rs),
        [so] "s"(soff), [vo0] "v"(vo0), [vo1] "v"(vo1), [vc] "v"(vctl), [fa] "v"(faddr)
      : "memory", "scc", "v20", "v21", "v22", "v23");
}

__global__ void __launch_bounds__(NTHR, 3)
mha_block_kernel(const unsigned short* __restrict__ x, const unsigned short* __restrict__ kc, const unsigned short* __restrict__ vc,
                 const unsigned short* __restrict__ wq, const unsigned short* __restrict__ wo, unsigned short* __restrict__ out,
                 unsigned char* __restrict__ ws) {
  extern __shared__ __attribute__((aligned(16))) char smem[];
  LAS char* L = (LAS char*)smem;
  const unsigned lbase = (unsigned)(size_t)L;
  const int lane = threadIdx.x & 63, wave = rfl(threadIdx.x >> 6);
  const int g = lane >> 4, c = lane & 15;
  const int j = blockIdx.x, h = j & 31, bp = j >> 5;
  const unsigned ctl = lbase + L_CTRL;
  const __amdgpu_buffer_rsrc_t rsO = __builtin_amdgcn_make_buffer_rsrc((void*)ws, 0, WS_USED, 0x00020000);

  if (wave == 0) {
    __builtin_amdgcn_s_setprio(3);
    __builtin_amdgcn_s_waitcnt(0x0F70);
    unsigned tv = 0, nstall = 0;
    const unsigned long long tl0 = __builtin_amdgcn_s_memrealtime();
    static_assert(WIN <= 24 && WIN > 16 && WIN0 >= WIN && WIN0 <= 40, "publish ladder below; published counts never decrease");
    const unsigned vo0 = lane * 16, vo1 = lane * 16 + 4096;
    const unsigned sbase = (unsigned)(bp * NH + h) * (SK * DH * 2);
    const __amdgpu_buffer_rsrc_t rsK = __builtin_amdgcn_make_buffer_rsrc((void*)kc, 0, NB * NH * SK * DH * 2, 0x00020000);
    const __amdgpu_buffer_rsrc_t rsV = __builtin_amdgcn_make_buffer_rsrc((void*)vc, 0, NB * NH * SK * DH * 2, 0x00020000);
    unsigned m0b = 0, u = 0, lim = NR;
#pragma unroll 1
    for (int n = 0; n < NI; ++n) {
      dma_unit<48>(rsK, sbase + (unsigned)n * 8192u, m0b, u, lim, vo0, vo1, ctl, ctl + 252);
      lim = NR;
      dma_unit<48>(rsV, sbase + (unsigned)n * 8192u, m0b, u, lim, vo0, vo1, ctl, ctl + 252);
      lim = NR;
      if (n == 0) __builtin_amdgcn_s_barrier();
    }
    vm_wait<0>(); ctl_wr(ctl + 4 * C_FULL, 2 * NI);
    ctl_wait<1>(ctl + 4 * C_QI, QI_GO);
#pragma unroll 1
    for (int n = NI; n < N0; ++n) {
      { const unsigned tn = (unsigned)(__builtin_amdgcn_s_memrealtime() - tl0); tv = lane == 32 + n ? tn : tv; }
      dma_unit<WIN0>(rsK, sbase + (unsigned)n * 8192u, m0b, u, lim, vo0, vo1, ctl, 2 * (n - NI) < (WIN0 - 1) / 8 ? ctl + 252 : ctl + 4 * C_FULL);
      dma_unit<WIN0>(rsV, sbase + (unsigned)n * 8192u, m0b, u, lim, vo0, vo1, ctl, 2 * (n - NI) + 1 < (WIN0 - 1) / 8 ? ctl + 252 : ctl + 4 * C_FULL);
    }
#pragma unroll 1
    for (int n = N0; n < 128 - NE; ++n) {
      if ((n & 3) == 0) { const unsigned tn = (unsigned)(__builtin_amdgcn_s_memrealtime() - tl0); tv = lane == (n >> 2) ? tn : tv; }
      const unsigned soff = sbase + (unsigned)(n >> 6) * (8u * NH * SK * DH * 2) + (unsigned)(n & 63) * 8192u;
      dma_unit<WIN>(rsK, soff, m0b, u, lim, vo0, vo1, ctl, ctl + 4 * C_FULL);
      dma_unit<WIN>(rsV, soff, m0b, u, lim, vo0, vo1, ctl, ctl + 4 * C_FULL);
    }
#pragma unroll 1
    for (int n = 128 - NE; n < 128; ++n) {
      const unsigned soff = sbase + (8u * NH * SK * DH * 2) + (unsigned)(n & 63) * 8192u;
      const int k = 2 * (n - (128 - NE));
      dma_unit<WINE>(rsK, soff, m0b, u, lim, vo0, vo1, ctl, k < (WINE - 1) / 8 - (WIN - 1) / 8 ? ctl + 252 : ctl + 4 * C_FULL);
      dma_unit<WINE>(rsV, soff, m0b, u, lim, vo0, vo1, ctl, k + 1 < (WINE - 1) / 8 - (WIN - 1) / 8 ? ctl + 252 : ctl + 4 * C_FULL);
    }
    if (WINE > 48) { vm_wait<48>(); ctl_wr(ctl + 4 * C_FULL, NUNIT - 6); }
    if (WINE > 40) { vm_wait<40>(); ctl_wr(ctl + 4 * C_FULL, NUNIT - 5); }
    if (WINE > 32) { vm_wait<32>(); ctl_wr(ctl + 4 * C_FULL, NUNIT - 4); }
    if (WINE > 24) { vm_wait<24>(); ctl_wr(ctl + 4 * C_FULL, NUNIT - 3); }
    vm_wait<16>(); ctl_wr(ctl + 4 * C_FULL, NUNIT - 2);
    vm_wait<8>(); ctl_wr(ctl + 4 * C_FULL, NUNIT - 1);
    vm_wait<0>(); ctl_wr(ctl + 4 * C_FULL, NUNIT);
    STAMPV(0, tl0); STAMP(1); STAMPV(2, nstall);
    ((GAS unsigned long long*)(ws + (17u << 20)))[blockIdx.x * 64 + lane] = tv;
    { unsigned xcc; asm volatile("s_getreg_b32 %0, hwreg(HW_REG_XCC_ID)" : "=s"(xcc)); STAMPV(3, xcc & 15); }
  } else if ((wave & 3) == 0 || wave == 1) {
    const int aw = wave == 1 ? 2 : (wave >> 2) - 1;
    if (aw == 0) { *(LAS unsigned*)(L + L_CTRL + 4 * lane) = 0u; LDS_WAIT(); }
    __builtin_amdgcn_s_barrier();
    const unsigned koff = (c & 7) * PIECE + (c >> 3) * 256 + g * 16;
    const unsigned voff = (4 * (g & 1) + (c >> 2)) * PIECE + (g >> 1) * 256 + (c & 3) * 16;
    const int obase = W_O + ((bp >> 1) * 8 + (j & 7)) * 16384 + (h >> 3) * 4096 + g * 256 + ((bp & 1) * 8 + (c & 7)) * 16;
    {
      const unsigned long long tag = __hip_atomic_load((GAS unsigned long long*)(ws + W_EPOCH), __ATOMIC_RELAXED, __HIP_MEMORY_SCOPE_AGENT) + 1ull;
      const v4u sv = {SENT, SENT, SENT, SENT};
      if (aw == 0) {
#pragma unroll
        for (int s = 0; s < 4; ++s) __builtin_amdgcn_raw_buffer_store_b128(sv, rsO, obase + (c >> 3) * (4 * 8 * 16384), s * 1024, O_ST_AUX);
        if (lane == 0) *(LAS v2u*)(L + L_CTRL + 4 * C_EP) = v2u{(unsigned)tag, (unsigned)(tag >> 32)};
      } else {
#pragma unroll
        for (int k = 0; k < 8; ++k) __builtin_amdgcn_raw_buffer_store_b128(sv, rsO, lane * 16, W_P + (aw - 1) * (2 << 20) + ((j >> 3) * 64 + k * 8 + (j & 7)) * 1024, P_ST_AUX);
      }
      vm_wait<0>();
      if (ctl_add(ctl + 4 * C_CLR) == NATT - 1 && lane == 0) __hip_atomic_store((GAS unsigned long long*)(ws + W_CF) + FSTR * j, tag, __ATOMIC_RELAXED, __HIP_MEMORY_SCOPE_AGENT);
    }
    v4u oA[4];
#pragma unroll
    for (int s = 0; s < 4; ++s) oA[s] = v4u{0u, 0u, 0u, 0u};
    v4u kf[8], vf[8];
    unsigned full = ctl_wait<1>(ctl + 4 * C_FULL, 2 * aw + 2);
    {
      CFENCE();
      LAS char* kb = L + (2 * aw) * UNIT + koff;
      LAS char* vb = L + (2 * aw + 1) * UNIT + voff;
#pragma unroll
      for (int i = 0; i < 8; ++i) kf[i] = *(LAS v4u*)(kb + (i >> 2) * 512 + (i & 3) * 64);
#pragma unroll
      for (int d = 0; d < 8; ++d) {
        const v2u a = trread(vb + (d >> 1) * 64 + (d & 1) * 8), b = trread(vb + 512 + (d >> 1) * 64 + (d & 1) * 8);
        vf[d] = v4u{a[0], a[1], b[0], b[1]};
      }
      LDS_WAIT();
      ctl_wr(ctl + 4 * (C_REL + aw), 2 * aw + 2 * NATT);
      PIN8(kf); PIN8(vf);
      if (aw == 0) STAMP(14);
      ctl_wait<1>(ctl + 4 * C_Q2, 4);
      CFENCE();
    }
    v4u qf[4];
#pragma unroll
    for (int s = 0; s < 4; ++s) {
      LAS char* qp = L + L_SCR + g * 2048 + (s * 16 + c) * 16;
      const v4f lo = (*(LAS v4f*)(qp) + *(LAS v4f*)(qp + 8192)) + (*(LAS v4f*)(qp + 16384) + *(LAS v4f*)(qp + 24576));
      const v4f hi = (*(LAS v4f*)(qp + 1024) + *(LAS v4f*)(qp + 9216)) + (*(LAS v4f*)(qp + 17408) + *(LAS v4f*)(qp + 25600));
      qf[s] = v4u{pk2(lo[0], lo[1]), pk2(lo[2], lo[3]), pk2(hi[0], hi[1]), pk2(hi[2], hi[3])};
      if (s & 1) asm volatile("" : "+v"(qf[s - 1]), "+v"(qf[s])::"memory");
    }
    if (aw == 0) STAMP(4);
    float m = -INFINITY, l = 0.f;
    v4f acc[8];
#pragma unroll
    for (int d = 0; d < 8; ++d) acc[d] = v4f{0.f, 0.f, 0.f, 0.f};
    unsigned pos = 2 * aw;
#pragma unroll 1
    for (int n = aw; n < 128; n += NATT) {
      const unsigned uk = 2 * n;
      const unsigned posv = pos + 1 == NR ? 0 : pos + 1;
      if (n >= NATT) {
        if (full < uk + 1) full = ctl_wait<1>(ctl + 4 * C_FULL, uk + 1);
        CFENCE();
        LAS char* kb = L + pos * UNIT + koff;
#pragma unroll
        for (int i = 0; i < 8; ++i) kf[i] = *(LAS v4u*)(kb + (i >> 2) * 512 + (i & 3) * 64);
        LDS_WAIT();
        ctl_wr(ctl + 4 * (C_REL + aw), uk + 1);
        PIN8(kf);
      }
      v4f s0 = {0.f, 0.f, 0.f, 0.f}, s1 = {0.f, 0.f, 0.f, 0.f};
#pragma unroll
      for (int s = 0; s < 4; ++s) { s0 = mfma(kf[s], qf[s], s0); s1 = mfma(kf[4 + s], qf[s], s1); }
      float mx = fmaxf(fmaxf(fmaxf(s0[0], s0[1]), fmaxf(s0[2], s0[3])), fmaxf(fmaxf(s1[0], s1[1]), fmaxf(s1[2], s1[3])));
      mx = xg_max(mx);
      const bool lastt = (n & 63) == 63;
      const int ph = n >> 6;
      LAS char* r0 = L + L_MRG;
      LAS char* r1 = L + L_MRG + MRG_REC;
      v2f a0 = {-INFINITY, 0.f}, a1 = {-INFINITY, 0.f};
      if (lastt) {
        ctl_wait<0>(ctl + 4 * (C_MRG + ph), 2);
        CFENCE();
        a0 = *(LAS v2f*)(r0 + 8192 + lane * 8);
        a1 = *(LAS v2f*)(r1 + 8192 + lane * 8);
        mx = fmaxf(mx, fmaxf(a0[0], a1[0]));
      }
      const float mn = fmaxf(m, mx);
      const float alpha = __builtin_amdgcn_exp2f((m - mn) * CSC);
      const float mc = mn * CSC;
      m = mn;
      float p[8];
#pragma unroll
      for (int i = 0; i < 4; ++i) { p[i] = __builtin_amdgcn_exp2f(fmaf(s0[i], CSC, -mc)); p[4 + i] = __builtin_amdgcn_exp2f(fmaf(s1[i], CSC, -mc)); }
      l = fmaf(l, alpha, ((p[0] + p[1]) + (p[2] + p[3])) + ((p[4] + p[5]) + (p[6] + p[7])));
      v4u pf = {pk2(p[0], p[1]), pk2(p[2], p[3]), pk2(p[4], p[5]), pk2(p[6], p[7])};
#pragma unroll
      for (int d = 0; d < 8; ++d) acc[d] *= alpha;
      if (lastt) {
        const float w0 = __builtin_amdgcn_exp2f((a0[0] - mn) * CSC), w1 = __builtin_amdgcn_exp2f((a1[0] - mn) * CSC);
        l = 1.0f / xg_sum(fmaf(w0, a0[1], fmaf(w1, a1[1], l)));
#pragma unroll
        for (int d = 0; d < 8; ++d) acc[d] += *(LAS v4f*)(r0 + d * 1024 + lane * 16) * w0 + *(LAS v4f*)(r1 + d * 1024 + lane * 16) * w1;
      }
      asm volatile("" : "+v"(pf), "+v"(l));
      PIN8(acc);
      if (n >= NATT) {
        if (full < uk + 2) full = ctl_wait<0>(ctl + 4 * C_FULL, uk + 2);
        CFENCE();
        LAS char* vb = L + posv * UNIT + voff;
#pragma unroll
        for (int d = 0; d < 8; ++d) {
          const v2u a = trread(vb + (d >> 1) * 64 + (d & 1) * 8), b = trread(vb + 512 + (d >> 1) * 64 + (d & 1) * 8);
          vf[d] = v4u{a[0], a[1], b[0], b[1]};
        }
        LDS_WAIT();
        ctl_wr(ctl + 4 * (C_REL + aw), uk + 2 * NATT);
        PIN8(vf);
      }
#pragma unroll
      for (int d = 0; d < 8; ++d) acc[d] = mfma(vf[d], pf, acc[d]);
      pos += 2 * NATT;
      if (pos >= NR) pos -= NR;
      if (n == OA_AT && c < 8) {
#pragma unroll
        for (int s = 0; s < 4; ++s) __builtin_amdgcn_raw_buffer_store_b128(oA[s], rsO, obase + s * 1024, 0, O_ST_AUX);
      }
      if ((n & 63) + NATT >= 64) {
        const bool mine = (c >> 3) == ph;
        if (!lastt) {
          LAS char* rec = L + L_MRG + (62 - (n & 63)) * MRG_REC;
          if (mine) {
#pragma unroll
            for (int d = 0; d < 8; ++d) *(LAS v4f*)(rec + d * 1024 + lane * 16) = acc[d];
            *(LAS v2f*)(rec + 8192 + lane * 8) = v2f{m, l};
          }
          LDS_WAIT();
          ctl_add(ctl + 4 * (C_MRG + ph));
        } else {
#pragma unroll
          for (int s = 0; s < 4; ++s) {
            const v4f x0 = acc[2 * s] * l, x1 = acc[2 * s + 1] * l;
            v4u o = {pk2(x0[0], x0[1]), pk2(x0[2], x0[3]), pk2(x1[0], x1[1]), pk2(x1[2], x1[3])};
#pragma unroll
            for (int k = 0; k < 4; ++k) o[k] = o[k] == SENT ? 0x7FFF7FFFu : o[k];
            if (ph == 0) oA[s] = o;
            else if (mine) __builtin_amdgcn_raw_buffer_store_b128(o, rsO, obase + s * 1024, 4 * 8 * 16384, O_ST_AUX);
          }
          STAMP(5 + ph);
        }
        m = -INFINITY; l = 0.f;
#pragma unroll
        for (int d = 0; d < 8; ++d) acc[d] = v4f{0.f, 0.f, 0.f, 0.f};
      }
    }
  } else {
    const int gw = wave - 2 - (wave >> 2);
    {
      const __amdgpu_buffer_rsrc_t rsQ = __builtin_amdgcn_make_buffer_rsrc((void*)wq, 0, ABL_NOQ ? 0 : DM * NH * DH * 2, 0x00020000);
      const int wvo = (8 * g * (NH * DH) + 8 * c) * 2, wso = (128 * gw * (NH * DH) + h * DH) * 2;
      const int xrow = ((c < 8 ? bp : bp + 8) * SQ + (c & 7));
      const GAS char* xb = (const GAS char*)x + ((size_t)xrow * DM + 128 * gw + 8 * g) * 2;
#define WQ_ROW(t, jj) ((32 * (((t) + (bp >> 1)) & 3) + ((jj) ^ (4 * (bp & 1)))) * (NH * DH * 2))
      v4u xf[4], w[4][8];
      __builtin_amdgcn_s_barrier();
      __builtin_amdgcn_sched_barrier(0);
#pragma unroll
      for (int t = 0; t < 3; ++t)
#pragma unroll
        for (int jj = 0; jj < 8; ++jj) {
          if (t == 0 && jj == 4) { vm_wait<0>(); __builtin_amdgcn_s_sleep(Q_GAP); }
          w[t][jj] = __builtin_amdgcn_raw_buffer_load_b128(rsQ, wvo, wso + WQ_ROW(t, jj), AUX_NT);
          if (t == 0 && jj == 3) {
#pragma unroll
            for (int tt = 0; tt < 4; ++tt) xf[tt] = X_NT ? __builtin_nontemporal_load((const GAS v4u*)(xb + ((tt + (bp >> 1)) & 3) * 64)) : *(const GAS v4u*)(xb + ((tt + (bp >> 1)) & 3) * 64);
          }
          __builtin_amdgcn_sched_barrier(0);
        }
      ctl_add(ctl + 4 * C_QI);
      if (bp & 1) {
#pragma unroll
        for (int t = 0; t < 4; ++t) xf[t] = v4u{xf[t][2], xf[t][3], xf[t][0], xf[t][1]};
      }
      v4f qa[8];
#pragma unroll
      for (int d = 0; d < 8; ++d) qa[d] = v4f{0.f, 0.f, 0.f, 0.f};
#pragma unroll
      for (int t = 0; t < 4; ++t) {
#pragma unroll
        for (int dd = 0; dd < 8; ++dd) {
          v4u f;
#pragma unroll
          for (int v = 0; v < 4; ++v) f[v] = __builtin_amdgcn_perm(w[t][2 * v + 1][dd >> 1], w[t][2 * v][dd >> 1], (dd & 1) ? 0x07060302u : 0x05040100u);
          qa[dd] = mfma(f, xf[t], qa[dd]);
          if (dd & 1) __builtin_amdgcn_sched_barrier(0);
        }
        if (t == 0) {
#pragma unroll
          for (int jj = 0; jj < 8; ++jj) {
            w[3][jj] = __builtin_amdgcn_raw_buffer_load_b128(rsQ, wvo, wso + WQ_ROW(3, jj), AUX_NT);
            __builtin_amdgcn_sched_barrier(0);
          }
        }
      }
      STAMP(16 + gw);
      LAS char* part = L + L_SCR + (gw & 3) * 8192 + lane * 16;
      if (gw >= 4) {
        ctl_wait<0>(ctl + 4 * C_Q1, 4);
        CFENCE();
      }
#pragma unroll
      for (int i = 0; i < 4; ++i) {
        v4f lo = {qa[0][i], qa[1][i], qa[2][i], qa[3][i]}, hi = {qa[4][i], qa[5][i], qa[6][i], qa[7][i]};
        if (gw >= 4) { lo += *(LAS v4f*)(part + i * 2048); hi += *(LAS v4f*)(part + i * 2048 + 1024); }
        *(LAS v4f*)(part + i * 2048) = lo;
        *(LAS v4f*)(part + i * 2048 + 1024) = hi;
      }
      LDS_WAIT();
      ctl_add(ctl + 4 * (gw >= 4 ? C_Q2 : C_Q1));
      if (gw == 0) STAMP(7);
      if (gw == 7) STAMP(8);
    }
    const int xc = j & 7, idx = j >> 3, ct = idx & 7, rg = idx >> 3;
    unsigned hb = 0;
#define HBAR(SLP) do { hb += NHELP; ctl_add(ctl + 4 * C_HB); ctl_wait<SLP>(ctl + 4 * C_HB, hb); CFENCE(); } while (0)
    const int pdst = W_P + (idx * 64 + gw * 8 + xc) * 1024;
    v4u wf[16];
    {
      const __amdgpu_buffer_rsrc_t rsW = __builtin_amdgcn_make_buffer_rsrc((void*)wo, 0, ABL_NOWO ? 0 : DM * NH * DH * 2, 0x00020000);
      ctl_wait<32>(ctl + 4 * C_FULL, 40 + 2 * gw);
#pragma unroll
      for (int ks = 0; ks < 16; ++ks) {
        wf[ks] = __builtin_amdgcn_raw_buffer_load_b128(rsW, g * 2048 + c * 16, (((ks >> 2) * 8 + xc) * 128 + (ks & 3) * 32 + 4 * gw) * 2048 + ct * 256, AUX_NT);
        __builtin_amdgcn_s_sleep(WF_SLEEP);
        __builtin_amdgcn_sched_barrier(0);
      }
      LAS char* sw = L + L_OB + gw * PIECE + lane * 16;
      LAS char* sr = L + L_OB + 2 * g * PIECE + (c >> 2) * 256 + gw * 32 + (c & 3) * 8;
#pragma unroll
      for (int r = 0; r < 8; ++r) {
        *(LAS v4u*)(sw) = wf[2 * r];
        *(LAS v4u*)(sw + 8 * PIECE) = wf[2 * r + 1];
        LDS_WAIT();
        HBAR(2);
        const v2u a0 = trread(sr), b0 = trread(sr + PIECE), a1 = trread(sr + 8 * PIECE), b1 = trread(sr + 9 * PIECE);
        wf[2 * r] = v4u{a0[0], a0[1], b0[0], b0[1]};
        wf[2 * r + 1] = v4u{a1[0], a1[1], b1[0], b1[1]};
        LDS_WAIT();
        asm volatile("" : "+v"(wf[2 * r]), "+v"(wf[2 * r + 1])::"memory");
        HBAR(2);
      }
    }
    const v2u tagv = *(LAS v2u*)(L + L_CTRL + 4 * C_EP);
    const unsigned long long tag = ((unsigned long long)tagv[1] << 32) | tagv[0];
    if (gw == 7) STAMP(9);
    ctl_wait<16>(ctl + 4 * C_FULL, NUNIT - G1_LEAD);
    if (gw == 7) STAMP(10);
    {
      const int src = lane < 8 ? idx * 8 + lane : (2 * rg + (lane & 1)) * 32 + (gw >> 1) * 8 + xc;
      GAS unsigned long long* fl = (GAS unsigned long long*)(ws + W_CF) + FSTR * src;
      for (unsigned sp = 0; sp < (1u << 16); ++sp) {
        if (__all(__hip_atomic_load(fl, __ATOMIC_RELAXED, __HIP_MEMORY_SCOPE_AGENT) == tag)) break;
        __builtin_amdgcn_s_sleep(32);
      }
      __builtin_amdgcn_fence(__ATOMIC_ACQUIRE, "wavefront");
    }
    if (gw == 7) STAMP(11);
#pragma unroll 1
    for (int ph = 0; ph < 2; ++ph) {
      v4u f0, f1;
      for (unsigned sp = 0; sp < (1u << 16); ++sp) {
        CFENCE();
        f0 = __builtin_amdgcn_raw_buffer_load_b128(rsO, lane * 16, W_O + ((ph * 4 + rg) * 8 + xc) * 16384 + gw * 2048, AUX_SC1);
        f1 = __builtin_amdgcn_raw_buffer_load_b128(rsO, lane * 16, W_O + ((ph * 4 + rg) * 8 + xc) * 16384 + gw * 2048 + 1024, AUX_SC1);
        bool ok = true;
#pragma unroll
        for (int k = 0; k < 4; ++k) ok &= (f0[k] != SENT) & (f1[k] != SENT);
        if (__all(ok)) break;
        __builtin_amdgcn_s_sleep(G1_SLEEP);
      }
      *(LAS v4u*)(L + L_OB + gw * 2048 + lane * 16) = f0;
      *(LAS v4u*)(L + L_OB + gw * 2048 + 1024 + lane * 16) = f1;
      LDS_WAIT();
      HBAR(0);
      v4f oa[4];
#pragma unroll
      for (int k = 0; k < 4; ++k) oa[k] = v4f{0.f, 0.f, 0.f, 0.f};
#pragma unroll
      for (int f = 0; f < 16; ++f) oa[f & 3] = mfma(wf[f], *(LAS v4u*)(L + L_OB + f * 1024 + lane * 16), oa[f & 3]);
      v4u pt = __builtin_bit_cast(v4u, (oa[0] + oa[1]) + (oa[2] + oa[3]));
#pragma unroll
      for (int k = 0; k < 4; ++k) pt[k] = pt[k] == SENT ? 0x7FFFFFFFu : pt[k];
      __builtin_amdgcn_raw_buffer_store_b128(pt, rsO, lane * 16, pdst + ph * (2 << 20), P_ST_AUX);
      if (gw == 7) STAMP(24 + ph);
      if (ph == 0) HBAR(0);
    }
    if (gw < 2) {
      v4u q[8];
      for (unsigned sp = 0; sp < (1u << 16); ++sp) {
        CFENCE();
        bool ok = true;
#pragma unroll
        for (int k = 0; k < 8; ++k) q[k] = __builtin_amdgcn_raw_buffer_load_b128(rsO, lane * 16, W_P + gw * (2 << 20) + (idx * 64 + xc * 8 + k) * 1024, AUX_SC1);
#pragma unroll
        for (int k = 0; k < 8; ++k) ok &= (q[k][0] != SENT) & (q[k][1] != SENT) & (q[k][2] != SENT) & (q[k][3] != SENT);
        if (__all(ok)) break;
        __builtin_amdgcn_s_sleep(1);
      }
      v4f s = __builtin_bit_cast(v4f, q[0]);
#pragma unroll
      for (int k = 1; k < 8; ++k) s += __builtin_bit_cast(v4f, q[k]);
      *(GAS v2u*)((GAS char*)out + ((size_t)(64 * gw + 16 * rg + c) * DM + 128 * ct + 16 * xc + 4 * g) * 2) = v2u{pk2(s[0], s[1]), pk2(s[2], s[3])};
      STAMP(12 + gw);
    } else if (gw == 2 && j == 0) {
      GAS unsigned long long* fl = (GAS unsigned long long*)(ws + W_CF) + FSTR * lane;
      for (unsigned sp = 0; sp < (1u << 16); ++sp) {
        bool ok = true;
#pragma unroll
        for (int k = 0; k < 4; ++k) ok &= __hip_atomic_load(fl + FSTR * 64 * k, __ATOMIC_RELAXED, __HIP_MEMORY_SCOPE_AGENT) == tag;
        if (__all(ok)) break;
        __builtin_amdgcn_s_sleep(32);
      }
      if (lane == 0) __hip_atomic_store((GAS unsigned long long*)(ws + W_EPOCH), tag, __ATOMIC_RELAXED, __HIP_MEMORY_SCOPE_AGENT);
    }
  }
}
}

__global__ void k_report(unsigned char* ws) {
  const unsigned long long* h = (const unsigned long long*)(ws + (16u << 20));
  const unsigned long long* p = (const unsigned long long*)(ws + (17u << 20));
  if (threadIdx.x != 0) return;
  unsigned long long t0 = ~0ull;
  for (int j = 0; j < 256; ++j) t0 = h[j * 64] < t0 ? h[j * 64] : t0;
  const int ks[8] = {0, 1, 4, 6, 9, 12, 13, 25};
  for (int q = 0; q < 8; ++q) { int k = ks[q]; unsigned long long mn = ~0ull, mx = 0, sm = 0; for (int j = 0; j < 256; ++j) { unsigned long long v = h[j * 64 + k] - t0; mn = v < mn ? v : mn; mx = v > mx ? v : mx; sm += v; }
    printf("stamp %d: min %d avg %d max %d (x10ns)\n", k, (int)mn, (int)(sm / 256), (int)mx); }
  printf("avg progress tiles 2..11:"); for (int k = 34; k < 44; ++k) { unsigned long long sm = 0; for (int j = 0; j < 256; ++j) sm += p[j * 64 + k]; printf(" %d", (int)(sm / 256)); }
  printf("\navg progress tiles 12,16..:"); for (int k = 3; k < 31; ++k) { unsigned long long sm = 0; for (int j = 0; j < 256; ++j) sm += p[j * 64 + k]; printf(" %d", (int)(sm / 256)); }
  printf("\n");
}
extern "C" void kernel_launch(void* const* d_in, const int* in_sizes, int n_in, void* d_out, int out_size, void* d_ws, size_t ws_size,
                              hipStream_t stream) {
  (void)in_sizes; (void)n_in; (void)out_size; (void)ws_size;
  (void)hipFuncSetAttribute((const void*)mha_block_kernel, hipFuncAttributeMaxDynamicSharedMemorySize, LDS_BYTES);
  mha_block_kernel<<<NWG, NTHR, LDS_BYTES, stream>>>((const unsigned short*)d_in[0], (const unsigned short*)d_in[1], (const unsigned short*)d_in[2],
                                                     (const unsigned short*)d_in[3], (const unsigned short*)d_in[4], (unsigned short*)d_out,
                                                     (unsigned char*)d_ws);
  k_report<<<1, 64, 0, stream>>>((unsigned char*)d_ws);
}
```

```cpp
#define STAMPS 1
#define WHAT 5
#include <hip/hip_runtime.h>
#include <hip/hip_bf16.h>
#include <stdint.h>

#define GAS __attribute__((address_space(1)))
#define LAS __attribute__((address_space(3)))
typedef unsigned v4u __attribute__((ext_vector_type(4)));
typedef unsigned v2u __attribute__((ext_vector_type(2)));
typedef float v4f __attribute__((ext_vector_type(4)));
typedef float v2f __attribute__((ext_vector_type(2)));
typedef short v4s __attribute__((ext_vector_type(4)));
typedef __bf16 v8bf __attribute__((ext_vector_type(8)));
typedef __bf16 v2bf __attribute__((ext_vector_type(2)));

#define WIN 20
#define WIN0 24
#define N0 12
#define WINE 40
#define NE 4
#define NI 2
#define QI_GO 5
#define HQ_WIN 40
#define WF_SLEEP 16
#define G1_LEAD 4
#define G1_SLEEP 4
#define ABL_NOST 0
#define ABL_NOG0 0
#define Q_GAP 4
#define ABL_NOQ 0
#define X_NT 0
#define P_ST_AUX 16
#define G0_AT 160
#define ABL_NOWO 0
#define ABL_NOCLR 0
#define OA_AT 126
#define O_ST_AUX 16

namespace {
constexpr int NB = 16, SQ = 8, DM = 1024, NH = 32, SK = 2048, DH = 128;
constexpr int NWG = 256, NTHR = 768, NATT = 3, NHELP = 8;
constexpr int PIECE = 1056;
constexpr int UNIT = 8 * PIECE;
constexpr int NR = 15;
constexpr int NUNIT = 256;
constexpr int AUX_NT = 2, AUX_SC1 = 16;
constexpr int L_CTRL = NR * UNIT;
constexpr int L_SCR = L_CTRL + 256;
constexpr int L_MRG = L_SCR;
constexpr int MRG_REC = 8192 + 512;
constexpr int L_OB = L_SCR + 2 * MRG_REC;
constexpr int LDS_BYTES = 160 * 1024;
static_assert(L_SCR + 32768 <= LDS_BYTES && L_OB + 16 * PIECE <= LDS_BYTES, "LDS map");
enum { C_FULL = 0, C_REL = 4, C_Q1 = 8, C_Q2 = 9, C_QI = 10, C_HB = 11, C_MRG = 12, C_EP = 14, C_CLR = 16 };
constexpr int W_EPOCH = 0, W_CF = 65536, FSTR = 16;
constexpr int W_O = 1 << 20;
constexpr int W_P = 2 << 20;
constexpr int WS_USED = 6 << 20;
constexpr unsigned SENT = 0xFFFFFFFFu;
constexpr float CSC = 0.08838834764831845f * 1.4426950408889634f;
constexpr unsigned SPIN_CAP = 1u << 22;

__device__ __forceinline__ unsigned rfl(unsigned x) { return __builtin_amdgcn_readfirstlane(x); }
__device__ __forceinline__ v4f mfma(v4u a, v4u b, v4f c) {
  return __builtin_amdgcn_mfma_f32_16x16x32_bf16(__builtin_bit_cast(v8bf, a), __builtin_bit_cast(v8bf, b), c, 0, 0, 0);
}
__device__ __forceinline__ unsigned pk2(float lo, float hi) {
  v2f x = {lo, hi};
  return __builtin_bit_cast(unsigned, __builtin_convertvector(x, v2bf));
}
__device__ __forceinline__ v2u trread(LAS char* p) {
  return __builtin_bit_cast(v2u, __builtin_amdgcn_ds_read_tr16_b64_v4i16((LAS v4s*)p));
}
__device__ __forceinline__ float xg_max(float x) {
  auto s = __builtin_amdgcn_permlane16_swap(__float_as_uint(x), __float_as_uint(x), false, false);
  x = fmaxf(__uint_as_float(s[0]), __uint_as_float(s[1]));
  auto t = __builtin_amdgcn_permlane32_swap(__float_as_uint(x), __float_as_uint(x), false, false);
  return fmaxf(__uint_as_float(t[0]), __uint_as_float(t[1]));
}
__device__ __forceinline__ float xg_sum(float x) {
  auto s = __builtin_amdgcn_permlane16_swap(__float_as_uint(x), __float_as_uint(x), false, false);
  x = __uint_as_float(s[0]) + __uint_as_float(s[1]);
  auto t = __builtin_amdgcn_permlane32_swap(__float_as_uint(x), __float_as_uint(x), false, false);
  return __uint_as_float(t[0]) + __uint_as_float(t[1]);
}
#define STAMP(k) do { if (lane == 0) ((GAS unsigned long long*)(ws + (16u << 20)))[blockIdx.x * 64 + (k)] = __builtin_amdgcn_s_memrealtime(); } while (0)
#define STAMPV(k, v) do { if (lane == 0) ((GAS unsigned long long*)(ws + (16u << 20)))[blockIdx.x * 64 + (k)] = (v); } while (0)
#define LDS_WAIT() asm volatile("s_waitcnt lgkmcnt(0)" ::: "memory")
#define CFENCE() asm volatile("" ::: "memory")
#define PIN8(a) asm volatile("" : "+v"(a[0]), "+v"(a[1]), "+v"(a[2]), "+v"(a[3]), "+v"(a[4]), "+v"(a[5]), "+v"(a[6]), "+v"(a[7])::"memory")
template <int N> __device__ __forceinline__ void vm_wait() { asm volatile("s_waitcnt vmcnt(%c0)" ::"n"(N) : "memory"); }
__device__ __forceinline__ unsigned ctl_rd(unsigned addr) {
  unsigned v;
  asm volatile("ds_read_b32 %0, %1\n\ts_waitcnt lgkmcnt(0)" : "=v"(v) : "v"(addr) : "memory");
  return rfl(v);
}
__device__ __forceinline__ void ctl_wr(unsigned addr, unsigned val) {
  asm volatile("s_mov_b64 exec, 1\n\tds_write_b32 %0, %1\n\ts_mov_b64 exec, -1" ::"v"(addr), "v"(val) : "memory");
}
__device__ __forceinline__ unsigned ctl_add(unsigned addr) {
  unsigned v;
  asm volatile("s_mov_b64 exec, 1\n\tds_add_rtn_u32 %0, %1, %2\n\ts_mov_b64 exec, -1\n\ts_waitcnt lgkmcnt(0)" : "=&v"(v) : "v"(addr), "v"(1u) : "memory");
  return rfl(v);
}
template <int SLP> __device__ __forceinline__ unsigned ctl_wait(unsigned addr, unsigned need) {
  unsigned v = ctl_rd(addr);
  for (unsigned sp = 0; v < need && sp < SPIN_CAP; ++sp) {
    __builtin_amdgcn_s_sleep(SLP);
    v = ctl_rd(addr);
  }
  return v;
}

#define PUBL(k) ".if %c[b] == " #k "\n\ts_mov_b64 exec, 1\n\tds_write_b32 %[fa], v23\n\ts_mov_b64 exec, -1\n\t.endif\n\t"
#define PC(k, VO, IMM) "s_waitcnt vmcnt(%c[w])\n\t" PUBL(k) ".if %c[nt]\n\tbuffer_load_dwordx4 " VO ", %[rs], %[so] offen offset:" IMM " nt lds\n\t.else\n\tbuffer_load_dwordx4 " VO ", %[rs], %[so] offen offset:" IMM " lds\n\t.endif\n\t"
template <int W, int NTF = 1>
__device__ __forceinline__ void dma_unit(__amdgpu_buffer_rsrc_t rs, unsigned soff, unsigned& m0b, unsigned& u, unsigned& lim, unsigned vo0, unsigned vo1,
                                         unsigned vctl, unsigned faddr) {
  unsigned sp;
  asm volatile(
      "s_cmp_lt_u32 %[u], %[lim]\n\t"
      "s_cbranch_scc1 2f\n\t"
      "s_mov_b32 %[sp], 0\n"
      "1:\n\t"
      "ds_read_b32 v20, %[vc] offset:16\n\tds_read_b32 v21, %[vc] offset:20\n\tds_read_b32 v22, %[vc] offset:24\n\t"
      "s_waitcnt lgkmcnt(0)\n\t"
      "v_min3_u32 v20, v20, v21, v22\n\t"
      "s_add_u32 %[sp], %[sp], 1\n\t"
      "v_readfirstlane_b32 %[lim], v20\n\t"
      "s_add_u32 %[lim], %[lim], %c[nr]\n\t"
      "s_cmp_lt_u32 %[u], %[lim]\n\t"
      "s_cbranch_scc1 2f\n\t"
      "s_sleep 1\n\t"
      "s_cmp_lt_u32 %[sp], %c[cap]\n\t"
      "s_cbranch_scc1 1b\n"
      "2:\n\t"
      "s_mov_b32 m0, %[m0b]\n\t"
      "v_mov_b32 v23, %[u]\n\t"
      "v_subrev_u32 v23, %c[a], v23\n\t"
      PC(0, "%[vo0]", "0")
      "ds_read_b32 v20, %[vc] offset:16\n\tds_read_b32 v21, %[vc] offset:20\n\tds_read_b32 v22, %[vc] offset:24\n\t"
      "s_add_u32 m0, m0, 32\n\t"
      PC(1, "%[vo0]", "1024")
      "s_add_u32 m0, m0, 32\n\t"
      PC(2, "%[vo0]", "2048")
      "s_add_u32 m0, m0, 32\n\t"
      PC(3, "%[vo0]", "3072")
      "s_add_u32 m0, m0, 0x1020\n\t"
      PC(4, "%[vo1]", "0")
      "s_waitcnt lgkmcnt(0)\n\t"
      "v_min3_u32 v20, v20, v21, v22\n\t"
      "s_add_u32 m0, m0, 32\n\t"
      PC(5, "%[vo1]", "1024")
      "v_readfirstlane_b32 %[lim], v20\n\t"
      "s_add_u32 %[m0b], %[m0b], %c[unit]\n\t"
      "s_add_u32 m0, m0, 32\n\t"
      PC(6, "%[vo1]", "2048")
      "s_add_u32 %[lim], %[lim], %c[nr]\n\t"
      "s_cmp_lt_u32 %[m0b], %c[ring]\n\t"
      "s_cselect_b32 %[m0b], %[m0b], 0\n\t"
      "s_add_u32 %[u], %[u], 1\n\t"
      "s_add_u32 m0, m0, 32\n\t"
      PC(7, "%[vo1]", "3072")
      : [m0b] "+s"(m0b), [u] "+s"(u), [lim] "+s"(lim), [sp] "=&s"(sp)
      : [nt] "n"(NTF), [w] "n"(W - 1), [a] "n"((W - 1) / 8), [b] "n"((W - 1) % 8), [nr] "n"(NR), [cap] "n"(SPIN_CAP), [unit] "n"(UNIT), [ring] "n"(NR * UNIT), [rs] "s"(rs),
        [so] "s"(soff), [vo0] "v"(vo0), [vo1] "v"(vo1), [vc] "v"(vctl), [fa] "v"(faddr)
      : "memory", "scc", "v20", "v21", "v22", "v23");
}

__global__ void __launch_bounds__(NTHR, 3)
mha_block_kernel(const unsigned short* __restrict__ x, const unsigned short* __restrict__ kc, const unsigned short* __restrict__ vc,
                 const unsigned short* __restrict__ wq, const unsigned short* __restrict__ wo, unsigned short* __restrict__ out,
                 unsigned char* __restrict__ ws) {
  extern __shared__ __attribute__((aligned(16))) char smem[];
  LAS char* L = (LAS char*)smem;
  const unsigned lbase = (unsigned)(size_t)L;
  const int lane = threadIdx.x & 63, wave = rfl(threadIdx.x >> 6);
  const int g = lane >> 4, c = lane & 15;
  const int j = blockIdx.x, h = j & 31, bp = j >> 5;
  const unsigned ctl = lbase + L_CTRL;
  const __amdgpu_buffer_rsrc_t rsO = __builtin_amdgcn_make_buffer_rsrc((void*)ws, 0, WS_USED, 0x00020000);

  if (wave == 0) {
    __builtin_amdgcn_s_setprio(3);
    __builtin_amdgcn_s_waitcnt(0x0F70);
    unsigned tv = 0, nstall = 0;
    const unsigned long long tl0 = __builtin_amdgcn_s_memrealtime();
    static_assert(WIN <= 24 && WIN > 16 && WIN0 >= WIN && WIN0 <= 40, "publish ladder below; published counts never decrease");
    const unsigned vo0 = lane * 16, vo1 = lane * 16 + 4096;
    const unsigned sbase = (unsigned)(bp * NH + h) * (SK * DH * 2);
    const __amdgpu_buffer_rsrc_t rsK = __builtin_amdgcn_make_buffer_rsrc((void*)kc, 0, NB * NH * SK * DH * 2, 0x00020000);
    const __amdgpu_buffer_rsrc_t rsV = __builtin_amdgcn_make_buffer_rsrc((void*)vc, 0, NB * NH * SK * DH * 2, 0x00020000);
    unsigned m0b = 0, u = 0, lim = NR;
#pragma unroll 1
    for (int n = 0; n < NI; ++n) {
      dma_unit<48>(rsK, sbase + (unsigned)n * 8192u, m0b, u, lim, vo0, vo1, ctl, ctl + 252);
      lim = NR;
      dma_unit<48>(rsV, sbase + (unsigned)n * 8192u, m0b, u, lim, vo0, vo1, ctl, ctl + 252);
      lim = NR;
      if (n == 0) __builtin_amdgcn_s_barrier();
    }
    vm_wait<0>(); ctl_wr(ctl + 4 * C_FULL, 2 * NI);
    ctl_wait<1>(ctl + 4 * C_QI, QI_GO);
#pragma unroll 1
    for (int n = NI; n < N0; ++n) {
      { const unsigned tn = (unsigned)(__builtin_amdgcn_s_memrealtime() - tl0); tv = lane == 32 + n ? tn : tv; }
      dma_unit<WIN0>(rsK, sbase + (unsigned)n * 8192u, m0b, u, lim, vo0, vo1, ctl, 2 * (n - NI) < (WIN0 - 1) / 8 ? ctl + 252 : ctl + 4 * C_FULL);
      dma_unit<WIN0>(rsV, sbase + (unsigned)n * 8192u, m0b, u, lim, vo0, vo1, ctl, 2 * (n - NI) + 1 < (WIN0 - 1) / 8 ? ctl + 252 : ctl + 4 * C_FULL);
    }
#pragma unroll 1
    for (int n = N0; n < 128 - NE; ++n) {
      if ((n & 3) == 0) { const unsigned tn = (unsigned)(__builtin_amdgcn_s_memrealtime() - tl0); tv = lane == (n >> 2) ? tn : tv; }
      const unsigned soff = sbase + (unsigned)(n >> 6) * (8u * NH * SK * DH * 2) + (unsigned)(n & 63) * 8192u;
      dma_unit<WIN>(rsK, soff, m0b, u, lim, vo0, vo1, ctl, ctl + 4 * C_FULL);
      dma_unit<WIN>(rsV, soff, m0b, u, lim, vo0, vo1, ctl, ctl + 4 * C_FULL);
    }
#pragma unroll 1
    for (int n = 128 - NE; n < 128; ++n) {
      const unsigned soff = sbase + (8u * NH * SK * DH * 2) + (unsigned)(n & 63) * 8192u;
      const int k = 2 * (n - (128 - NE));
      dma_unit<WINE>(rsK, soff, m0b, u, lim, vo0, vo1, ctl, k < (WINE - 1) / 8 - (WIN - 1) / 8 ? ctl + 252 : ctl + 4 * C_FULL);
      dma_unit<WINE>(rsV, soff, m0b, u, lim, vo0, vo1, ctl, k + 1 < (WINE - 1) / 8 - (WIN - 1) / 8 ? ctl + 252 : ctl + 4 * C_FULL);
    }
    if (WINE > 48) { vm_wait<48>(); ctl_wr(ctl + 4 * C_FULL, NUNIT - 6); }
    if (WINE > 40) { vm_wait<40>(); ctl_wr(ctl + 4 * C_FULL, NUNIT - 5); }
    if (WINE > 32) { vm_wait<32>(); ctl_wr(ctl + 4 * C_FULL, NUNIT - 4); }
    if (WINE > 24) { vm_wait<24>(); ctl_wr(ctl + 4 * C_FULL, NUNIT - 3); }
    vm_wait<16>(); ctl_wr(ctl + 4 * C_FULL, NUNIT - 2);
    vm_wait<8>(); ctl_wr(ctl + 4 * C_FULL, NUNIT - 1);
    vm_wait<0>(); ctl_wr(ctl + 4 * C_FULL, NUNIT);
    STAMPV(0, tl0); STAMP(1); STAMPV(2, nstall);
    ((GAS unsigned long long*)(ws + (17u << 20)))[blockIdx.x * 64 + lane] = tv;
    { unsigned xcc; asm volatile("s_getreg_b32 %0, hwreg(HW_REG_XCC_ID)" : "=s"(xcc)); STAMPV(3, xcc & 15); }
  } else if ((wave & 3) == 0 || wave == 1) {
    const int aw = wave == 1 ? 2 : (wave >> 2) - 1;
    if (aw == 0) { *(LAS unsigned*)(L + L_CTRL + 4 * lane) = 0u; LDS_WAIT(); }
    __builtin_amdgcn_s_barrier();
    const unsigned koff = (c & 7) * PIECE + (c >> 3) * 256 + g * 16;
    const unsigned voff = (4 * (g & 1) + (c >> 2)) * PIECE + (g >> 1) * 256 + (c & 3) * 16;
    const int obase = W_O + ((bp >> 1) * 8 + (j & 7)) * 16384 + (h >> 3) * 4096 + g * 256 + ((bp & 1) * 8 + (c & 7)) * 16;
    {
      const unsigned long long tag = __hip_atomic_load((GAS unsigned long long*)(ws + W_EPOCH), __ATOMIC_RELAXED, __HIP_MEMORY_SCOPE_AGENT) + 1ull;
      const v4u sv = {SENT, SENT, SENT, SENT};
      if (aw == 0) {
#pragma unroll
        for (int s = 0; s < 4; ++s) __builtin_amdgcn_raw_buffer_store_b128(sv, rsO, obase + (c >> 3) * (4 * 8 * 16384), s * 1024, O_ST_AUX);
        if (lane == 0) *(LAS v2u*)(L + L_CTRL + 4 * C_EP) = v2u{(unsigned)tag, (unsigned)(tag >> 32)};
      } else {
#pragma unroll
        for (int k = 0; k < 8; ++k) __builtin_amdgcn_raw_buffer_store_b128(sv, rsO, lane * 16, W_P + (aw - 1) * (2 << 20) + ((j >> 3) * 64 + k * 8 + (j & 7)) * 1024, P_ST_AUX);
      }
      vm_wait<0>();
      if (ctl_add(ctl + 4 * C_CLR) == NATT - 1 && lane == 0) __hip_atomic_store((GAS unsigned long long*)(ws + W_CF) + FSTR * j, tag, __ATOMIC_RELAXED, __HIP_MEMORY_SCOPE_AGENT);
    }
    v4u oA[4];
#pragma unroll
    for (int s = 0; s < 4; ++s) oA[s] = v4u{0u, 0u, 0u, 0u};
    v4u kf[8], vf[8];
    unsigned full = ctl_wait<1>(ctl + 4 * C_FULL, 2 * aw + 2);
    {
      CFENCE();
      LAS char* kb = L + (2 * aw) * UNIT + koff;
      LAS char* vb = L + (2 * aw + 1) * UNIT + voff;
#pragma unroll
      for (int i = 0; i < 8; ++i) kf[i] = *(LAS v4u*)(kb + (i >> 2) * 512 + (i & 3) * 64);
#pragma unroll
      for (int d = 0; d < 8; ++d) {
        const v2u a = trread(vb + (d >> 1) * 64 + (d & 1) * 8), b = trread(vb + 512 + (d >> 1) * 64 + (d & 1) * 8);
        vf[d] = v4u{a[0], a[1], b[0], b[1]};
      }
      LDS_WAIT();
      ctl_wr(ctl + 4 * (C_REL + aw), 2 * aw + 2 * NATT);
      PIN8(kf); PIN8(vf);
      if (aw == 0) STAMP(14);
      ctl_wait<1>(ctl + 4 * C_Q2, 4);
      CFENCE();
    }
    v4u qf[4];
#pragma unroll
    for (int s = 0; s < 4; ++s) {
      LAS char* qp = L + L_SCR + g * 2048 + (s * 16 + c) * 16;
      const v4f lo = (*(LAS v4f*)(qp) + *(LAS v4f*)(qp + 8192)) + (*(LAS v4f*)(qp + 16384) + *(LAS v4f*)(qp + 24576));
      const v4f hi = (*(LAS v4f*)(qp + 1024) + *(LAS v4f*)(qp + 9216)) + (*(LAS v4f*)(qp + 17408) + *(LAS v4f*)(qp + 25600));
      qf[s] = v4u{pk2(lo[0], lo[1]), pk2(lo[2], lo[3]), pk2(hi[0], hi[1]), pk2(hi[2], hi[3])};
      if (s & 1) asm volatile("" : "+v"(qf[s - 1]), "+v"(qf[s])::"memory");
    }
    if (aw == 0) STAMP(4);
    float m = -INFINITY, l = 0.f;
    v4f acc[8];
#pragma unroll
    for (int d = 0; d < 8; ++d) acc[d] = v4f{0.f, 0.f, 0.f, 0.f};
    unsigned pos = 2 * aw;
#pragma unroll 1
    for (int n = aw; n < 128; n += NATT) {
      const unsigned uk = 2 * n;
      const unsigned posv = pos + 1 == NR ? 0 : pos + 1;
      if (n >= NATT) {
        if (full < uk + 1) full = ctl_wait<1>(ctl + 4 * C_FULL, uk + 1);
        CFENCE();
        LAS char* kb = L + pos * UNIT + koff;
#pragma unroll
        for (int i = 0; i < 8; ++i) kf[i] = *(LAS v4u*)(kb + (i >> 2) * 512 + (i & 3) * 64);
        LDS_WAIT();
        ctl_wr(ctl + 4 * (C_REL + aw), uk + 1);
        PIN8(kf);
      }
      v4f s0 = {0.f, 0.f, 0.f, 0.f}, s1 = {0.f, 0.f, 0.f, 0.f};
#pragma unroll
      for (int s = 0; s < 4; ++s) { s0 = mfma(kf[s], qf[s], s0); s1 = mfma(kf[4 + s], qf[s], s1); }
      float mx = fmaxf(fmaxf(fmaxf(s0[0], s0[1]), fmaxf(s0[2], s0[3])), fmaxf(fmaxf(s1[0], s1[1]), fmaxf(s1[2], s1[3])));
      mx = xg_max(mx);
      const bool lastt = (n & 63) == 63;
      const int ph = n >> 6;
      LAS char* r0 = L + L_MRG;
      LAS char* r1 = L + L_MRG + MRG_REC;
      v2f a0 = {-INFINITY, 0.f}, a1 = {-INFINITY, 0.f};
      if (lastt) {
        ctl_wait<0>(ctl + 4 * (C_MRG + ph), 2);
        CFENCE();
        a0 = *(LAS v2f*)(r0 + 8192 + lane * 8);
        a1 = *(LAS v2f*)(r1 + 8192 + lane * 8);
        mx = fmaxf(mx, fmaxf(a0[0], a1[0]));
      }
      const float mn = fmaxf(m, mx);
      const float alpha = __builtin_amdgcn_exp2f((m - mn) * CSC);
      const float mc = mn * CSC;
      m = mn;
      float p[8];
#pragma unroll
      for (int i = 0; i < 4; ++i) { p[i] = __builtin_amdgcn_exp2f(fmaf(s0[i], CSC, -mc)); p[4 + i] = __builtin_amdgcn_exp2f(fmaf(s1[i], CSC, -mc)); }
      l = fmaf(l, alpha, ((p[0] + p[1]) + (p[2] + p[3])) + ((p[4] + p[5]) + (p[6] + p[7])));
      v4u pf = {pk2(p[0], p[1]), pk2(p[2], p[3]), pk2(p[4], p[5]), pk2(p[6], p[7])};
#pragma unroll
      for (int d = 0; d < 8; ++d) acc[d] *= alpha;
      if (lastt) {
        const float w0 = __builtin_amdgcn_exp2f((a0[0] - mn) * CSC), w1 = __builtin_amdgcn_exp2f((a1[0] - mn) * CSC);
        l = 1.0f / xg_sum(fmaf(w0, a0[1], fmaf(w1, a1[1], l)));
#pragma unroll
        for (int d = 0; d < 8; ++d) acc[d] += *(LAS v4f*)(r0 + d * 1024 + lane * 16) * w0 + *(LAS v4f*)(r1 + d * 1024 + lane * 16) * w1;
      }
      asm volatile("" : "+v"(pf), "+v"(l));
      PIN8(acc);
      if (n >= NATT) {
        if (full < uk + 2) full = ctl_wait<0>(ctl + 4 * C_FULL, uk + 2);
        CFENCE();
        LAS char* vb = L + posv * UNIT + voff;
#pragma unroll
        for (int d = 0; d < 8; ++d) {
          const v2u a = trread(vb + (d >> 1) * 64 + (d & 1) * 8), b = trread(vb + 512 + (d >> 1) * 64 + (d & 1) * 8);
          vf[d] = v4u{a[0], a[1], b[0], b[1]};
        }
        LDS_WAIT();
        ctl_wr(ctl + 4 * (C_REL + aw), uk + 2 * NATT);
        PIN8(vf);
      }
#pragma unroll
      for (int d = 0; d < 8; ++d) acc[d] = mfma(vf[d], pf, acc[d]);
      pos += 2 * NATT;
      if (pos >= NR) pos -= NR;
      if (n == OA_AT && c < 8) {
#pragma unroll
        for (int s = 0; s < 4; ++s) __builtin_amdgcn_raw_buffer_store_b128(oA[s], rsO, obase + s * 1024, 0, O_ST_AUX);
      }
      if ((n & 63) + NATT >= 64) {
        const bool mine = (c >> 3) == ph;
        if (!lastt) {
          LAS char* rec = L + L_MRG + (62 - (n & 63)) * MRG_REC;
          if (mine) {
#pragma unroll
            for (int d = 0; d < 8; ++d) *(LAS v4f*)(rec + d * 1024 + lane * 16) = acc[d];
            *(LAS v2f*)(rec + 8192 + lane * 8) = v2f{m, l};
          }
          LDS_WAIT();
          ctl_add(ctl + 4 * (C_MRG + ph));
        } else {
#pragma unroll
          for (int s = 0; s < 4; ++s) {
            const v4f x0 = acc[2 * s] * l, x1 = acc[2 * s + 1] * l;
            v4u o = {pk2(x0[0], x0[1]), pk2(x0[2], x0[3]), pk2(x1[0], x1[1]), pk2(x1[2], x1[3])};
#pragma unroll
            for (int k = 0; k < 4; ++k) o[k] = o[k] == SENT ? 0x7FFF7FFFu : o[k];
            if (ph == 0) oA[s] = o;
            else if (mine) __builtin_amdgcn_raw_buffer_store_b128(o, rsO, obase + s * 1024, 4 * 8 * 16384, O_ST_AUX);
          }
          STAMP(5 + ph);
        }
        m = -INFINITY; l = 0.f;
#pragma unroll
        for (int d = 0; d < 8; ++d) acc[d] = v4f{0.f, 0.f, 0.f, 0.f};
      }
    }
  } else {
    const int gw = wave - 2 - (wave >> 2);
    {
      const __amdgpu_buffer_rsrc_t rsQ = __builtin_amdgcn_make_buffer_rsrc((void*)wq, 0, ABL_NOQ ? 0 : DM * NH * DH * 2, 0x00020000);
      const int wvo = (8 * g * (NH * DH) + 8 * c) * 2, wso = (128 * gw * (NH * DH) + h * DH) * 2;
      const int xrow = ((c < 8 ? bp : bp + 8) * SQ + (c & 7));
      const GAS char* xb = (const GAS char*)x + ((size_t)xrow * DM + 128 * gw + 8 * g) * 2;
#define WQ_ROW(t, jj) ((32 * (((t) + (bp >> 1)) & 3) + ((jj) ^ (4 * (bp & 1)))) * (NH * DH * 2))
      v4u xf[4], w[4][8];
      __builtin_amdgcn_s_barrier();
      __builtin_amdgcn_sched_barrier(0);
#pragma unroll
      for (int t = 0; t < 3; ++t)
#pragma unroll
        for (int jj = 0; jj < 8; ++jj) {
          if (t == 0 && jj == 4) { vm_wait<0>(); __builtin_amdgcn_s_sleep(Q_GAP); }
          w[t][jj] = __builtin_amdgcn_raw_buffer_load_b128(rsQ, wvo, wso + WQ_ROW(t, jj), AUX_NT);
          if (t == 0 && jj == 3) {
#pragma unroll
            for (int tt = 0; tt < 4; ++tt) xf[tt] = X_NT ? __builtin_nontemporal_load((const GAS v4u*)(xb + ((tt + (bp >> 1)) & 3) * 64)) : *(const GAS v4u*)(xb + ((tt + (bp >> 1)) & 3) * 64);
          }
          __builtin_amdgcn_sched_barrier(0);
        }
      ctl_add(ctl + 4 * C_QI);
      if (bp & 1) {
#pragma unroll
        for (int t = 0; t < 4; ++t) xf[t] = v4u{xf[t][2], xf[t][3], xf[t][0], xf[t][1]};
      }
      v4f qa[8];
#pragma unroll
      for (int d = 0; d < 8; ++d) qa[d] = v4f{0.f, 0.f, 0.f, 0.f};
#pragma unroll
      for (int t = 0; t < 4; ++t) {
#pragma unroll
        for (int dd = 0; dd < 8; ++dd) {
          v4u f;
#pragma unroll
          for (int v = 0; v < 4; ++v) f[v] = __builtin_amdgcn_perm(w[t][2 * v + 1][dd >> 1], w[t][2 * v][dd >> 1], (dd & 1) ? 0x07060302u : 0x05040100u);
          qa[dd] = mfma(f, xf[t], qa[dd]);
          if (dd & 1) __builtin_amdgcn_sched_barrier(0);
        }
        if (t == 0) {
#pragma unroll
          for (int jj = 0; jj < 8; ++jj) {
            w[3][jj] = __builtin_amdgcn_raw_buffer_load_b128(rsQ, wvo, wso + WQ_ROW(3, jj), AUX_NT);
            __builtin_amdgcn_sched_barrier(0);
          }
        }
      }
      STAMP(16 + gw);
      LAS char* part = L + L_SCR + (gw & 3) * 8192 + lane * 16;
      if (gw >= 4) {
        ctl_wait<0>(ctl + 4 * C_Q1, 4);
        CFENCE();
      }
#pragma unroll
      for (int i = 0; i < 4; ++i) {
        v4f lo = {qa[0][i], qa[1][i], qa[2][i], qa[3][i]}, hi = {qa[4][i], qa[5][i], qa[6][i], qa[7][i]};
        if (gw >= 4) { lo += *(LAS v4f*)(part + i * 2048); hi += *(LAS v4f*)(part + i * 2048 + 1024); }
        *(LAS v4f*)(part + i * 2048) = lo;
        *(LAS v4f*)(part + i * 2048 + 1024) = hi;
      }
      LDS_WAIT();
      ctl_add(ctl + 4 * (gw >= 4 ? C_Q2 : C_Q1));
      if (gw == 0) STAMP(7);
      if (gw == 7) STAMP(8);
    }
    const int xc = j & 7, idx = j >> 3, ct = idx & 7, rg = idx >> 3;
    unsigned hb = 0;
#define HBAR(SLP) do { hb += NHELP; ctl_add(ctl + 4 * C_HB); ctl_wait<SLP>(ctl + 4 * C_HB, hb); CFENCE(); } while (0)
    const int pdst = W_P + (idx * 64 + gw * 8 + xc) * 1024;
    v4u wf[16];
    {
      const __amdgpu_buffer_rsrc_t rsW = __builtin_amdgcn_make_buffer_rsrc((void*)wo, 0, ABL_NOWO ? 0 : DM * NH * DH * 2, 0x00020000);
      ctl_wait<32>(ctl + 4 * C_FULL, 40 + 2 * gw);
#pragma unroll
      for (int ks = 0; ks < 16; ++ks) {
        wf[ks] = __builtin_amdgcn_raw_buffer_load_b128(rsW, g * 2048 + c * 16, (((ks >> 2) * 8 + xc) * 128 + (ks & 3) * 32 + 4 * gw) * 2048 + ct * 256, AUX_NT);
        __builtin_amdgcn_s_sleep(WF_SLEEP);
        __builtin_amdgcn_sched_barrier(0);
      }
      LAS char* sw = L + L_OB + gw * PIECE + lane * 16;
      LAS char* sr = L + L_OB + 2 * g * PIECE + (c >> 2) * 256 + gw * 32 + (c & 3) * 8;
#pragma unroll
      for (int r = 0; r < 8; ++r) {
        *(LAS v4u*)(sw) = wf[2 * r];
        *(LAS v4u*)(sw + 8 * PIECE) = wf[2 * r + 1];
        LDS_WAIT();
        HBAR(2);
        const v2u a0 = trread(sr), b0 = trread(sr + PIECE), a1 = trread(sr + 8 * PIECE), b1 = trread(sr + 9 * PIECE);
        wf[2 * r] = v4u{a0[0], a0[1], b0[0], b0[1]};
        wf[2 * r + 1] = v4u{a1[0], a1[1], b1[0], b1[1]};
        LDS_WAIT();
        asm volatile("" : "+v"(wf[2 * r]), "+v"(wf[2 * r + 1])::"memory");
        HBAR(2);
      }
    }
    const v2u tagv = *(LAS v2u*)(L + L_CTRL + 4 * C_EP);
    const unsigned long long tag = ((unsigned long long)tagv[1] << 32) | tagv[0];
    if (gw == 7) STAMP(9);
    ctl_wait<16>(ctl + 4 * C_FULL, NUNIT - G1_LEAD);
    if (gw == 7) STAMP(10);
    {
      const int src = lane < 8 ? idx * 8 + lane : (2 * rg + (lane & 1)) * 32 + (gw >> 1) * 8 + xc;
      GAS unsigned long long* fl = (GAS unsigned long long*)(ws + W_CF) + FSTR * src;
      for (unsigned sp = 0; sp < (1u << 16); ++sp) {
        if (__all(__hip_atomic_load(fl, __ATOMIC_RELAXED, __HIP_MEMORY_SCOPE_AGENT) == tag)) break;
        __builtin_amdgcn_s_sleep(32);
      }
      __builtin_amdgcn_fence(__ATOMIC_ACQUIRE, "wavefront");
    }
    if (gw == 7) STAMP(11);
#pragma unroll 1
    for (int ph = 0; ph < 2; ++ph) {
      v4u f0, f1;
      for (unsigned sp = 0; sp < (1u << 16); ++sp) {
        CFENCE();
        f0 = __builtin_amdgcn_raw_buffer_load_b128(rsO, lane * 16, W_O + ((ph * 4 + rg) * 8 + xc) * 16384 + gw * 2048, AUX_SC1);
        f1 = __builtin_amdgcn_raw_buffer_load_b128(rsO, lane * 16, W_O + ((ph * 4 + rg) * 8 + xc) * 16384 + gw * 2048 + 1024, AUX_SC1);
        bool ok = true;
#pragma unroll
        for (int k = 0; k < 4; ++k) ok &= (f0[k] != SENT) & (f1[k] != SENT);
        if (__all(ok)) break;
        __builtin_amdgcn_s_sleep(G1_SLEEP);
      }
      *(LAS v4u*)(L + L_OB + gw * 2048 + lane * 16) = f0;
      *(LAS v4u*)(L + L_OB + gw * 2048 + 1024 + lane * 16) = f1;
      LDS_WAIT();
      HBAR(0);
      v4f oa[4];
#pragma unroll
      for (int k = 0; k < 4; ++k) oa[k] = v4f{0.f, 0.f, 0.f, 0.f};
#pragma unroll
      for (int f = 0; f < 16; ++f) oa[f & 3] = mfma(wf[f], *(LAS v4u*)(L + L_OB + f * 1024 + lane * 16), oa[f & 3]);
      v4u pt = __builtin_bit_cast(v4u, (oa[0] + oa[1]) + (oa[2] + oa[3]));
#pragma unroll
      for (int k = 0; k < 4; ++k) pt[k] = pt[k] == SENT ? 0x7FFFFFFFu : pt[k];
      __builtin_amdgcn_raw_buffer_store_b128(pt, rsO, lane * 16, pdst + ph * (2 << 20), P_ST_AUX);
      if (gw == 7) STAMP(24 + ph);
      if (ph == 0) HBAR(0);
    }
    if (gw < 2) {
      v4u q[8];
      for (unsigned sp = 0; sp < (1u << 16); ++sp) {
        CFENCE();
        bool ok = true;
#pragma unroll
        for (int k = 0; k < 8; ++k) q[k] = __builtin_amdgcn_raw_buffer_load_b128(rsO, lane * 16, W_P + gw * (2 << 20) + (idx * 64 + xc * 8 + k) * 1024, AUX_SC1);
#pragma unroll
        for (int k = 0; k < 8; ++k) ok &= (q[k][0] != SENT) & (q[k][1] != SENT) & (q[k][2] != SENT) & (q[k][3] != SENT);
        if (__all(ok)) break;
        __builtin_amdgcn_s_sleep(1);
      }
      v4f s = __builtin_bit_cast(v4f, q[0]);
#pragma unroll
      for (int k = 1; k < 8; ++k) s += __builtin_bit_cast(v4f, q[k]);
      *(GAS v2u*)((GAS char*)out + ((size_t)(64 * gw + 16 * rg + c) * DM + 128 * ct + 16 * xc + 4 * g) * 2) = v2u{pk2(s[0], s[1]), pk2(s[2], s[3])};
      STAMP(12 + gw);
    } else if (gw == 2 && j == 0) {
      GAS unsigned long long* fl = (GAS unsigned long long*)(ws + W_CF) + FSTR * lane;
      for (unsigned sp = 0; sp < (1u << 16); ++sp) {
        bool ok = true;
#pragma unroll
        for (int k = 0; k < 4; ++k) ok &= __hip_atomic_load(fl + FSTR * 64 * k, __ATOMIC_RELAXED, __HIP_MEMORY_SCOPE_AGENT) == tag;
        if (__all(ok)) break;
        __builtin_amdgcn_s_sleep(32);
      }
      if (lane == 0) __hip_atomic_store((GAS unsigned long long*)(ws + W_EPOCH), tag, __ATOMIC_RELAXED, __HIP_MEMORY_SCOPE_AGENT);
    }
  }
}
}

__global__ void k_delay(unsigned char* ws) {
  const unsigned long long tn = __builtin_amdgcn_s_memrealtime();
  const unsigned long long* h = (const unsigned long long*)(ws + (16u << 20));
  const unsigned long long* p = (const unsigned long long*)(ws + (17u << 20));
  const int t = threadIdx.x;
  __shared__ unsigned long long a0[256], a1[256], a2[256];
  a0[t] = h[t * 64]; a1[t] = h[t * 64 + 13]; a2[t] = WHAT == 1 ? p[t * 64 + 30] : (WHAT == 3 ? p[t * 64 + 3] : h[t * 64 + 1]);
  __syncthreads();
  if (t == 0) {
    unsigned long long t0 = ~0ull, t1 = 0, sm = 0, mx = 0;
    for (int j = 0; j < 256; ++j) { t0 = a0[j] < t0 ? a0[j] : t0; t1 = a1[j] > t1 ? a1[j] : t1; sm += a2[j]; mx = a2[j] > mx ? a2[j] : mx; }
    long long S;
    if (WHAT == 0) S = (long long)(t1 - t0) - 7000;
    else if (WHAT == 1) S = (long long)(sm / 256) - 6000;
    else if (WHAT == 2) S = (long long)(tn - t1);
    else if (WHAT == 3) S = (long long)(sm / 256);
    else if (WHAT == 4) S = (long long)(mx - t0) - 7000;
    else S = 0;
    if (S < 0) S = 0; if (S > 20000) S = 20000;
    while ((long long)(__builtin_amdgcn_s_memrealtime() - tn) < 10 * S + 1000) __builtin_amdgcn_s_sleep(1);
  }
}
extern "C" void kernel_launch(void* const* d_in, const int* in_sizes, int n_in, void* d_out, int out_size, void* d_ws, size_t ws_size,
                              hipStream_t stream) {
  (void)in_sizes; (void)n_in; (void)out_size; (void)ws_size;
  (void)hipFuncSetAttribute((const void*)mha_block_kernel, hipFuncAttributeMaxDynamicSharedMemorySize, LDS_BYTES);
  mha_block_kernel<<<NWG, NTHR, LDS_BYTES, stream>>>((const unsigned short*)d_in[0], (const unsigned short*)d_in[1], (const unsigned short*)d_in[2],
                                                     (const unsigned short*)d_in[3], (const unsigned short*)d_in[4], (unsigned short*)d_out,
                                                     (unsigned char*)d_ws);
  k_delay<<<1, 256, 0, stream>>>((unsigned char*)d_ws);
}
```

```cpp
#include <hip/hip_runtime.h>
#include <hip/hip_bf16.h>
#include <stdint.h>

#define GAS __attribute__((address_space(1)))
#define LAS __attribute__((address_space(3)))
typedef unsigned v4u __attribute__((ext_vector_type(4)));
typedef unsigned v2u __attribute__((ext_vector_type(2)));
typedef float v4f __attribute__((ext_vector_type(4)));
typedef float v2f __attribute__((ext_vector_type(2)));
typedef short v4s __attribute__((ext_vector_type(4)));
typedef __bf16 v8bf __attribute__((ext_vector_type(8)));
typedef __bf16 v2bf __attribute__((ext_vector_type(2)));

#define WIN 20
#define WIN0 24
#define N0 12
#define WINE 40
#define NE 4
#define WINB 28
#define CK_N 40
#define CK_T 2760
#define CK_G 80
#define NI 6
#define R1_GO 1
#define N0A 12
#define N0B 10
#define QB_AT 13
#define CLRB_AT 9
#define QI_GO 5
#define HQ_WIN 40
#define WF_SLEEP 16
#define G1_LEAD 4
#define G1_SLEEP 1
#define ABL_NOST 0
#define ABL_NOG0 0
#define Q_GAP 4
#define ABL_NOQ 0
#define X_NT 0
#define P_ST_AUX 16
#define G0_AT 160
#define ABL_NOWO 0
#define ABL_NOCLR 0
#define OA_AT 123
#define MAP1 1
#define O_ST_AUX 16

namespace {
constexpr int NB = 16, SQ = 8, DM = 1024, NH = 32, SK = 2048, DH = 128;
constexpr int NWG = 256, NTHR = 768, NATT = 3, NHELP = 8;
constexpr int PIECE = 1056;
constexpr int UNIT = 8 * PIECE;
constexpr int NR = 15;
constexpr int NUNIT = 256;
constexpr int AUX_NT = 2, AUX_SC1 = 16;
constexpr int L_CTRL = NR * UNIT;
constexpr int L_SCR = L_CTRL + 256;
constexpr int L_MRG = L_SCR;
constexpr int MRG_REC = 8192 + 512;
constexpr int L_OB = L_SCR + 2 * MRG_REC;
constexpr int LDS_BYTES = 160 * 1024;
static_assert(L_SCR + 32768 <= LDS_BYTES && L_OB + 16 * PIECE <= LDS_BYTES, "LDS map");
enum { C_FULL = 0, C_REL = 4, C_Q1 = 8, C_Q2 = 9, C_QI = 10, C_HB = 11, C_EP = 14, C_CLR = 16, C_R1 = 17, C_ML = 18, C_MRG = 20, C_LDONE = 24 };
constexpr int W_EPOCH = 0, W_CF = 65536, FSTR = 16;
constexpr int W_O = 1 << 20;
constexpr int W_P = 2 << 20;
constexpr int WS_USED = 6 << 20;
constexpr unsigned SENT = 0xFFFFFFFFu;
constexpr float CSC = 0.08838834764831845f * 1.4426950408889634f;
constexpr unsigned SPIN_CAP = 1u << 22;

__device__ __forceinline__ unsigned rfl(unsigned x) { return __builtin_amdgcn_readfirstlane(x); }
__device__ __forceinline__ v4f mfma(v4u a, v4u b, v4f c) {
  return __builtin_amdgcn_mfma_f32_16x16x32_bf16(__builtin_bit_cast(v8bf, a), __builtin_bit_cast(v8bf, b), c, 0, 0, 0);
}
__device__ __forceinline__ unsigned pk2(float lo, float hi) {
  v2f x = {lo, hi};
  return __builtin_bit_cast(unsigned, __builtin_convertvector(x, v2bf));
}
__device__ __forceinline__ v2u trread(LAS char* p) {
  return __builtin_bit_cast(v2u, __builtin_amdgcn_ds_read_tr16_b64_v4i16((LAS v4s*)p));
}
__device__ __forceinline__ float xg_max(float x) {
  auto s = __builtin_amdgcn_permlane16_swap(__float_as_uint(x), __float_as_uint(x), false, false);
  x = fmaxf(__uint_as_float(s[0]), __uint_as_float(s[1]));
  auto t = __builtin_amdgcn_permlane32_swap(__float_as_uint(x), __float_as_uint(x), false, false);
  return fmaxf(__uint_as_float(t[0]), __uint_as_float(t[1]));
}
__device__ __forceinline__ float xg_sum(float x) {
  auto s = __builtin_amdgcn_permlane16_swap(__float_as_uint(x), __float_as_uint(x), false, false);
  x = __uint_as_float(s[0]) + __uint_as_float(s[1]);
  auto t = __builtin_amdgcn_permlane32_swap(__float_as_uint(x), __float_as_uint(x), false, false);
  return __uint_as_float(t[0]) + __uint_as_float(t[1]);
}
#define STAMP(k)
#define STAMPV(k, v)
#define LDS_WAIT() asm volatile("s_waitcnt lgkmcnt(0)" ::: "memory")
#define CFENCE() asm volatile("" ::: "memory")
#define PIN8(a) asm volatile("" : "+v"(a[0]), "+v"(a[1]), "+v"(a[2]), "+v"(a[3]), "+v"(a[4]), "+v"(a[5]), "+v"(a[6]), "+v"(a[7])::"memory")
template <int N> __device__ __forceinline__ void vm_wait() { asm volatile("s_waitcnt vmcnt(%c0)" ::"n"(N) : "memory"); }
__device__ __forceinline__ unsigned ctl_rd(unsigned addr) {
  unsigned v;
  asm volatile("ds_read_b32 %0, %1\n\ts_waitcnt lgkmcnt(0)" : "=v"(v) : "v"(addr) : "memory");
  return rfl(v);
}
__device__ __forceinline__ void ctl_wr(unsigned addr, unsigned val) {
  asm volatile("s_mov_b64 exec, 1\n\tds_write_b32 %0, %1\n\ts_mov_b64 exec, -1" ::"v"(addr), "v"(val) : "memory");
}
__device__ __forceinline__ unsigned ctl_add(unsigned addr) {
  unsigned v;
  asm volatile("s_mov_b64 exec, 1\n\tds_add_rtn_u32 %0, %1, %2\n\ts_mov_b64 exec, -1\n\ts_waitcnt lgkmcnt(0)" : "=&v"(v) : "v"(addr), "v"(1u) : "memory");
  return rfl(v);
}
template <int SLP> __device__ __forceinline__ unsigned ctl_wait(unsigned addr, unsigned need) {
  unsigned v = ctl_rd(addr);
  for (unsigned sp = 0; v < need && sp < SPIN_CAP; ++sp) {
    __builtin_amdgcn_s_sleep(SLP);
    v = ctl_rd(addr);
  }
  return v;
}

#define PUBL(k) ".if %c[b] == " #k "\n\ts_mov_b64 exec, 1\n\tds_write_b32 %[fa], v23\n\ts_mov_b64 exec, -1\n\t.endif\n\t"
#define PC(k, VO, IMM) "s_waitcnt vmcnt(%c[w])\n\t" PUBL(k) ".if %c[nt]\n\tbuffer_load_dwordx4 " VO ", %[rs], %[so] offen offset:" IMM " nt lds\n\t.else\n\tbuffer_load_dwordx4 " VO ", %[rs], %[so] offen offset:" IMM " lds\n\t.endif\n\t"
template <int W, int NTF = 1>
__device__ __forceinline__ void dma_unit(__amdgpu_buffer_rsrc_t rs, unsigned soff, unsigned& m0b, unsigned& u, unsigned& lim, unsigned vo0, unsigned vo1,
                                         unsigned vctl, unsigned faddr) {
  unsigned sp;
  asm volatile(
      "s_cmp_lt_u32 %[u], %[lim]\n\t"
      "s_cbranch_scc1 2f\n\t"
      "s_mov_b32 %[sp], 0\n"
      "1:\n\t"
      "ds_read_b32 v20, %[vc] offset:16\n\tds_read_b32 v21, %[vc] offset:20\n\tds_read_b32 v22, %[vc] offset:24\n\t"
      "s_waitcnt lgkmcnt(0)\n\t"
      "v_min3_u32 v20, v20, v21, v22\n\t"
      "s_add_u32 %[sp], %[sp], 1\n\t"
      "v_readfirstlane_b32 %[lim], v20\n\t"
      "s_add_u32 %[lim], %[lim], %c[nr]\n\t"
      "s_cmp_lt_u32 %[u], %[lim]\n\t"
      "s_cbranch_scc1 2f\n\t"
      "s_sleep 1\n\t"
      "s_cmp_lt_u32 %[sp], %c[cap]\n\t"
      "s_cbranch_scc1 1b\n"
      "2:\n\t"
      "s_mov_b32 m0, %[m0b]\n\t"
      "v_mov_b32 v23, %[u]\n\t"
      "v_subrev_u32 v23, %c[a], v23\n\t"
      PC(0, "%[vo0]", "0")
      "ds_read_b32 v20, %[vc] offset:16\n\tds_read_b32 v21, %[vc] offset:20\n\tds_read_b32 v22, %[vc] offset:24\n\t"
      "s_add_u32 m0, m0, 32\n\t"
      PC(1, "%[vo0]", "1024")
      "s_add_u32 m0, m0, 32\n\t"
      PC(2, "%[vo0]", "2048")
      "s_add_u32 m0, m0, 32\n\t"
      PC(3, "%[vo0]", "3072")
      "s_add_u32 m0, m0, 0x1020\n\t"
      PC(4, "%[vo1]", "0")
      "s_waitcnt lgkmcnt(0)\n\t"
      "v_min3_u32 v20, v20, v21, v22\n\t"
      "s_add_u32 m0, m0, 32\n\t"
      PC(5, "%[vo1]", "1024")
      "v_readfirstlane_b32 %[lim], v20\n\t"
      "s_add_u32 %[m0b], %[m0b], %c[unit]\n\t"
      "s_add_u32 m0, m0, 32\n\t"
      PC(6, "%[vo1]", "2048")
      "s_add_u32 %[lim], %[lim], %c[nr]\n\t"
      "s_cmp_lt_u32 %[m0b], %c[ring]\n\t"
      "s_cselect_b32 %[m0b], %[m0b], 0\n\t"
      "s_add_u32 %[u], %[u], 1\n\t"
      "s_add_u32 m0, m0, 32\n\t"
      PC(7, "%[vo1]", "3072")
      : [m0b] "+s"(m0b), [u] "+s"(u), [lim] "+s"(lim), [sp] "=&s"(sp)
      : [nt] "n"(NTF), [w] "n"(W - 1), [a] "n"((W - 1) / 8), [b] "n"((W - 1) % 8), [nr] "n"(NR), [cap] "n"(SPIN_CAP), [unit] "n"(UNIT), [ring] "n"(NR * UNIT), [rs] "s"(rs),
        [so] "s"(soff), [vo0] "v"(vo0), [vo1] "v"(vo1), [vc] "v"(vctl), [fa] "v"(faddr)
      : "memory", "scc", "v20", "v21", "v22", "v23");
}

__global__ void __launch_bounds__(NTHR, 3)
mha_block_kernel(const unsigned short* __restrict__ x, const unsigned short* __restrict__ kc, const unsigned short* __restrict__ vc,
                 const unsigned short* __restrict__ wq, const unsigned short* __restrict__ wo, unsigned short* __restrict__ out,
                 unsigned char* __restrict__ ws) {
  extern __shared__ __attribute__((aligned(16))) char smem[];
  LAS char* L = (LAS char*)smem;
  const unsigned lbase = (unsigned)(size_t)L;
  const int lane = threadIdx.x & 63, wave = rfl(threadIdx.x >> 6);
  const int g = lane >> 4, c = lane & 15;
  const int j = blockIdx.x, hq = (j >> 3) & 3, h = MAP1 ? 4 * (j & 7) + hq : (j & 31), bp = j >> 5;
  const unsigned ctl = lbase + L_CTRL;
  const __amdgpu_buffer_rsrc_t rsO = __builtin_amdgcn_make_buffer_rsrc((void*)ws, 0, WS_USED, 0x00020000);

  if (wave == 0) {
    __builtin_amdgcn_s_setprio(3);
    __builtin_amdgcn_s_waitcnt(0x0F70);
    const unsigned long long tl0 = __builtin_amdgcn_s_memrealtime();
    static_assert(WIN <= 24 && WIN > 16 && WIN0 >= WIN && WIN0 <= 56, "publish ladder below; published counts never decrease");
    const unsigned vo0 = lane * 16, vo1 = lane * 16 + 4096;
    const unsigned sbase = (unsigned)(bp * NH + h) * (SK * DH * 2);
    const __amdgpu_buffer_rsrc_t rsK = __builtin_amdgcn_make_buffer_rsrc((void*)kc, 0, NB * NH * SK * DH * 2, 0x00020000);
    const __amdgpu_buffer_rsrc_t rsV = __builtin_amdgcn_make_buffer_rsrc((void*)vc, 0, NB * NH * SK * DH * 2, 0x00020000);
    unsigned m0b = 0, u = 0, lim = NR;
    dma_unit<WIN0>(rsK, sbase, m0b, u, lim, vo0, vo1, ctl, ctl + 252);
    lim = NR;
    dma_unit<WIN0>(rsV, sbase, m0b, u, lim, vo0, vo1, ctl, ctl + 252);
    lim = NR;
    __builtin_amdgcn_s_barrier();
    int pubmin = (WIN0 - 1) / 8;
    bool paused = false;
#pragma unroll 1
    for (int n = 1; n < N0; ++n) {
      if (!paused && (n >= NI || ctl_rd(ctl + 4 * C_R1) >= R1_GO)) {
        paused = true;
        vm_wait<0>(); ctl_wr(ctl + 4 * C_FULL, 2 * n);
        pubmin += 2 * n;
        ctl_wait<1>(ctl + 4 * C_QI, QI_GO);
      }
      dma_unit<WIN0>(rsK, sbase + (unsigned)n * 8192u, m0b, u, lim, vo0, vo1, ctl, 2 * n < pubmin ? ctl + 252 : ctl + 4 * C_FULL);
      dma_unit<WIN0>(rsV, sbase + (unsigned)n * 8192u, m0b, u, lim, vo0, vo1, ctl, 2 * n + 1 < pubmin ? ctl + 252 : ctl + 4 * C_FULL);
    }
    int nb = 0;
#pragma unroll 1
    for (int n = N0; n < 128 - NE; ++n) {
      const unsigned soff = sbase + (unsigned)(n >> 6) * (8u * NH * SK * DH * 2) + (unsigned)(n & 63) * 8192u;
      if (n == CK_N) {
        const int late = (int)(unsigned)(__builtin_amdgcn_s_memrealtime() - tl0) - CK_T;
        nb = CK_N + (late > 0 ? (late * CK_G) >> 10 : 0);
      }
      if (n < nb) {
        dma_unit<WINB>(rsK, soff, m0b, u, lim, vo0, vo1, ctl, ctl + 4 * C_FULL);
        dma_unit<WINB>(rsV, soff, m0b, u, lim, vo0, vo1, ctl, ctl + 4 * C_FULL);
      } else {
        dma_unit<WIN>(rsK, soff, m0b, u, lim, vo0, vo1, ctl, ctl + 4 * C_FULL);
        dma_unit<WIN>(rsV, soff, m0b, u, lim, vo0, vo1, ctl, ctl + 4 * C_FULL);
      }
    }
#pragma unroll 1
    for (int k = 0; k < 2 * NE; ++k) {
      const int e = k - (2 * NE - 6);
      const bool isv = e < 0 ? (k & 1) : e >= 3;
      const int n = e < 0 ? 128 - NE + (k >> 1) : 125 + (e >= 3 ? e - 3 : e);
      const __amdgpu_buffer_rsrc_t rs = __builtin_amdgcn_make_buffer_rsrc((void*)(isv ? vc : kc), 0, NB * NH * SK * DH * 2, 0x00020000);
      dma_unit<WINE>(rs, sbase + (8u * NH * SK * DH * 2) + (unsigned)(n & 63) * 8192u, m0b, u, lim, vo0, vo1, ctl,
                     k < (WINE - 1) / 8 - (WIN - 1) / 8 ? ctl + 252 : ctl + 4 * C_FULL);
    }
    ctl_wr(ctl + 4 * C_LDONE, 1u);
    if (WINE > 48) { vm_wait<48>(); ctl_wr(ctl + 4 * C_FULL, NUNIT - 6); }
    if (WINE > 40) { vm_wait<40>(); ctl_wr(ctl + 4 * C_FULL, NUNIT - 5); }
    if (WINE > 32) { vm_wait<32>(); ctl_wr(ctl + 4 * C_FULL, NUNIT - 4); }
    if (WINE > 24) { vm_wait<24>(); ctl_wr(ctl + 4 * C_FULL, NUNIT - 3); }
    vm_wait<16>(); ctl_wr(ctl + 4 * C_FULL, NUNIT - 2);
    vm_wait<8>(); ctl_wr(ctl + 4 * C_FULL, NUNIT - 1);
    vm_wait<0>(); ctl_wr(ctl + 4 * C_FULL, NUNIT);
  } else if ((wave & 3) == 0 || wave == 1) {
    const int aw = wave == 1 ? 2 : (wave >> 2) - 1;
    if (aw == 0) { *(LAS unsigned*)(L + L_CTRL + 4 * lane) = 0u; LDS_WAIT(); }
    __builtin_amdgcn_s_barrier();
    const unsigned koff = (c & 7) * PIECE + (c >> 3) * 256 + g * 16;
    const unsigned voff = (4 * (g & 1) + (c >> 2)) * PIECE + (g >> 1) * 256 + (c & 3) * 16;
    const int obase = W_O + ((bp >> 1) * 8 + (j & 7)) * 16384 + hq * 4096 + g * 256 + ((bp & 1) * 8 + (c & 7)) * 16;
    v4u oA[4];
#pragma unroll
    for (int s = 0; s < 4; ++s) oA[s] = v4u{0u, 0u, 0u, 0u};
    {
      const v4u sv = {SENT, SENT, SENT, SENT};
      if (aw == 0) {
        const unsigned long long tag = __hip_atomic_load((GAS unsigned long long*)(ws + W_EPOCH), __ATOMIC_RELAXED, __HIP_MEMORY_SCOPE_AGENT) + 1ull;
#pragma unroll
        for (int s = 0; s < 4; ++s) __builtin_amdgcn_raw_buffer_store_b128(sv, rsO, obase + (c >> 3) * (4 * 8 * 16384), s * 1024, O_ST_AUX);
        if (lane == 0) *(LAS v2u*)(L + L_CTRL + 4 * C_EP) = v2u{(unsigned)tag, (unsigned)(tag >> 32)};
      } else {
#pragma unroll
        for (int k = 0; k < 8; ++k) __builtin_amdgcn_raw_buffer_store_b128(sv, rsO, lane * 16, W_P + (((j >> 3) * 2 + aw - 1) * 64 + k * 8 + (j & 7)) * 1024, P_ST_AUX);
      }
    }
    v4u kf[8], vf[8];
    unsigned full = ctl_wait<1>(ctl + 4 * C_FULL, 2 * aw + 2);
    {
      CFENCE();
      LAS char* kb = L + (2 * aw) * UNIT + koff;
      LAS char* vb = L + (2 * aw + 1) * UNIT + voff;
#pragma unroll
      for (int i = 0; i < 8; ++i) kf[i] = *(LAS v4u*)(kb + (i >> 2) * 512 + (i & 3) * 64);
#pragma unroll
      for (int d = 0; d < 8; ++d) {
        const v2u a = trread(vb + (d >> 1) * 64 + (d & 1) * 8), b = trread(vb + 512 + (d >> 1) * 64 + (d & 1) * 8);
        vf[d] = v4u{a[0], a[1], b[0], b[1]};
      }
      LDS_WAIT();
      ctl_wr(ctl + 4 * (C_REL + aw), 2 * aw + 2 * NATT);
      PIN8(kf); PIN8(vf);
      if (aw == 0) STAMP(14);
      ctl_wait<1>(ctl + 4 * C_Q2, 4);
      CFENCE();
    }
    v4u qf[4];
#pragma unroll
    for (int s = 0; s < 4; ++s) {
      LAS char* qp = L + L_SCR + g * 2048 + (s * 16 + c) * 16;
      const v4f lo = (*(LAS v4f*)(qp) + *(LAS v4f*)(qp + 8192)) + (*(LAS v4f*)(qp + 16384) + *(LAS v4f*)(qp + 24576));
      const v4f hi = (*(LAS v4f*)(qp + 1024) + *(LAS v4f*)(qp + 9216)) + (*(LAS v4f*)(qp + 17408) + *(LAS v4f*)(qp + 25600));
      qf[s] = v4u{pk2(lo[0], lo[1]), pk2(lo[2], lo[3]), pk2(hi[0], hi[1]), pk2(hi[2], hi[3])};
      if (s & 1) asm volatile("" : "+v"(qf[s - 1]), "+v"(qf[s])::"memory");
    }
    if (aw == 0) STAMP(4);
    float m = -INFINITY, l = 0.f;
    v4f acc[8];
#pragma unroll
    for (int d = 0; d < 8; ++d) acc[d] = v4f{0.f, 0.f, 0.f, 0.f};
#define TA(k)
#define UK(n) ((n) < 125 ? 2 * (n) : 125 + (n))
#pragma unroll 1
    for (int n = aw; n < 128; n += NATT) {
      const int t = n & 63, ph = n >> 6;
      const unsigned uk = UK(n), uv = n < 125 ? uk + 1 : uk + 3;
      if (n >= NATT) {
        TA(0);
        if (full < uk + 1) full = ctl_wait<0>(ctl + 4 * C_FULL, uk + 1);
        CFENCE();
        TA(1);
        LAS char* kb = L + (uk % NR) * UNIT + koff;
#pragma unroll
        for (int i = 0; i < 8; ++i) kf[i] = *(LAS v4u*)(kb + (i >> 2) * 512 + (i & 3) * 64);
        PIN8(kf);
        ctl_wr(ctl + 4 * (C_REL + aw), uv);
        __builtin_amdgcn_sched_barrier(0);
      }
      v4f s0 = {0.f, 0.f, 0.f, 0.f}, s1 = {0.f, 0.f, 0.f, 0.f};
#pragma unroll
      for (int s = 0; s < 4; ++s) { s0 = mfma(kf[s], qf[s], s0); s1 = mfma(kf[4 + s], qf[s], s1); }
      float mx = fmaxf(fmaxf(fmaxf(s0[0], s0[1]), fmaxf(s0[2], s0[3])), fmaxf(fmaxf(s1[0], s1[1]), fmaxf(s1[2], s1[3])));
      mx = xg_max(mx);
      const float mn = fmaxf(m, mx);
      const float alpha = __builtin_amdgcn_exp2f((m - mn) * CSC);
      const float mc = mn * CSC;
      m = mn;
      float p[8];
#pragma unroll
      for (int i = 0; i < 4; ++i) { p[i] = __builtin_amdgcn_exp2f(fmaf(s0[i], CSC, -mc)); p[4 + i] = __builtin_amdgcn_exp2f(fmaf(s1[i], CSC, -mc)); }
      l = fmaf(l, alpha, ((p[0] + p[1]) + (p[2] + p[3])) + ((p[4] + p[5]) + (p[6] + p[7])));
      v4u pf = {pk2(p[0], p[1]), pk2(p[2], p[3]), pk2(p[4], p[5]), pk2(p[6], p[7])};
#pragma unroll
      for (int d = 0; d < 8; ++d) acc[d] *= alpha;
      const bool mine = (c >> 3) == ph;
      LAS char* r0 = L + L_MRG;
      LAS char* r1 = L + L_MRG + MRG_REC;
      float f0 = 0.f, f1 = 0.f;
      if (t == 63) {
        ctl_wait<0>(ctl + 4 * (C_ML + ph), 2);
        CFENCE();
        const v2f a0 = *(LAS v2f*)(r0 + 8192 + lane * 8), a1 = *(LAS v2f*)(r1 + 8192 + lane * 8);
        const float M = fmaxf(m, fmaxf(a0[0], a1[0]));
        const float w = __builtin_amdgcn_exp2f((m - M) * CSC), w0 = __builtin_amdgcn_exp2f((a0[0] - M) * CSC), w1 = __builtin_amdgcn_exp2f((a1[0] - M) * CSC);
        const float inv = 1.0f / xg_sum(fmaf(w, l, fmaf(w0, a0[1], w1 * a1[1])));
        l = w * inv; f0 = w0 * inv; f1 = w1 * inv;
        ctl_wait<0>(ctl + 4 * (C_MRG + 2 * ph), 1);
        CFENCE();
#pragma unroll
        for (int d = 0; d < 8; ++d) kf[d] = __builtin_bit_cast(v4u, *(LAS v4f*)(r0 + d * 1024 + lane * 16) * f0);
        PIN8(kf);
      } else if (t >= 61) {
        if (mine) *(LAS v2f*)(L + L_MRG + (t - 61) * MRG_REC + 8192 + lane * 8) = v2f{m, l};
        LDS_WAIT();
        ctl_add(ctl + 4 * (C_ML + ph));
      }
      asm volatile("" : "+v"(pf), "+v"(l), "+v"(f0), "+v"(f1));
      PIN8(acc);
      if (n >= NATT) {
        TA(2);
        if (full < uv + 1) full = ctl_wait<0>(ctl + 4 * C_FULL, uv + 1);
        CFENCE();
        TA(3);
        LAS char* vb = L + (uv % NR) * UNIT + voff;
#pragma unroll
        for (int d = 0; d < 8; ++d) {
          const v2u a = trread(vb + (d >> 1) * 64 + (d & 1) * 8), b = trread(vb + 512 + (d >> 1) * 64 + (d & 1) * 8);
          vf[d] = v4u{a[0], a[1], b[0], b[1]};
        }
        PIN8(vf);
        ctl_wr(ctl + 4 * (C_REL + aw), UK(n + NATT));
        __builtin_amdgcn_sched_barrier(0);
      }
#pragma unroll
      for (int d = 0; d < 8; ++d) acc[d] = mfma(vf[d], pf, acc[d]);
      TA(4);
      if (n >= 6 * NATT && n < 7 * NATT) { vm_wait<0>(); ctl_add(ctl + 4 * C_CLR); }
      if (n == OA_AT) {
        ctl_wait<1>(ctl + 4 * C_CLR, NATT);
        const v2u tg = *(LAS v2u*)(L + L_CTRL + 4 * C_EP);
        if (lane == 0) __hip_atomic_store((GAS unsigned long long*)(ws + W_CF) + FSTR * j, ((unsigned long long)tg[1] << 32) | tg[0], __ATOMIC_RELAXED, __HIP_MEMORY_SCOPE_AGENT);
        if (c < 8) {
#pragma unroll
          for (int s = 0; s < 4; ++s) __builtin_amdgcn_raw_buffer_store_b128(oA[s], rsO, obase + s * 1024, 0, O_ST_AUX);
        }
      }
      if (t >= 61) {
        if (t != 63) {
          LAS char* rec = L + L_MRG + (t - 61) * MRG_REC;
          if (mine) {
#pragma unroll
            for (int d = 0; d < 8; ++d) *(LAS v4f*)(rec + d * 1024 + lane * 16) = acc[d];
          }
          LDS_WAIT();
          ctl_wr(ctl + 4 * (C_MRG + 2 * ph + t - 61), 1u);
        } else {
          ctl_wait<0>(ctl + 4 * (C_MRG + 2 * ph + 1), 1);
          CFENCE();
#pragma unroll
          for (int d = 0; d < 8; ++d) vf[d] = *(LAS v4u*)(r1 + d * 1024 + lane * 16);
#pragma unroll
          for (int s = 0; s < 4; ++s) {
            const v4f x0 = __builtin_elementwise_fma(__builtin_bit_cast(v4f, vf[2 * s]), v4f{f1, f1, f1, f1}, __builtin_elementwise_fma(acc[2 * s], v4f{l, l, l, l}, __builtin_bit_cast(v4f, kf[2 * s])));
            const v4f x1 = __builtin_elementwise_fma(__builtin_bit_cast(v4f, vf[2 * s + 1]), v4f{f1, f1, f1, f1}, __builtin_elementwise_fma(acc[2 * s + 1], v4f{l, l, l, l}, __builtin_bit_cast(v4f, kf[2 * s + 1])));
            v4u o = {pk2(x0[0], x0[1]), pk2(x0[2], x0[3]), pk2(x1[0], x1[1]), pk2(x1[2], x1[3])};
#pragma unroll
            for (int k = 0; k < 4; ++k) o[k] = min(o[k], SENT - 1u);
            if (ph == 0) oA[s] = o;
            else if (mine) __builtin_amdgcn_raw_buffer_store_b128(o, rsO, obase + s * 1024, 4 * 8 * 16384, O_ST_AUX);
          }
          STAMP(5 + ph);
        }
        m = -INFINITY; l = 0.f;
#pragma unroll
        for (int d = 0; d < 8; ++d) acc[d] = v4f{0.f, 0.f, 0.f, 0.f};
      }
      TA(5);
    }
  } else {
    const int gw = wave - 2 - (wave >> 2);
    {
      const __amdgpu_buffer_rsrc_t rsQ = __builtin_amdgcn_make_buffer_rsrc((void*)wq, 0, ABL_NOQ ? 0 : DM * NH * DH * 2, 0x00020000);
      const int wvo = (8 * g * (NH * DH) + 8 * c) * 2, wso = (128 * gw * (NH * DH) + h * DH) * 2;
      const int xrow = ((c < 8 ? bp : bp + 8) * SQ + (c & 7));
      const GAS char* xb = (const GAS char*)x + ((size_t)xrow * DM + 128 * gw + 8 * g) * 2;
#define WQ_ROW(t, jj) ((32 * (((t) + (bp >> 1)) & 3) + ((jj) ^ (4 * (bp & 1)))) * (NH * DH * 2))
      v4u xf[4], w[4][8];
      __builtin_amdgcn_s_barrier();
      __builtin_amdgcn_sched_barrier(0);
      {
#pragma unroll
        for (int jj = 0; jj < 4; ++jj) w[0][jj] = __builtin_amdgcn_raw_buffer_load_b128(rsQ, wvo, wso + WQ_ROW(0, jj), AUX_NT);
        const unsigned pf = *(const GAS unsigned*)(xb + (hq & 1) * 128);
        asm volatile("" ::"v"(pf));
        vm_wait<0>();
        ctl_add(ctl + 4 * C_R1);
        __builtin_amdgcn_s_sleep(Q_GAP);
        __builtin_amdgcn_sched_barrier(0);
      }
#pragma unroll
      for (int jj = 4; jj < 8; ++jj) w[0][jj] = __builtin_amdgcn_raw_buffer_load_b128(rsQ, wvo, wso + WQ_ROW(0, jj), AUX_NT);
#pragma unroll
      for (int tt = 0; tt < 4; ++tt) xf[tt] = *(const GAS v4u*)(xb + ((tt + (bp >> 1)) & 3) * 64);
#pragma unroll
      for (int t = 1; t < 3; ++t)
#pragma unroll
        for (int jj = 0; jj < 8; ++jj) w[t][jj] = __builtin_amdgcn_raw_buffer_load_b128(rsQ, wvo, wso + WQ_ROW(t, jj), AUX_NT);
      __builtin_amdgcn_sched_barrier(0);
      ctl_add(ctl + 4 * C_QI);
      if (bp & 1) {
#pragma unroll
        for (int t = 0; t < 4; ++t) xf[t] = v4u{xf[t][2], xf[t][3], xf[t][0], xf[t][1]};
      }
      v4f qa[8];
#pragma unroll
      for (int d = 0; d < 8; ++d) qa[d] = v4f{0.f, 0.f, 0.f, 0.f};
#pragma unroll
      for (int t = 0; t < 4; ++t) {
#pragma unroll
        for (int dd = 0; dd < 8; ++dd) {
          v4u f;
#pragma unroll
          for (int v = 0; v < 4; ++v) f[v] = __builtin_amdgcn_perm(w[t][2 * v + 1][dd >> 1], w[t][2 * v][dd >> 1], (dd & 1) ? 0x07060302u : 0x05040100u);
          qa[dd] = mfma(f, xf[t], qa[dd]);
          if (dd & 1) __builtin_amdgcn_sched_barrier(0);
        }
        if (t == 0) {
#pragma unroll
          for (int jj = 0; jj < 8; ++jj) {
            w[3][jj] = __builtin_amdgcn_raw_buffer_load_b128(rsQ, wvo, wso + WQ_ROW(3, jj), AUX_NT);
            __builtin_amdgcn_sched_barrier(0);
          }
        }
      }
      STAMP(16 + gw);
      LAS char* part = L + L_SCR + (gw & 3) * 8192 + lane * 16;
      if (gw >= 4) {
        ctl_wait<0>(ctl + 4 * C_Q1, 4);
        CFENCE();
      }
#pragma unroll
      for (int i = 0; i < 4; ++i) {
        v4f lo = {qa[0][i], qa[1][i], qa[2][i], qa[3][i]}, hi = {qa[4][i], qa[5][i], qa[6][i], qa[7][i]};
        if (gw >= 4) { lo += *(LAS v4f*)(part + i * 2048); hi += *(LAS v4f*)(part + i * 2048 + 1024); }
        *(LAS v4f*)(part + i * 2048) = lo;
        *(LAS v4f*)(part + i * 2048 + 1024) = hi;
      }
      LDS_WAIT();
      ctl_add(ctl + 4 * (gw >= 4 ? C_Q2 : C_Q1));
      if (gw == 0) STAMP(7);
      if (gw == 7) STAMP(8);
    }
    const int xc = j & 7, idx = j >> 3, ct = idx & 7, rg = idx >> 3;
    unsigned hb = 0;
#define HBAR(SLP) do { hb += NHELP; ctl_add(ctl + 4 * C_HB); ctl_wait<SLP>(ctl + 4 * C_HB, hb); CFENCE(); } while (0)
    const int pdst = W_P + (idx * 128 + gw * 8 + xc) * 1024;
    v4u wf[16];
    {
      const __amdgpu_buffer_rsrc_t rsW = __builtin_amdgcn_make_buffer_rsrc((void*)wo, 0, ABL_NOWO ? 0 : DM * NH * DH * 2, 0x00020000);
      ctl_wait<32>(ctl + 4 * C_FULL, 40 + 2 * gw);
#pragma unroll
      for (int ks = 0; ks < 16; ++ks) {
        wf[ks] = __builtin_amdgcn_raw_buffer_load_b128(rsW, g * 2048 + c * 16, ((MAP1 ? 4 * xc + (ks >> 2) : (ks >> 2) * 8 + xc) * 128 + (ks & 3) * 32 + 4 * gw) * 2048 + ct * 256, AUX_NT);
        __builtin_amdgcn_s_sleep(WF_SLEEP);
        __builtin_amdgcn_sched_barrier(0);
      }
      LAS char* sw = L + L_OB + gw * PIECE + lane * 16;
      LAS char* sr = L + L_OB + 2 * g * PIECE + (c >> 2) * 256 + gw * 32 + (c & 3) * 8;
#pragma unroll
      for (int r = 0; r < 8; ++r) {
        *(LAS v4u*)(sw) = wf[2 * r];
        *(LAS v4u*)(sw + 8 * PIECE) = wf[2 * r + 1];
        LDS_WAIT();
        HBAR(2);
        const v2u a0 = trread(sr), b0 = trread(sr + PIECE), a1 = trread(sr + 8 * PIECE), b1 = trread(sr + 9 * PIECE);
        wf[2 * r] = v4u{a0[0], a0[1], b0[0], b0[1]};
        wf[2 * r + 1] = v4u{a1[0], a1[1], b1[0], b1[1]};
        LDS_WAIT();
        asm volatile("" : "+v"(wf[2 * r]), "+v"(wf[2 * r + 1])::"memory");
        HBAR(2);
      }
    }
    const v2u tagv = *(LAS v2u*)(L + L_CTRL + 4 * C_EP);
    const unsigned long long tag = ((unsigned long long)tagv[1] << 32) | tagv[0];
    if (gw == 7) STAMP(9);
    ctl_wait<16>(ctl + 4 * C_FULL, NUNIT - 16);
    ctl_wait<2>(ctl + 4 * C_LDONE, 1);
    if (gw == 7) STAMP(10);
    {
      const int src = lane < 8 ? idx * 8 + lane : (2 * rg + (lane & 1)) * 32 + (gw >> 1) * 8 + xc;
      GAS unsigned long long* fl = (GAS unsigned long long*)(ws + W_CF) + FSTR * src;
      for (unsigned sp = 0; sp < (1u << 16); ++sp) {
        if (__all(__hip_atomic_load(fl, __ATOMIC_RELAXED, __HIP_MEMORY_SCOPE_AGENT) == tag)) break;
        __builtin_amdgcn_s_sleep(G1_SLEEP);
      }
      __builtin_amdgcn_fence(__ATOMIC_ACQUIRE, "wavefront");
    }
    if (gw == 7) STAMP(11);
#pragma unroll 1
    for (int ph = 0; ph < 2; ++ph) {
      v4u f0, f1;
      for (unsigned sp = 0; sp < (1u << 16); ++sp) {
        CFENCE();
        f0 = __builtin_amdgcn_raw_buffer_load_b128(rsO, lane * 16, W_O + ((ph * 4 + rg) * 8 + xc) * 16384 + gw * 2048, AUX_SC1);
        f1 = __builtin_amdgcn_raw_buffer_load_b128(rsO, lane * 16, W_O + ((ph * 4 + rg) * 8 + xc) * 16384 + gw * 2048 + 1024, AUX_SC1);
        bool ok = true;
#pragma unroll
        for (int k = 0; k < 4; ++k) ok &= (f0[k] != SENT) & (f1[k] != SENT);
        if (__all(ok)) break;
        __builtin_amdgcn_s_sleep(G1_SLEEP);
      }
      *(LAS v4u*)(L + L_OB + gw * 2048 + lane * 16) = f0;
      *(LAS v4u*)(L + L_OB + gw * 2048 + 1024 + lane * 16) = f1;
      LDS_WAIT();
      HBAR(0);
      v4f oa[4];
#pragma unroll
      for (int k = 0; k < 4; ++k) oa[k] = v4f{0.f, 0.f, 0.f, 0.f};
#pragma unroll
      for (int f = 0; f < 16; ++f) oa[f & 3] = mfma(wf[f], *(LAS v4u*)(L + L_OB + f * 1024 + lane * 16), oa[f & 3]);
      v4u pt = __builtin_bit_cast(v4u, (oa[0] + oa[1]) + (oa[2] + oa[3]));
#pragma unroll
      for (int k = 0; k < 4; ++k) pt[k] = min(pt[k], SENT - 1u);
      __builtin_amdgcn_raw_buffer_store_b128(pt, rsO, lane * 16, pdst + ph * 65536, P_ST_AUX);
      if (gw == 7) STAMP(24 + ph);
      if (ph == 0) HBAR(0);
    }
    if (gw < 2) {
      v4u q[8];
      for (unsigned sp = 0; sp < (1u << 16); ++sp) {
        CFENCE();
        bool ok = true;
#pragma unroll
        for (int k = 0; k < 8; ++k) q[k] = __builtin_amdgcn_raw_buffer_load_b128(rsO, lane * 16, W_P + ((idx * 2 + gw) * 64 + xc * 8 + k) * 1024, AUX_SC1);
#pragma unroll
        for (int k = 0; k < 8; ++k) ok &= (q[k][0] != SENT) & (q[k][1] != SENT) & (q[k][2] != SENT) & (q[k][3] != SENT);
        if (__all(ok)) break;
        __builtin_amdgcn_s_sleep(1);
      }
      v4f s = __builtin_bit_cast(v4f, q[0]);
#pragma unroll
      for (int k = 1; k < 8; ++k) s += __builtin_bit_cast(v4f, q[k]);
      *(GAS v2u*)((GAS char*)out + ((size_t)(64 * gw + 16 * rg + c) * DM + 128 * ct + 16 * xc + 4 * g) * 2) = v2u{pk2(s[0], s[1]), pk2(s[2], s[3])};
      STAMP(12 + gw);
    } else if (gw == 2 && j == 0) {
      GAS unsigned long long* fl = (GAS unsigned long long*)(ws + W_CF) + FSTR * lane;
      for (unsigned sp = 0; sp < (1u << 16); ++sp) {
        bool ok = true;
#pragma unroll
        for (int k = 0; k < 4; ++k) ok &= __hip_atomic_load(fl + FSTR * 64 * k, __ATOMIC_RELAXED, __HIP_MEMORY_SCOPE_AGENT) == tag;
        if (__all(ok)) break;
        __builtin_amdgcn_s_sleep(32);
      }
      if (lane == 0) __hip_atomic_store((GAS unsigned long long*)(ws + W_EPOCH), tag, __ATOMIC_RELAXED, __HIP_MEMORY_SCOPE_AGENT);
    }
  }
}
}

extern "C" void kernel_launch(void* const* d_in, const int* in_sizes, int n_in, void* d_out, int out_size, void* d_ws, size_t ws_size,
                              hipStream_t stream) {
  (void)in_sizes; (void)n_in; (void)out_size; (void)ws_size;
  (void)hipFuncSetAttribute((const void*)mha_block_kernel, hipFuncAttributeMaxDynamicSharedMemorySize, LDS_BYTES);
  mha_block_kernel<<<NWG, NTHR, LDS_BYTES, stream>>>((const unsigned short*)d_in[0], (const unsigned short*)d_in[1], (const unsigned short*)d_in[2],
                                                     (const unsigned short*)d_in[3], (const unsigned short*)d_in[4], (unsigned short*)d_out,
                                                     (unsigned char*)d_ws);
}
```

```cpp
#define PTAG 0
#include <hip/hip_runtime.h>
#include <hip/hip_bf16.h>
#include <stdint.h>

#define GAS __attribute__((address_space(1)))
#define LAS __attribute__((address_space(3)))
typedef unsigned v4u __attribute__((ext_vector_type(4)));
typedef unsigned v2u __attribute__((ext_vector_type(2)));
typedef float v4f __attribute__((ext_vector_type(4)));
typedef float v2f __attribute__((ext_vector_type(2)));
typedef short v4s __attribute__((ext_vector_type(4)));
typedef __bf16 v8bf __attribute__((ext_vector_type(8)));
typedef __bf16 v2bf __attribute__((ext_vector_type(2)));

#define WIN 20
#define WIN0 24
#define U0 24
#define UI 3
#define EARLYQ 1
#define CLR_LATE 0
#define QI_GO 5
#define Q_GAP 4
#define WINB 28
#define CK_U 80
#define CK_T 2680
#define CK_G 160
#define WINE 32
#define UE 16
#define WF_SLEEP 16
#define G1_SLEEP 1
#define OA_AT 57

namespace {
constexpr int NB = 16, SQ = 8, DM = 1024, NH = 32, SK = 2048, DH = 128;
constexpr int NWG = 256, NTHR = 768, NATT = 3, NHELP = 8;
constexpr int PIECE = 1056;
constexpr int UNIT = 8 * PIECE;
constexpr int NR = 14;
constexpr int NUNIT = 256;
constexpr int AUX_NT = 2, AUX_SC1 = 16;
constexpr int L_CTRL = NR * UNIT;
constexpr int PREC = 544;
constexpr int L_PS = L_CTRL + 256;
constexpr int L_OB = L_PS;
constexpr int L_MRG = L_PS + 32 * PREC;
constexpr int L_ST = L_PS + 64 * PREC;
constexpr int L_STG = L_ST + 1024;
constexpr int LDS_BYTES = 160 * 1024;
static_assert(L_STG + 8 * PIECE <= LDS_BYTES && 16384 <= 32 * PREC && 32 * PREC + 8192 <= 48 * PREC && NR <= 14, "LDS map");
enum { C_FULL = 0, C_REL = 4, C_Q1 = 8, C_Q2 = 9, C_QI = 10, C_HB = 11, C_QR = 12, C_EP = 14, C_CLR = 16, C_ML = 18, C_MRG = 20, C_LDONE = 24 };
constexpr int W_EPOCH = 0, W_CF = 65536, FSTR = 16;
constexpr int W_O = 1 << 20;
constexpr int W_P = 2 << 20;
constexpr int WS_USED = 6 << 20;
constexpr unsigned SENT = 0xFFFFFFFFu;
constexpr float CSC = 0.08838834764831845f * 1.4426950408889634f;
constexpr unsigned SPIN_CAP = 1u << 22;

__device__ __forceinline__ unsigned rfl(unsigned x) { return __builtin_amdgcn_readfirstlane(x); }
__device__ __forceinline__ v4f mfma(v4u a, v4u b, v4f c) {
  return __builtin_amdgcn_mfma_f32_16x16x32_bf16(__builtin_bit_cast(v8bf, a), __builtin_bit_cast(v8bf, b), c, 0, 0, 0);
}
__device__ __forceinline__ unsigned pk2(float lo, float hi) {
  v2f x = {lo, hi};
  return __builtin_bit_cast(unsigned, __builtin_convertvector(x, v2bf));
}
__device__ __forceinline__ v2u trread(LAS char* p) {
  return __builtin_bit_cast(v2u, __builtin_amdgcn_ds_read_tr16_b64_v4i16((LAS v4s*)p));
}
__device__ __forceinline__ float xg_max(float x) {
  auto s = __builtin_amdgcn_permlane16_swap(__float_as_uint(x), __float_as_uint(x), false, false);
  x = fmaxf(__uint_as_float(s[0]), __uint_as_float(s[1]));
  auto t = __builtin_amdgcn_permlane32_swap(__float_as_uint(x), __float_as_uint(x), false, false);
  return fmaxf(__uint_as_float(t[0]), __uint_as_float(t[1]));
}
__device__ __forceinline__ float xg_sum(float x) {
  auto s = __builtin_amdgcn_permlane16_swap(__float_as_uint(x), __float_as_uint(x), false, false);
  x = __uint_as_float(s[0]) + __uint_as_float(s[1]);
  auto t = __builtin_amdgcn_permlane32_swap(__float_as_uint(x), __float_as_uint(x), false, false);
  return __uint_as_float(t[0]) + __uint_as_float(t[1]);
}
#define STAMP(k)
#define STAMPV(k, v)
#define LDS_WAIT() asm volatile("s_waitcnt lgkmcnt(0)" ::: "memory")
#define CFENCE() asm volatile("" ::: "memory")
#define PIN8(a) asm volatile("" : "+v"(a[0]), "+v"(a[1]), "+v"(a[2]), "+v"(a[3]), "+v"(a[4]), "+v"(a[5]), "+v"(a[6]), "+v"(a[7])::"memory")
template <int N> __device__ __forceinline__ void vm_wait() { asm volatile("s_waitcnt vmcnt(%c0)" ::"n"(N) : "memory"); }
__device__ __forceinline__ unsigned ctl_rd(unsigned addr) {
  unsigned v;
  asm volatile("ds_read_b32 %0, %1\n\ts_waitcnt lgkmcnt(0)" : "=v"(v) : "v"(addr) : "memory");
  return rfl(v);
}
__device__ __forceinline__ void ctl_wr(unsigned addr, unsigned val) {
  asm volatile("s_mov_b64 exec, 1\n\tds_write_b32 %0, %1\n\ts_mov_b64 exec, -1" ::"v"(addr), "v"(val) : "memory");
}
__device__ __forceinline__ unsigned ctl_add(unsigned addr) {
  unsigned v;
  asm volatile("s_mov_b64 exec, 1\n\tds_add_rtn_u32 %0, %1, %2\n\ts_mov_b64 exec, -1\n\ts_waitcnt lgkmcnt(0)" : "=&v"(v) : "v"(addr), "v"(1u) : "memory");
  return rfl(v);
}
template <int SLP> __device__ __forceinline__ unsigned ctl_wait(unsigned addr, unsigned need) {
  unsigned v = ctl_rd(addr);
  for (unsigned sp = 0; v < need && sp < SPIN_CAP; ++sp) {
    __builtin_amdgcn_s_sleep(SLP);
    v = ctl_rd(addr);
  }
  return v;
}

#define PUBL(k) ".if %c[b] == " #k "\n\ts_mov_b64 exec, 1\n\tds_write_b32 %[fa], v23\n\ts_mov_b64 exec, -1\n\t.endif\n\t"
#define PC(k, VO, IMM) "s_waitcnt vmcnt(%c[w])\n\t" PUBL(k) ".if %c[nt]\n\tbuffer_load_dwordx4 " VO ", %[rs], %[so] offen offset:" IMM " nt lds\n\t.else\n\tbuffer_load_dwordx4 " VO ", %[rs], %[so] offen offset:" IMM " lds\n\t.endif\n\t"
template <int W, int NTF = 1>
__device__ __forceinline__ void dma_unit(__amdgpu_buffer_rsrc_t rs, unsigned soff, unsigned& m0b, unsigned& u, unsigned& lim, unsigned vo0, unsigned vo1,
                                         unsigned vctl, unsigned faddr) {
  unsigned sp;
  asm volatile(
      "s_cmp_lt_u32 %[u], %[lim]\n\t"
      "s_cbranch_scc1 2f\n\t"
      "s_mov_b32 %[sp], 0\n"
      "1:\n\t"
      "ds_read_b32 v20, %[vc] offset:16\n\tds_read_b32 v21, %[vc] offset:20\n\tds_read_b32 v22, %[vc] offset:24\n\t"
      "s_waitcnt lgkmcnt(0)\n\t"
      "v_min3_u32 v20, v20, v21, v22\n\t"
      "s_add_u32 %[sp], %[sp], 1\n\t"
      "v_readfirstlane_b32 %[lim], v20\n\t"
      "s_add_u32 %[lim], %[lim], %c[nr]\n\t"
      "s_cmp_lt_u32 %[u], %[lim]\n\t"
      "s_cbranch_scc1 2f\n\t"
      "s_sleep 1\n\t"
      "s_cmp_lt_u32 %[sp], %c[cap]\n\t"
      "s_cbranch_scc1 1b\n"
      "2:\n\t"
      "s_mov_b32 m0, %[m0b]\n\t"
      "v_mov_b32 v23, %[u]\n\t"
      "v_subrev_u32 v23, %c[a], v23\n\t"
      PC(0, "%[vo0]", "0")
      "ds_read_b32 v20, %[vc] offset:16\n\tds_read_b32 v21, %[vc] offset:20\n\tds_read_b32 v22, %[vc] offset:24\n\t"
      "s_add_u32 m0, m0, 32\n\t"
      PC(1, "%[vo0]", "1024")
      "s_add_u32 m0, m0, 32\n\t"
      PC(2, "%[vo0]", "2048")
      "s_add_u32 m0, m0, 32\n\t"
      PC(3, "%[vo0]", "3072")
      "s_add_u32 m0, m0, 0x1020\n\t"
      PC(4, "%[vo1]", "0")
      "s_waitcnt lgkmcnt(0)\n\t"
      "v_min3_u32 v20, v20, v21, v22\n\t"
      "s_add_u32 m0, m0, 32\n\t"
      PC(5, "%[vo1]", "1024")
      "v_readfirstlane_b32 %[lim], v20\n\t"
      "s_add_u32 %[m0b], %[m0b], %c[unit]\n\t"
      "s_add_u32 m0, m0, 32\n\t"
      PC(6, "%[vo1]", "2048")
      "s_add_u32 %[lim], %[lim], %c[nr]\n\t"
      "s_cmp_lt_u32 %[m0b], %c[ring]\n\t"
      "s_cselect_b32 %[m0b], %[m0b], 0\n\t"
      "s_add_u32 %[u], %[u], 1\n\t"
      "s_add_u32 m0, m0, 32\n\t"
      PC(7, "%[vo1]", "3072")
      : [m0b] "+s"(m0b), [u] "+s"(u), [lim] "+s"(lim), [sp] "=&s"(sp)
      : [nt] "n"(NTF), [w] "n"(W - 1), [a] "n"((W - 1) / 8), [b] "n"((W - 1) % 8), [nr] "n"(NR), [cap] "n"(SPIN_CAP), [unit] "n"(UNIT), [ring] "n"(NR * UNIT), [rs] "s"(rs),
        [so] "s"(soff), [vo0] "v"(vo0), [vo1] "v"(vo1), [vc] "v"(vctl), [fa] "v"(faddr)
      : "memory", "scc", "v20", "v21", "v22", "v23");
}

__global__ void __launch_bounds__(NTHR, 3)
mha_block_kernel(const unsigned short* __restrict__ x, const unsigned short* __restrict__ kc, const unsigned short* __restrict__ vc,
                 const unsigned short* __restrict__ wq, const unsigned short* __restrict__ wo, unsigned short* __restrict__ out,
                 unsigned char* __restrict__ ws) {
  extern __shared__ __attribute__((aligned(16))) char smem[];
  LAS char* L = (LAS char*)smem;
  const unsigned lbase = (unsigned)(size_t)L;
  const int lane = threadIdx.x & 63, wave = rfl(threadIdx.x >> 6);
  const int g = lane >> 4, c = lane & 15;
  const int j = blockIdx.x, hq = (j >> 3) & 3, h = 4 * (j & 7) + hq, bp = j >> 5;
  const unsigned ctl = lbase + L_CTRL;
  const __amdgpu_buffer_rsrc_t rsO = __builtin_amdgcn_make_buffer_rsrc((void*)ws, 0, WS_USED, 0x00020000);

  if (wave == 0) {
    __builtin_amdgcn_s_setprio(3);
    __builtin_amdgcn_s_waitcnt(0x0F70);
    const unsigned long long tl0 = __builtin_amdgcn_s_memrealtime();
    const unsigned vo0 = lane * 16, vo1 = lane * 16 + 4096;
    const unsigned sbase = (unsigned)(bp * NH + h) * (SK * DH * 2);
    unsigned m0b = 0, u = 0, lim = NR;
    unsigned ub = 0;
#pragma unroll 1
    for (int run = 0; run < 4; ++run) {
      const __amdgpu_buffer_rsrc_t rs = __builtin_amdgcn_make_buffer_rsrc((void*)((run & 1) ? vc : kc), 0, NB * NH * SK * DH * 2, 0x00020000);
      unsigned soff = sbase + (unsigned)(run >> 1) * (8u * NH * SK * DH * 2);
#pragma unroll 1
      for (int t = 0; t < 64; ++t, soff += 8192u) {
        if (u < U0) {
          if (u == 2) __builtin_amdgcn_s_barrier();
          if (u == UI) {
            vm_wait<0>(); ctl_wr(ctl + 4 * C_FULL, UI);
            ctl_wait<1>(ctl + 4 * C_QI, QI_GO);
          }
          dma_unit<WIN0>(rs, soff, m0b, u, lim, vo0, vo1, ctl, u < UI + (WIN0 - 1) / 8 ? ctl + 252 : ctl + 4 * C_FULL);
          if (u <= 2) lim = NR;
        } else if (u >= NUNIT - UE) {
          dma_unit<WINE>(rs, soff, m0b, u, lim, vo0, vo1, ctl, ctl + 4 * C_FULL);
        } else {
          if (u == CK_U) {
            const int late = (int)(unsigned)(__builtin_amdgcn_s_memrealtime() - tl0) - CK_T;
            ub = CK_U + (late > 0 ? (late * CK_G) >> 10 : 0);
          }
          if (u < ub) dma_unit<WINB>(rs, soff, m0b, u, lim, vo0, vo1, ctl, ctl + 4 * C_FULL);
          else dma_unit<WIN>(rs, soff, m0b, u, lim, vo0, vo1, ctl, ctl + 4 * C_FULL);
        }
      }
    }
    ctl_wr(ctl + 4 * C_LDONE, 1u);
    if (WINE > 48) { vm_wait<48>(); ctl_wr(ctl + 4 * C_FULL, NUNIT - 6); }
    if (WINE > 40) { vm_wait<40>(); ctl_wr(ctl + 4 * C_FULL, NUNIT - 5); }
    if (WINE > 32) { vm_wait<32>(); ctl_wr(ctl + 4 * C_FULL, NUNIT - 4); }
    if (WINE > 24) { vm_wait<24>(); ctl_wr(ctl + 4 * C_FULL, NUNIT - 3); }
    vm_wait<16>(); ctl_wr(ctl + 4 * C_FULL, NUNIT - 2);
    vm_wait<8>(); ctl_wr(ctl + 4 * C_FULL, NUNIT - 1);
    vm_wait<0>(); ctl_wr(ctl + 4 * C_FULL, NUNIT);
  } else if ((wave & 3) == 0 || wave == 1) {
    const int aw = wave == 1 ? 2 : (wave >> 2) - 1;
    if (aw == 0) { *(LAS unsigned*)(L + L_CTRL + 4 * lane) = 0u; LDS_WAIT(); }
    __builtin_amdgcn_s_barrier();
    const unsigned koff = (c & 7) * PIECE + (c >> 3) * 256 + g * 16;
    const unsigned voff = (4 * (g & 1) + (c >> 2)) * PIECE + (g >> 1) * 256 + (c & 3) * 16;
    const int obase = W_O + ((bp >> 1) * 8 + (j & 7)) * 16384 + hq * 4096 + g * 256 + ((bp & 1) * 8 + (c & 7)) * 16;
    v4u oA[4];
#pragma unroll
    for (int s = 0; s < 4; ++s) oA[s] = v4u{0u, 0u, 0u, 0u};
    {
      const v4u sv = {SENT, SENT, SENT, SENT};
      if (CLR_LATE) ctl_wait<1>(ctl + 4 * C_QI, CLR_LATE);
      if (aw == 0) {
        const unsigned long long tag = __hip_atomic_load((GAS unsigned long long*)(ws + W_EPOCH), __ATOMIC_RELAXED, __HIP_MEMORY_SCOPE_AGENT) + 1ull;
#pragma unroll
        for (int s = 0; s < 4; ++s) __builtin_amdgcn_raw_buffer_store_b128(sv, rsO, obase + (c >> 3) * (4 * 8 * 16384), s * 1024, AUX_SC1);
        if (lane == 0) *(LAS v2u*)(L + L_CTRL + 4 * C_EP) = v2u{(unsigned)tag, (unsigned)(tag >> 32)};
      } else if (!PTAG) {
#pragma unroll
        for (int k = 0; k < 8; ++k) __builtin_amdgcn_raw_buffer_store_b128(sv, rsO, lane * 16, W_P + (((j >> 3) * 2 + aw - 1) * 64 + k * 8 + (j & 7)) * 1024, AUX_SC1);
      }
    }
    v4u kf[8], vf[8];
    unsigned full = ctl_wait<1>(ctl + 4 * C_FULL, aw + 1);
    CFENCE();
#pragma unroll
    for (int i = 0; i < 8; ++i) kf[i] = *(LAS v4u*)(L + aw * UNIT + koff + (i >> 2) * 512 + (i & 3) * 64);
    PIN8(kf);
    ctl_wr(ctl + 4 * (C_REL + aw), aw + NATT);
    full = ctl_wait<1>(ctl + 4 * C_FULL, aw + NATT + 1);
    CFENCE();
#pragma unroll
    for (int i = 0; i < 8; ++i) vf[i] = *(LAS v4u*)(L + (aw + NATT) * UNIT + koff + (i >> 2) * 512 + (i & 3) * 64);
    PIN8(vf);
    ctl_wr(ctl + 4 * (C_REL + aw), aw + 2 * NATT);
    if (aw == 0) STAMP(14);
    ctl_wait<1>(ctl + 4 * C_Q2, 4);
    CFENCE();
    v4u qf[4];
#pragma unroll
    for (int s = 0; s < 4; ++s) {
      LAS char* qp = L + L_PS + g * 2048 + (s * 16 + c) * 16;
      const v4f lo = (*(LAS v4f*)(qp) + *(LAS v4f*)(qp + 8192)) + (*(LAS v4f*)(qp + 16384) + *(LAS v4f*)(qp + 24576));
      const v4f hi = (*(LAS v4f*)(qp + 1024) + *(LAS v4f*)(qp + 9216)) + (*(LAS v4f*)(qp + 17408) + *(LAS v4f*)(qp + 25600));
      qf[s] = v4u{pk2(lo[0], lo[1]), pk2(lo[2], lo[3]), pk2(hi[0], hi[1]), pk2(hi[2], hi[3])};
      if (s & 1) asm volatile("" : "+v"(qf[s - 1]), "+v"(qf[s])::"memory");
    }
    ctl_add(ctl + 4 * C_QR);
    ctl_wait<0>(ctl + 4 * C_QR, NATT);
    if (aw == 0) STAMP(4);
#define TA(k)
    const unsigned poff = L_PS + (g * 8 + (c & 7)) * 16, aoff = L_PS + 512 + (c & 7) * 4;
#pragma unroll 1
    for (int ph = 0; ph < 2; ++ph) {
      const bool mine = (c >> 3) == ph;
      float m = -INFINITY, l = 0.f;
#pragma unroll 1
      for (int t = aw; t < 64; t += NATT) {
        const unsigned uk = 128 * ph + t;
        if (uk >= 2 * NATT) {
          if (full < uk + 1) full = ctl_wait<0>(ctl + 4 * C_FULL, uk + 1);
          CFENCE();
          LAS char* kb = L + (uk % NR) * UNIT + koff;
#pragma unroll
          for (int i = 0; i < 8; ++i) kf[i] = *(LAS v4u*)(kb + (i >> 2) * 512 + (i & 3) * 64);
          PIN8(kf);
          ctl_wr(ctl + 4 * (C_REL + aw), t + NATT < 64 ? uk + NATT : 128 * ph + 64 + aw);
          __builtin_amdgcn_sched_barrier(0);
        } else if (uk >= NATT) {
#pragma unroll
          for (int i = 0; i < 8; ++i) kf[i] = vf[i];
        }
        v4f s0 = {0.f, 0.f, 0.f, 0.f}, s1 = {0.f, 0.f, 0.f, 0.f};
#pragma unroll
        for (int s = 0; s < 4; ++s) { s0 = mfma(kf[s], qf[s], s0); s1 = mfma(kf[4 + s], qf[s], s1); }
        float mx = fmaxf(fmaxf(fmaxf(s0[0], s0[1]), fmaxf(s0[2], s0[3])), fmaxf(fmaxf(s1[0], s1[1]), fmaxf(s1[2], s1[3])));
        mx = xg_max(mx);
        const float mn = fmaxf(m, mx);
        const float alpha = __builtin_amdgcn_exp2f((m - mn) * CSC);
        const float mc = mn * CSC;
        m = mn;
        float p[8];
#pragma unroll
        for (int i = 0; i < 4; ++i) { p[i] = __builtin_amdgcn_exp2f(fmaf(s0[i], CSC, -mc)); p[4 + i] = __builtin_amdgcn_exp2f(fmaf(s1[i], CSC, -mc)); }
        l = fmaf(l, alpha, ((p[0] + p[1]) + (p[2] + p[3])) + ((p[4] + p[5]) + (p[6] + p[7])));
        if (mine) {
          *(LAS v4u*)(L + poff + t * PREC) = v4u{pk2(p[0], p[1]), pk2(p[2], p[3]), pk2(p[4], p[5]), pk2(p[6], p[7])};
          if (g == 0) *(LAS float*)(L + aoff + t * PREC) = alpha;
        }
        if (uk >= 6 * NATT && uk < 7 * NATT) { vm_wait<0>(); ctl_add(ctl + 4 * C_CLR); }
      }
      float f0 = 0.f, f1 = 0.f;
      if (aw == 0) {
        ctl_wait<0>(ctl + 4 * (C_ML + ph), 2);
        CFENCE();
        const v2f a0 = *(LAS v2f*)(L + L_ST + lane * 8), a1 = *(LAS v2f*)(L + L_ST + 512 + lane * 8);
        const float M = fmaxf(m, fmaxf(a0[0], a1[0]));
        const float w = __builtin_amdgcn_exp2f((m - M) * CSC), w0 = __builtin_amdgcn_exp2f((a0[0] - M) * CSC), w1 = __builtin_amdgcn_exp2f((a1[0] - M) * CSC);
        const float inv = 1.0f / xg_sum(fmaf(w, l, fmaf(w0, a0[1], w1 * a1[1])));
        l = w * inv; f0 = w0 * inv; f1 = w1 * inv;
      } else {
        if (mine) *(LAS v2f*)(L + L_ST + (aw - 1) * 512 + lane * 8) = v2f{m, l};
        LDS_WAIT();
        ctl_add(ctl + 4 * (C_ML + ph));
      }
      v4f acc[8];
#pragma unroll
      for (int d = 0; d < 8; ++d) acc[d] = v4f{0.f, 0.f, 0.f, 0.f};
      LAS char* rec = L + L_MRG + (g * 8 + (c & 7)) * 16;
#pragma unroll 1
      for (int t = aw; t < 64; t += NATT) {
        const unsigned uv = 128 * ph + 64 + t;
        v4u pf = *(LAS v4u*)(L + poff + t * PREC);
        const float alpha = *(LAS float*)(L + aoff + t * PREC);
#pragma unroll
        for (int d = 0; d < 8; ++d) acc[d] *= alpha;
        if (t == 63) {
          ctl_wait<0>(ctl + 4 * (C_MRG + 2 * ph), 1);
          CFENCE();
#pragma unroll
          for (int d = 0; d < 8; ++d) kf[d] = __builtin_bit_cast(v4u, *(LAS v4f*)(rec + d * 512) * f0);
          PIN8(kf);
        }
        asm volatile("" : "+v"(pf));
        PIN8(acc);
        TA(0);
        if (full < uv + 1) full = ctl_wait<0>(ctl + 4 * C_FULL, uv + 1);
        CFENCE();
        TA(1);
        LAS char* vb = L + (uv % NR) * UNIT + voff;
#pragma unroll
        for (int d = 0; d < 8; ++d) {
          const v2u a = trread(vb + (d >> 1) * 64 + (d & 1) * 8), b = trread(vb + 512 + (d >> 1) * 64 + (d & 1) * 8);
          vf[d] = v4u{a[0], a[1], b[0], b[1]};
        }
        PIN8(vf);
        ctl_wr(ctl + 4 * (C_REL + aw), t + NATT < 64 ? uv + NATT : 128 * (ph + 1) + aw);
        __builtin_amdgcn_sched_barrier(0);
#pragma unroll
        for (int d = 0; d < 8; ++d) acc[d] = mfma(vf[d], pf, acc[d]);
        TA(2);
        if (ph == 1 && t == OA_AT) {
          ctl_wait<1>(ctl + 4 * C_CLR, NATT);
          const v2u tg = *(LAS v2u*)(L + L_CTRL + 4 * C_EP);
          if (lane == 0) __hip_atomic_store((GAS unsigned long long*)(ws + W_CF) + FSTR * j, ((unsigned long long)tg[1] << 32) | tg[0], __ATOMIC_RELAXED, __HIP_MEMORY_SCOPE_AGENT);
          if (c < 8) {
#pragma unroll
            for (int s = 0; s < 4; ++s) __builtin_amdgcn_raw_buffer_store_b128(oA[s], rsO, obase + s * 1024, 0, AUX_SC1);
          }
        }
      }
      if (aw != 0) {
        if (c < 8) {
#pragma unroll
          for (int d = 0; d < 8; ++d) *(LAS v4f*)(rec + (aw - 1) * 4096 + d * 512) = acc[d];
        }
        LDS_WAIT();
        ctl_wr(ctl + 4 * (C_MRG + 2 * ph + aw - 1), 1u);
      } else {
        ctl_wait<0>(ctl + 4 * (C_MRG + 2 * ph + 1), 1);
        CFENCE();
#pragma unroll
        for (int d = 0; d < 8; ++d) vf[d] = *(LAS v4u*)(rec + 4096 + d * 512);
#pragma unroll
        for (int s = 0; s < 4; ++s) {
          const v4f x0 = __builtin_elementwise_fma(__builtin_bit_cast(v4f, vf[2 * s]), v4f{f1, f1, f1, f1}, __builtin_elementwise_fma(acc[2 * s], v4f{l, l, l, l}, __builtin_bit_cast(v4f, kf[2 * s])));
          const v4f x1 = __builtin_elementwise_fma(__builtin_bit_cast(v4f, vf[2 * s + 1]), v4f{f1, f1, f1, f1}, __builtin_elementwise_fma(acc[2 * s + 1], v4f{l, l, l, l}, __builtin_bit_cast(v4f, kf[2 * s + 1])));
          v4u o = {pk2(x0[0], x0[1]), pk2(x0[2], x0[3]), pk2(x1[0], x1[1]), pk2(x1[2], x1[3])};
#pragma unroll
          for (int k = 0; k < 4; ++k) o[k] = min(o[k], SENT - 1u);
          if (ph == 0) oA[s] = o;
          else if (mine) __builtin_amdgcn_raw_buffer_store_b128(o, rsO, obase + s * 1024, 4 * 8 * 16384, AUX_SC1);
        }
        STAMP(5 + ph);
      }
    }
  } else {
    const int gw = wave - 2 - (wave >> 2);
    {
      const __amdgpu_buffer_rsrc_t rsQ = __builtin_amdgcn_make_buffer_rsrc((void*)wq, 0, DM * NH * DH * 2, 0x00020000);
      const int wvo = (8 * g * (NH * DH) + 8 * c) * 2, wso = (128 * gw * (NH * DH) + h * DH) * 2;
      const int xrow = ((c < 8 ? bp : bp + 8) * SQ + (c & 7));
      const GAS char* xb = (const GAS char*)x + ((size_t)xrow * DM + 128 * gw + 8 * g) * 2;
#define WQ_ROW(t, jj) ((32 * (((t) + (bp >> 1)) & 3) + ((jj) ^ (4 * (bp & 1)))) * (NH * DH * 2))
      v4u xf[4], w[4][8];
      {
#pragma unroll
        for (int jj = 0; jj < 4; ++jj) w[0][jj] = __builtin_amdgcn_raw_buffer_load_b128(rsQ, wvo, wso + WQ_ROW(0, jj), AUX_NT);
        const unsigned pf = *(const GAS unsigned*)(xb + (hq & 1) * 128);
        __builtin_amdgcn_sched_barrier(0);
        __builtin_amdgcn_s_barrier();
        __builtin_amdgcn_sched_barrier(0);
        asm volatile("" ::"v"(pf));
        vm_wait<0>();
        __builtin_amdgcn_s_sleep(Q_GAP);
        __builtin_amdgcn_sched_barrier(0);
      }
#pragma unroll
      for (int jj = 4; jj < 8; ++jj) w[0][jj] = __builtin_amdgcn_raw_buffer_load_b128(rsQ, wvo, wso + WQ_ROW(0, jj), AUX_NT);
#pragma unroll
      for (int tt = 0; tt < 4; ++tt) xf[tt] = *(const GAS v4u*)(xb + ((tt + (bp >> 1)) & 3) * 64);
#pragma unroll
      for (int t = 1; t < 3; ++t)
#pragma unroll
        for (int jj = 0; jj < 8; ++jj) w[t][jj] = __builtin_amdgcn_raw_buffer_load_b128(rsQ, wvo, wso + WQ_ROW(t, jj), AUX_NT);
      __builtin_amdgcn_sched_barrier(0);
      ctl_add(ctl + 4 * C_QI);
      if (bp & 1) {
#pragma unroll
        for (int t = 0; t < 4; ++t) xf[t] = v4u{xf[t][2], xf[t][3], xf[t][0], xf[t][1]};
      }
      v4f qa[8];
#pragma unroll
      for (int d = 0; d < 8; ++d) qa[d] = v4f{0.f, 0.f, 0.f, 0.f};
#pragma unroll
      for (int t = 0; t < 4; ++t) {
#pragma unroll
        for (int dd = 0; dd < 8; ++dd) {
          v4u f;
#pragma unroll
          for (int v = 0; v < 4; ++v) f[v] = __builtin_amdgcn_perm(w[t][2 * v + 1][dd >> 1], w[t][2 * v][dd >> 1], (dd & 1) ? 0x07060302u : 0x05040100u);
          qa[dd] = mfma(f, xf[t], qa[dd]);
          if (dd & 1) __builtin_amdgcn_sched_barrier(0);
        }
        if (t == 0) {
#pragma unroll
          for (int jj = 0; jj < 8; ++jj) {
            w[3][jj] = __builtin_amdgcn_raw_buffer_load_b128(rsQ, wvo, wso + WQ_ROW(3, jj), AUX_NT);
            __builtin_amdgcn_sched_barrier(0);
          }
        }
      }
      STAMP(16 + gw);
      LAS char* part = L + L_PS + (gw & 3) * 8192 + lane * 16;
      if (gw >= 4) {
        ctl_wait<0>(ctl + 4 * C_Q1, 4);
        CFENCE();
      }
#pragma unroll
      for (int i = 0; i < 4; ++i) {
        v4f lo = {qa[0][i], qa[1][i], qa[2][i], qa[3][i]}, hi = {qa[4][i], qa[5][i], qa[6][i], qa[7][i]};
        if (gw >= 4) { lo += *(LAS v4f*)(part + i * 2048); hi += *(LAS v4f*)(part + i * 2048 + 1024); }
        *(LAS v4f*)(part + i * 2048) = lo;
        *(LAS v4f*)(part + i * 2048 + 1024) = hi;
      }
      LDS_WAIT();
      ctl_add(ctl + 4 * (gw >= 4 ? C_Q2 : C_Q1));
      if (gw == 0) STAMP(7);
      if (gw == 7) STAMP(8);
    }
    const int xc = j & 7, idx = j >> 3, ct = idx & 7, rg = idx >> 3;
    unsigned hb = 0;
#define HBAR(SLP) do { hb += NHELP; ctl_add(ctl + 4 * C_HB); ctl_wait<SLP>(ctl + 4 * C_HB, hb); CFENCE(); } while (0)
    const int pdst = W_P + (idx * 128 + gw * 8 + xc) * 1024;
    v4u wf[16];
    {
      const __amdgpu_buffer_rsrc_t rsW = __builtin_amdgcn_make_buffer_rsrc((void*)wo, 0, DM * NH * DH * 2, 0x00020000);
      ctl_wait<32>(ctl + 4 * C_FULL, 40 + 2 * gw);
#pragma unroll
      for (int ks = 0; ks < 16; ++ks) {
        wf[ks] = __builtin_amdgcn_raw_buffer_load_b128(rsW, g * 2048 + c * 16, ((4 * xc + (ks >> 2)) * 128 + (ks & 3) * 32 + 4 * gw) * 2048 + ct * 256, AUX_NT);
        __builtin_amdgcn_s_sleep(WF_SLEEP);
        __builtin_amdgcn_sched_barrier(0);
      }
      LAS char* sw = L + L_STG + gw * PIECE + lane * 16;
      LAS char* sr = L + L_STG + 2 * g * PIECE + (c >> 2) * 256 + gw * 32 + (c & 3) * 8;
#pragma unroll
      for (int r = 0; r < 16; ++r) {
        *(LAS v4u*)(sw) = wf[r];
        LDS_WAIT();
        HBAR(2);
        const v2u a0 = trread(sr), b0 = trread(sr + PIECE);
        wf[r] = v4u{a0[0], a0[1], b0[0], b0[1]};
        asm volatile("" : "+v"(wf[r])::"memory");
        HBAR(2);
      }
    }
    const v2u tagv = *(LAS v2u*)(L + L_CTRL + 4 * C_EP);
    const unsigned long long tag = ((unsigned long long)tagv[1] << 32) | tagv[0];
    unsigned tb[4];
    {
      const unsigned hs = (unsigned)((tag - 1ull) >> 8) * 0x9E3779B1u ^ (unsigned)((tag - 1ull) >> 40) * 0x85EBCA77u;
#pragma unroll
      for (int k = 0; k < 4; ++k) tb[k] = rfl((tagv[0] + ((hs >> (8 * k)) & 0xFFu) % 254u) & 0xFFu);
    }
    if (gw == 7) STAMP(9);
    ctl_wait<16>(ctl + 4 * C_FULL, NUNIT - 16);
    ctl_wait<2>(ctl + 4 * C_LDONE, 1);
    if (gw == 7) STAMP(10);
    {
      const int src = (!PTAG && lane < 8) ? idx * 8 + lane : (2 * rg + (lane & 1)) * 32 + (gw >> 1) * 8 + xc;
      GAS unsigned long long* fl = (GAS unsigned long long*)(ws + W_CF) + FSTR * src;
      for (unsigned sp = 0; sp < (1u << 16); ++sp) {
        if (__all(__hip_atomic_load(fl, __ATOMIC_RELAXED, __HIP_MEMORY_SCOPE_AGENT) == tag)) break;
        __builtin_amdgcn_s_sleep(G1_SLEEP);
      }
      __builtin_amdgcn_fence(__ATOMIC_ACQUIRE, "wavefront");
    }
    if (gw == 7) STAMP(11);
#pragma unroll 1
    for (int ph = 0; ph < 2; ++ph) {
      v4u f0, f1;
      for (unsigned sp = 0; sp < (1u << 16); ++sp) {
        CFENCE();
        f0 = __builtin_amdgcn_raw_buffer_load_b128(rsO, lane * 16, W_O + ((ph * 4 + rg) * 8 + xc) * 16384 + gw * 2048, AUX_SC1);
        f1 = __builtin_amdgcn_raw_buffer_load_b128(rsO, lane * 16, W_O + ((ph * 4 + rg) * 8 + xc) * 16384 + gw * 2048 + 1024, AUX_SC1);
        bool ok = true;
#pragma unroll
        for (int k = 0; k < 4; ++k) ok &= (f0[k] != SENT) & (f1[k] != SENT);
        if (__all(ok)) break;
        __builtin_amdgcn_s_sleep(G1_SLEEP);
      }
      *(LAS v4u*)(L + L_OB + gw * 2048 + lane * 16) = f0;
      *(LAS v4u*)(L + L_OB + gw * 2048 + 1024 + lane * 16) = f1;
      LDS_WAIT();
      HBAR(0);
      v4f oa[4];
#pragma unroll
      for (int k = 0; k < 4; ++k) oa[k] = v4f{0.f, 0.f, 0.f, 0.f};
#pragma unroll
      for (int f = 0; f < 16; ++f) oa[f & 3] = mfma(wf[f], *(LAS v4u*)(L + L_OB + f * 1024 + lane * 16), oa[f & 3]);
      v4u pt = __builtin_bit_cast(v4u, (oa[0] + oa[1]) + (oa[2] + oa[3]));
#pragma unroll
      for (int k = 0; k < 4; ++k) pt[k] = PTAG ? (((pt[k] + ((pt[k] & 0x7F800000u) == 0x7F800000u ? 0u : 0x80u)) & ~0xFFu) | tb[k]) : min(pt[k], SENT - 1u);
      __builtin_amdgcn_raw_buffer_store_b128(pt, rsO, lane * 16, pdst + ph * 65536, AUX_SC1);
      if (gw == 7) STAMP(24 + ph);
      if (ph == 0) HBAR(0);
    }
    if (gw < 2) {
      v4u q[8];
      for (unsigned sp = 0; sp < (1u << 16); ++sp) {
        CFENCE();
        bool ok = true;
#pragma unroll
        for (int k = 0; k < 8; ++k) q[k] = __builtin_amdgcn_raw_buffer_load_b128(rsO, lane * 16, W_P + ((idx * 2 + gw) * 64 + xc * 8 + k) * 1024, AUX_SC1);
#pragma unroll
        for (int k = 0; k < 8; ++k)
          ok &= PTAG ? ((((q[k][0] ^ tb[0]) | (q[k][1] ^ tb[1]) | (q[k][2] ^ tb[2]) | (q[k][3] ^ tb[3])) & 0xFFu) == 0u)
                     : ((q[k][0] != SENT) & (q[k][1] != SENT) & (q[k][2] != SENT) & (q[k][3] != SENT));
        if (__all(ok)) break;
        __builtin_amdgcn_s_sleep(1);
      }
      const unsigned km = PTAG ? ~0xFFu : ~0u;
      v4f s = __builtin_bit_cast(v4f, q[0] & km);
#pragma unroll
      for (int k = 1; k < 8; ++k) s += __builtin_bit_cast(v4f, q[k] & km);
      *(GAS v2u*)((GAS char*)out + ((size_t)(64 * gw + 16 * rg + c) * DM + 128 * ct + 16 * xc + 4 * g) * 2) = v2u{pk2(s[0], s[1]), pk2(s[2], s[3])};
      STAMP(12 + gw);
    } else if (gw == 2 && j == 0) {
      GAS unsigned long long* fl = (GAS unsigned long long*)(ws + W_CF) + FSTR * lane;
      for (unsigned sp = 0; sp < (1u << 16); ++sp) {
        bool ok = true;
#pragma unroll
        for (int k = 0; k < 4; ++k) ok &= __hip_atomic_load(fl + FSTR * 64 * k, __ATOMIC_RELAXED, __HIP_MEMORY_SCOPE_AGENT) == tag;
        if (__all(ok)) break;
        __builtin_amdgcn_s_sleep(32);
      }
      if (lane == 0) __hip_atomic_store((GAS unsigned long long*)(ws + W_EPOCH), tag, __ATOMIC_RELAXED, __HIP_MEMORY_SCOPE_AGENT);
    }
  }
}
}

extern "C" void kernel_launch(void* const* d_in, const int* in_sizes, int n_in, void* d_out, int out_size, void* d_ws, size_t ws_size,
                              hipStream_t stream) {
  (void)in_sizes; (void)n_in; (void)out_size; (void)ws_size;
  (void)hipFuncSetAttribute((const void*)mha_block_kernel, hipFuncAttributeMaxDynamicSharedMemorySize, LDS_BYTES);
  mha_block_kernel<<<NWG, NTHR, LDS_BYTES, stream>>>((const unsigned short*)d_in[0], (const unsigned short*)d_in[1], (const unsigned short*)d_in[2],
                                                     (const unsigned short*)d_in[3], (const unsigned short*)d_in[4], (unsigned short*)d_out,
                                                     (unsigned char*)d_ws);
}
```

```cpp
#include <hip/hip_runtime.h>
#include <hip/hip_bf16.h>
#include <stdint.h>

#define GAS __attribute__((address_space(1)))
#define LAS __attribute__((address_space(3)))
typedef unsigned v4u __attribute__((ext_vector_type(4)));
typedef unsigned v2u __attribute__((ext_vector_type(2)));
typedef float v4f __attribute__((ext_vector_type(4)));
typedef float v2f __attribute__((ext_vector_type(2)));
typedef short v4s __attribute__((ext_vector_type(4)));
typedef __bf16 v8bf __attribute__((ext_vector_type(8)));
typedef __bf16 v2bf __attribute__((ext_vector_type(2)));

#define WIN 20
#define WIN0 24
#define U0 24
#define UI 3
#define X2_SLEEP 1
#define QI_GO 5
#define Q_GAP 4
#define WINB 28
#define CK_U0 24
#define CK_T0 1080
#define CK_U1 108
#define CK_T1 3530
#define CK_U2 200
#define CK_T2 6030
#define CT_ROT 2
#define WO_AT 68
#define CK_G 160
#define WINE 32
#define UE 16
#define WF_SLEEP 16
#define G1_SLEEP 1
#define OA_AT 57

namespace {
constexpr int NB = 16, SQ = 8, DM = 1024, NH = 32, SK = 2048, DH = 128;
constexpr int NWG = 256, NTHR = 768, NATT = 3, NHELP = 8;
constexpr int PIECE = 1056;
constexpr int UNIT = 8 * PIECE;
constexpr int NR = 14;
constexpr int NUNIT = 256;
constexpr int AUX_NT = 2, AUX_SC1 = 16;
constexpr int L_CTRL = NR * UNIT;
constexpr int PREC = 544;
constexpr int L_PS = L_CTRL + 256;
constexpr int L_OB = L_PS;
constexpr int L_MRG = L_PS + 32 * PREC;
constexpr int L_ST = L_PS + 64 * PREC;
constexpr int L_STG = L_ST + 1024;
constexpr int LDS_BYTES = 160 * 1024;
static_assert(L_STG + 8 * PIECE <= LDS_BYTES && 16384 <= 32 * PREC && 32 * PREC + 8192 <= 48 * PREC && NR <= 14, "LDS map");
enum { C_FULL = 0, C_REL = 4, C_Q1 = 8, C_Q2 = 9, C_QI = 10, C_HB = 11, C_QR = 12, C_EP = 14, C_ML = 18, C_MRG = 20, C_LDONE = 24 };
constexpr int W_EPOCH = 0, W_CF = 65536, FSTR = 16;
constexpr int W_O = 1 << 20;
constexpr int W_P = 2 << 20;
constexpr int WS_USED = 6 << 20;
constexpr unsigned SENT = 0xFFFFFFFFu;
constexpr float CSC = 0.08838834764831845f * 1.4426950408889634f;
constexpr unsigned SPIN_CAP = 1u << 22;

__device__ __forceinline__ unsigned rfl(unsigned x) { return __builtin_amdgcn_readfirstlane(x); }
__device__ __forceinline__ v4f mfma(v4u a, v4u b, v4f c) {
  return __builtin_amdgcn_mfma_f32_16x16x32_bf16(__builtin_bit_cast(v8bf, a), __builtin_bit_cast(v8bf, b), c, 0, 0, 0);
}
__device__ __forceinline__ unsigned pk2(float lo, float hi) {
  v2f x = {lo, hi};
  return __builtin_bit_cast(unsigned, __builtin_convertvector(x, v2bf));
}
__device__ __forceinline__ v2u trread(LAS char* p) {
  return __builtin_bit_cast(v2u, __builtin_amdgcn_ds_read_tr16_b64_v4i16((LAS v4s*)p));
}
__device__ __forceinline__ float xg_max(float x) {
  auto s = __builtin_amdgcn_permlane16_swap(__float_as_uint(x), __float_as_uint(x), false, false);
  x = fmaxf(__uint_as_float(s[0]), __uint_as_float(s[1]));
  auto t = __builtin_amdgcn_permlane32_swap(__float_as_uint(x), __float_as_uint(x), false, false);
  return fmaxf(__uint_as_float(t[0]), __uint_as_float(t[1]));
}
__device__ __forceinline__ float xg_sum(float x) {
  auto s = __builtin_amdgcn_permlane16_swap(__float_as_uint(x), __float_as_uint(x), false, false);
  x = __uint_as_float(s[0]) + __uint_as_float(s[1]);
  auto t = __builtin_amdgcn_permlane32_swap(__float_as_uint(x), __float_as_uint(x), false, false);
  return __uint_as_float(t[0]) + __uint_as_float(t[1]);
}
#define STAMP(k)
#define STAMPV(k, v)
#define LDS_WAIT() asm volatile("s_waitcnt lgkmcnt(0)" ::: "memory")
#define CFENCE() asm volatile("" ::: "memory")
#define PIN8(a) asm volatile("" : "+v"(a[0]), "+v"(a[1]), "+v"(a[2]), "+v"(a[3]), "+v"(a[4]), "+v"(a[5]), "+v"(a[6]), "+v"(a[7])::"memory")
template <int N> __device__ __forceinline__ void vm_wait() { asm volatile("s_waitcnt vmcnt(%c0)" ::"n"(N) : "memory"); }
__device__ __forceinline__ unsigned ctl_rd(unsigned addr) {
  unsigned v;
  asm volatile("ds_read_b32 %0, %1\n\ts_waitcnt lgkmcnt(0)" : "=v"(v) : "v"(addr) : "memory");
  return rfl(v);
}
__device__ __forceinline__ void ctl_wr(unsigned addr, unsigned val) {
  asm volatile("s_mov_b64 exec, 1\n\tds_write_b32 %0, %1\n\ts_mov_b64 exec, -1" ::"v"(addr), "v"(val) : "memory");
}
__device__ __forceinline__ unsigned ctl_add(unsigned addr) {
  unsigned v;
  asm volatile("s_mov_b64 exec, 1\n\tds_add_rtn_u32 %0, %1, %2\n\ts_mov_b64 exec, -1\n\ts_waitcnt lgkmcnt(0)" : "=&v"(v) : "v"(addr), "v"(1u) : "memory");
  return rfl(v);
}
template <int SLP> __device__ __forceinline__ unsigned ctl_wait(unsigned addr, unsigned need) {
  unsigned v = ctl_rd(addr);
  for (unsigned sp = 0; v < need && sp < SPIN_CAP; ++sp) {
    __builtin_amdgcn_s_sleep(SLP);
    v = ctl_rd(addr);
  }
  return v;
}

__device__ __forceinline__ void tag_bytes(v2u tagv, unsigned (&tb)[4]) {
  const unsigned long long e = (((unsigned long long)tagv[1] << 32) | tagv[0]) - 1ull;
  const unsigned hs = (unsigned)(e >> 8) * 0x9E3779B1u ^ (unsigned)(e >> 40) * 0x85EBCA77u;
#pragma unroll
  for (int k = 0; k < 4; ++k) tb[k] = rfl((tagv[0] + ((hs >> (8 * k)) & 0xFFu) % 254u) & 0xFFu);
}

#define PUBL(k) ".if %c[b] == " #k "\n\ts_mov_b64 exec, 1\n\tds_write_b32 %[fa], v23\n\ts_mov_b64 exec, -1\n\t.endif\n\t"
#define PC(k, VO, IMM) "s_waitcnt vmcnt(%c[w])\n\t" PUBL(k) "buffer_load_dwordx4 " VO ", %[rs], %[so] offen offset:" IMM " nt lds\n\t"
template <int W>
__device__ __forceinline__ void dma_units(__amdgpu_buffer_rsrc_t rs, unsigned soff, unsigned uend, unsigned& m0b, unsigned& u, unsigned& lim, unsigned vo0,
                                          unsigned vo1, unsigned vctl, unsigned faddr) {
  unsigned sp;
  asm volatile(
      "0:\n\t"
      "s_cmp_lt_u32 %[u], %[lim]\n\t"
      "s_cbranch_scc1 2f\n\t"
      "s_mov_b32 %[sp], 0\n"
      "1:\n\t"
      "ds_read_b32 v20, %[vc] offset:16\n\tds_read_b32 v21, %[vc] offset:20\n\tds_read_b32 v22, %[vc] offset:24\n\t"
      "s_waitcnt lgkmcnt(0)\n\t"
      "v_min3_u32 v20, v20, v21, v22\n\t"
      "s_add_u32 %[sp], %[sp], 1\n\t"
      "v_readfirstlane_b32 %[lim], v20\n\t"
      "s_add_u32 %[lim], %[lim], %c[nr]\n\t"
      "s_cmp_lt_u32 %[u], %[lim]\n\t"
      "s_cbranch_scc1 2f\n\t"
      "s_sleep 1\n\t"
      "s_cmp_lt_u32 %[sp], %c[cap]\n\t"
      "s_cbranch_scc1 1b\n"
      "2:\n\t"
      "s_mov_b32 m0, %[m0b]\n\t"
      "v_mov_b32 v23, %[u]\n\t"
      "v_subrev_u32 v23, %c[a], v23\n\t"
      PC(0, "%[vo0]", "0")
      "ds_read_b32 v20, %[vc] offset:16\n\tds_read_b32 v21, %[vc] offset:20\n\tds_read_b32 v22, %[vc] offset:24\n\t"
      "s_add_u32 m0, m0, 32\n\t"
      PC(1, "%[vo0]", "1024")
      "s_add_u32 m0, m0, 32\n\t"
      PC(2, "%[vo0]", "2048")
      "s_add_u32 m0, m0, 32\n\t"
      PC(3, "%[vo0]", "3072")
      "s_add_u32 m0, m0, 0x1020\n\t"
      PC(4, "%[vo1]", "0")
      "s_waitcnt lgkmcnt(0)\n\t"
      "v_min3_u32 v20, v20, v21, v22\n\t"
      "s_add_u32 m0, m0, 32\n\t"
      PC(5, "%[vo1]", "1024")
      "v_readfirstlane_b32 %[lim], v20\n\t"
      "s_add_u32 %[m0b], %[m0b], %c[unit]\n\t"
      "s_add_u32 m0, m0, 32\n\t"
      PC(6, "%[vo1]", "2048")
      "s_add_u32 %[lim], %[lim], %c[nr]\n\t"
      "s_cmp_lt_u32 %[m0b], %c[ring]\n\t"
      "s_cselect_b32 %[m0b], %[m0b], 0\n\t"
      "s_add_u32 %[u], %[u], 1\n\t"
      "s_add_u32 m0, m0, 32\n\t"
      PC(7, "%[vo1]", "3072")
      "s_add_u32 %[so], %[so], 0x2000\n\t"
      "s_cmp_lt_u32 %[u], %[ue]\n\t"
      "s_cbranch_scc1 0b"
      : [m0b] "+s"(m0b), [u] "+s"(u), [lim] "+s"(lim), [so] "+s"(soff), [sp] "=&s"(sp)
      : [ue] "s"(uend), [w] "n"(W - 1), [a] "n"((W - 1) / 8), [b] "n"((W - 1) % 8), [nr] "n"(NR), [cap] "n"(SPIN_CAP), [unit] "n"(UNIT), [ring] "n"(NR * UNIT), [rs] "s"(rs),
        [vo0] "v"(vo0), [vo1] "v"(vo1), [vc] "v"(vctl), [fa] "v"(faddr)
      : "memory", "scc", "v20", "v21", "v22", "v23");
}

__global__ void __launch_bounds__(NTHR, 3)
mha_block_kernel(const unsigned short* __restrict__ x, const unsigned short* __restrict__ kc, const unsigned short* __restrict__ vc,
                 const unsigned short* __restrict__ wq, const unsigned short* __restrict__ wo, unsigned short* __restrict__ out,
                 unsigned char* __restrict__ ws) {
  extern __shared__ __attribute__((aligned(16))) char smem[];
  LAS char* L = (LAS char*)smem;
  const unsigned lbase = (unsigned)(size_t)L;
  const int lane = threadIdx.x & 63, wave = rfl(threadIdx.x >> 6);
  const int g = lane >> 4, c = lane & 15;
  const int j = blockIdx.x, hq = (j >> 3) & 3, h = 4 * (j & 7) + hq, bp = j >> 5;
  const unsigned ctl = lbase + L_CTRL;
  const __amdgpu_buffer_rsrc_t rsO = __builtin_amdgcn_make_buffer_rsrc((void*)ws, 0, WS_USED, 0x00020000);

  if (wave == 0) {
    __builtin_amdgcn_s_setprio(3);
    __builtin_amdgcn_s_waitcnt(0x0F70);
    const unsigned long long tl0 = __builtin_amdgcn_s_memrealtime();
    const unsigned vo0 = lane * 16, vo1 = lane * 16 + 4096;
    const unsigned sbase = (unsigned)(bp * NH + h) * (SK * DH * 2);
    unsigned m0b = 0, u = 0, lim = NR;
    unsigned ub = 0;
    static_assert(UI >= 2 && UI + (WIN0 - 1) / 8 < U0 && U0 <= CK_U0 && CK_U0 < CK_U1 && CK_U1 < CK_U2 && CK_U2 < NUNIT - UE, "segment order");
#pragma unroll 1
    while (u < NUNIT) {
      const __amdgpu_buffer_rsrc_t rs = __builtin_amdgcn_make_buffer_rsrc((void*)((u & 64) ? vc : kc), 0, NB * NH * SK * DH * 2, 0x00020000);
      const unsigned soff = sbase + (u >> 7) * (8u * NH * SK * DH * 2) + (u & 63) * 8192u;
      unsigned ue = (u | 63) + 1;
      if (u < U0) {
        if (u == 2) __builtin_amdgcn_s_barrier();
        if (u == UI) {
          vm_wait<0>(); ctl_wr(ctl + 4 * C_FULL, UI);
          ctl_wait<1>(ctl + 4 * C_QI, QI_GO);
        }
        const bool early = u < UI + (WIN0 - 1) / 8;
        dma_units<WIN0>(rs, soff, min(ue, early ? u + 1 : (unsigned)U0), m0b, u, lim, vo0, vo1, ctl, early ? ctl + 252 : ctl + 4 * C_FULL);
        if (u <= 2) lim = NR;
      } else if (u >= NUNIT - UE) {
        dma_units<WINE>(rs, soff, ue, m0b, u, lim, vo0, vo1, ctl, ctl + 4 * C_FULL);
      } else {
        if (u == CK_U0 || u == CK_U1 || u == CK_U2) {
          const int late = (int)(unsigned)(__builtin_amdgcn_s_memrealtime() - tl0) - (u == CK_U0 ? CK_T0 : (u == CK_U1 ? CK_T1 : CK_T2));
          ub = u + (late > 0 ? min((late * CK_G) >> 10, NUNIT) : 0);
        }
        ue = min(ue, u < CK_U0 ? (unsigned)CK_U0 : (u < CK_U1 ? (unsigned)CK_U1 : (u < CK_U2 ? (unsigned)CK_U2 : (unsigned)(NUNIT - UE))));
        if (u < ub) dma_units<WINB>(rs, soff, min(ue, ub), m0b, u, lim, vo0, vo1, ctl, ctl + 4 * C_FULL);
        else dma_units<WIN>(rs, soff, ue, m0b, u, lim, vo0, vo1, ctl, ctl + 4 * C_FULL);
      }
    }
    ctl_wr(ctl + 4 * C_LDONE, 1u);
    if (WINE > 48) { vm_wait<48>(); ctl_wr(ctl + 4 * C_FULL, NUNIT - 6); }
    if (WINE > 40) { vm_wait<40>(); ctl_wr(ctl + 4 * C_FULL, NUNIT - 5); }
    if (WINE > 32) { vm_wait<32>(); ctl_wr(ctl + 4 * C_FULL, NUNIT - 4); }
    if (WINE > 24) { vm_wait<24>(); ctl_wr(ctl + 4 * C_FULL, NUNIT - 3); }
    vm_wait<16>(); ctl_wr(ctl + 4 * C_FULL, NUNIT - 2);
    vm_wait<8>(); ctl_wr(ctl + 4 * C_FULL, NUNIT - 1);
    vm_wait<0>(); ctl_wr(ctl + 4 * C_FULL, NUNIT);
  } else if ((wave & 3) == 0 || wave == 1) {
    const int aw = wave == 1 ? 2 : (wave >> 2) - 1;
    if (aw == 0) { *(LAS unsigned*)(L + L_CTRL + 4 * lane) = 0u; LDS_WAIT(); }
    __builtin_amdgcn_s_barrier();
    const unsigned koff = (c & 7) * PIECE + (c >> 3) * 256 + g * 16;
    const unsigned voff = (4 * (g & 1) + (c >> 2)) * PIECE + (g >> 1) * 256 + (c & 3) * 16;
    const int obase = W_O + ((bp >> 1) * 8 + (j & 7)) * 16384 + hq * 4096 + g * 256 + ((bp & 1) * 8 + (c & 7)) * 16;
    v4u oA[4];
#pragma unroll
    for (int s = 0; s < 4; ++s) oA[s] = v4u{0u, 0u, 0u, 0u};
    {
      const v4u sv = {SENT, SENT, SENT, SENT};
      if (aw == 0) {
        const unsigned long long tag = __hip_atomic_load((GAS unsigned long long*)(ws + W_EPOCH) + FSTR * (j >> 6), __ATOMIC_RELAXED, __HIP_MEMORY_SCOPE_AGENT) + 1ull;
#pragma unroll
        for (int s = 0; s < 4; ++s) __builtin_amdgcn_raw_buffer_store_b128(sv, rsO, obase + (c >> 3) * (4 * 8 * 16384), s * 1024, AUX_SC1);
        if (lane == 0) *(LAS v2u*)(L + L_CTRL + 4 * C_EP) = v2u{(unsigned)tag, (unsigned)(tag >> 32)};
      }
    }
    v4u kf[8], vf[8];
    unsigned full = ctl_wait<1>(ctl + 4 * C_FULL, aw + 1);
    CFENCE();
#pragma unroll
    for (int i = 0; i < 8; ++i) kf[i] = *(LAS v4u*)(L + aw * UNIT + koff + (i >> 2) * 512 + (i & 3) * 64);
    PIN8(kf);
    ctl_wr(ctl + 4 * (C_REL + aw), aw + NATT);
    full = ctl_wait<1>(ctl + 4 * C_FULL, aw + NATT + 1);
    CFENCE();
#pragma unroll
    for (int i = 0; i < 8; ++i) vf[i] = *(LAS v4u*)(L + (aw + NATT) * UNIT + koff + (i >> 2) * 512 + (i & 3) * 64);
    PIN8(vf);
    ctl_wr(ctl + 4 * (C_REL + aw), aw + 2 * NATT);
    if (aw == 0) STAMP(14);
    ctl_wait<1>(ctl + 4 * C_Q2, 4);
    CFENCE();
    v4u qf[4];
#pragma unroll
    for (int s = 0; s < 4; ++s) {
      LAS char* qp = L + L_PS + g * 2048 + (s * 16 + c) * 16;
      const v4f lo = (*(LAS v4f*)(qp) + *(LAS v4f*)(qp + 8192)) + (*(LAS v4f*)(qp + 16384) + *(LAS v4f*)(qp + 24576));
      const v4f hi = (*(LAS v4f*)(qp + 1024) + *(LAS v4f*)(qp + 9216)) + (*(LAS v4f*)(qp + 17408) + *(LAS v4f*)(qp + 25600));
      qf[s] = v4u{pk2(lo[0], lo[1]), pk2(lo[2], lo[3]), pk2(hi[0], hi[1]), pk2(hi[2], hi[3])};
      if (s & 1) asm volatile("" : "+v"(qf[s - 1]), "+v"(qf[s])::"memory");
    }
    ctl_add(ctl + 4 * C_QR);
    ctl_wait<0>(ctl + 4 * C_QR, NATT);
    if (aw == 0) STAMP(4);
#define TA(k)
    const unsigned poff = L_PS + (g * 8 + (c & 7)) * 16, aoff = L_PS + 512 + (c & 7) * 4;
#pragma unroll 1
    for (int ph = 0; ph < 2; ++ph) {
      const bool mine = (c >> 3) == ph;
      float m = -INFINITY, l = 0.f;
#pragma unroll 1
      for (int t = aw; t < 64; t += NATT) {
        const unsigned uk = 128 * ph + t;
        if (uk >= 2 * NATT) {
          if (full < uk + 1) full = ctl_wait<0>(ctl + 4 * C_FULL, uk + 1);
          CFENCE();
          LAS char* kb = L + (uk % NR) * UNIT + koff;
#pragma unroll
          for (int i = 0; i < 8; ++i) kf[i] = *(LAS v4u*)(kb + (i >> 2) * 512 + (i & 3) * 64);
          PIN8(kf);
          ctl_wr(ctl + 4 * (C_REL + aw), t + NATT < 64 ? uk + NATT : 128 * ph + 64 + aw);
          __builtin_amdgcn_sched_barrier(0);
        } else if (uk >= NATT) {
#pragma unroll
          for (int i = 0; i < 8; ++i) kf[i] = vf[i];
        }
        v4f s0 = {0.f, 0.f, 0.f, 0.f}, s1 = {0.f, 0.f, 0.f, 0.f};
#pragma unroll
        for (int s = 0; s < 4; ++s) { s0 = mfma(kf[s], qf[s], s0); s1 = mfma(kf[4 + s], qf[s], s1); }
        float mx = fmaxf(fmaxf(fmaxf(s0[0], s0[1]), fmaxf(s0[2], s0[3])), fmaxf(fmaxf(s1[0], s1[1]), fmaxf(s1[2], s1[3])));
        mx = xg_max(mx);
        const float mn = fmaxf(m, mx);
        const float alpha = __builtin_amdgcn_exp2f((m - mn) * CSC);
        const float mc = mn * CSC;
        m = mn;
        float p[8];
#pragma unroll
        for (int i = 0; i < 4; ++i) { p[i] = __builtin_amdgcn_exp2f(fmaf(s0[i], CSC, -mc)); p[4 + i] = __builtin_amdgcn_exp2f(fmaf(s1[i], CSC, -mc)); }
        l = fmaf(l, alpha, ((p[0] + p[1]) + (p[2] + p[3])) + ((p[4] + p[5]) + (p[6] + p[7])));
        if (mine) {
          *(LAS v4u*)(L + poff + t * PREC) = v4u{pk2(p[0], p[1]), pk2(p[2], p[3]), pk2(p[4], p[5]), pk2(p[6], p[7])};
          if (g == 0) *(LAS float*)(L + aoff + t * PREC) = alpha;
        }
      }
      float f0 = 0.f, f1 = 0.f;
      if (aw == 0) {
        ctl_wait<0>(ctl + 4 * (C_ML + ph), 2);
        CFENCE();
        const v2f a0 = *(LAS v2f*)(L + L_ST + lane * 8), a1 = *(LAS v2f*)(L + L_ST + 512 + lane * 8);
        const float M = fmaxf(m, fmaxf(a0[0], a1[0]));
        const float w = __builtin_amdgcn_exp2f((m - M) * CSC), w0 = __builtin_amdgcn_exp2f((a0[0] - M) * CSC), w1 = __builtin_amdgcn_exp2f((a1[0] - M) * CSC);
        const float inv = 1.0f / xg_sum(fmaf(w, l, fmaf(w0, a0[1], w1 * a1[1])));
        l = w * inv; f0 = w0 * inv; f1 = w1 * inv;
      } else {
        if (mine) *(LAS v2f*)(L + L_ST + (aw - 1) * 512 + lane * 8) = v2f{m, l};
        LDS_WAIT();
        ctl_add(ctl + 4 * (C_ML + ph));
      }
      v4f acc[8];
#pragma unroll
      for (int d = 0; d < 8; ++d) acc[d] = v4f{0.f, 0.f, 0.f, 0.f};
      LAS char* rec = L + L_MRG + (g * 8 + (c & 7)) * 16;
#pragma unroll 1
      for (int t = aw; t < 64; t += NATT) {
        const unsigned uv = 128 * ph + 64 + t;
        v4u pf = *(LAS v4u*)(L + poff + t * PREC);
        const float alpha = *(LAS float*)(L + aoff + t * PREC);
#pragma unroll
        for (int d = 0; d < 8; ++d) acc[d] *= alpha;
        if (t == 63) {
          ctl_wait<0>(ctl + 4 * (C_MRG + 2 * ph), 1);
          CFENCE();
#pragma unroll
          for (int d = 0; d < 8; ++d) kf[d] = __builtin_bit_cast(v4u, *(LAS v4f*)(rec + d * 512) * f0);
          PIN8(kf);
        }
        asm volatile("" : "+v"(pf));
        PIN8(acc);
        TA(0);
        if (full < uv + 1) full = ctl_wait<0>(ctl + 4 * C_FULL, uv + 1);
        CFENCE();
        TA(1);
        LAS char* vb = L + (uv % NR) * UNIT + voff;
#pragma unroll
        for (int d = 0; d < 8; ++d) {
          const v2u a = trread(vb + (d >> 1) * 64 + (d & 1) * 8), b = trread(vb + 512 + (d >> 1) * 64 + (d & 1) * 8);
          vf[d] = v4u{a[0], a[1], b[0], b[1]};
        }
        PIN8(vf);
        ctl_wr(ctl + 4 * (C_REL + aw), t + NATT < 64 ? uv + NATT : 128 * (ph + 1) + aw);
        __builtin_amdgcn_sched_barrier(0);
#pragma unroll
        for (int d = 0; d < 8; ++d) acc[d] = mfma(vf[d], pf, acc[d]);
        TA(2);
        if (ph == 1 && t == OA_AT) {
          vm_wait<0>();
          const v2u tg = *(LAS v2u*)(L + L_CTRL + 4 * C_EP);
          if (lane == 0) __hip_atomic_store((GAS unsigned long long*)(ws + W_CF) + FSTR * j, ((unsigned long long)tg[1] << 32) | tg[0], __ATOMIC_RELAXED, __HIP_MEMORY_SCOPE_AGENT);
          if (c < 8) {
#pragma unroll
            for (int s = 0; s < 4; ++s) __builtin_amdgcn_raw_buffer_store_b128(oA[s], rsO, obase + s * 1024, 0, AUX_SC1);
          }
        }
      }
      if (aw != 0) {
        if (c < 8) {
#pragma unroll
          for (int d = 0; d < 8; ++d) *(LAS v4f*)(rec + (aw - 1) * 4096 + d * 512) = acc[d];
        }
        LDS_WAIT();
        ctl_wr(ctl + 4 * (C_MRG + 2 * ph + aw - 1), 1u);
      } else {
        ctl_wait<0>(ctl + 4 * (C_MRG + 2 * ph + 1), 1);
        CFENCE();
#pragma unroll
        for (int d = 0; d < 8; ++d) vf[d] = *(LAS v4u*)(rec + 4096 + d * 512);
#pragma unroll
        for (int s = 0; s < 4; ++s) {
          const v4f x0 = __builtin_elementwise_fma(__builtin_bit_cast(v4f, vf[2 * s]), v4f{f1, f1, f1, f1}, __builtin_elementwise_fma(acc[2 * s], v4f{l, l, l, l}, __builtin_bit_cast(v4f, kf[2 * s])));
          const v4f x1 = __builtin_elementwise_fma(__builtin_bit_cast(v4f, vf[2 * s + 1]), v4f{f1, f1, f1, f1}, __builtin_elementwise_fma(acc[2 * s + 1], v4f{l, l, l, l}, __builtin_bit_cast(v4f, kf[2 * s + 1])));
          v4u o = {pk2(x0[0], x0[1]), pk2(x0[2], x0[3]), pk2(x1[0], x1[1]), pk2(x1[2], x1[3])};
#pragma unroll
          for (int k = 0; k < 4; ++k) o[k] = min(o[k], SENT - 1u);
          if (ph == 0) oA[s] = o;
          else if (mine) __builtin_amdgcn_raw_buffer_store_b128(o, rsO, obase + s * 1024, 4 * 8 * 16384, AUX_SC1);
        }
        STAMP(5 + ph);
      }
    }
  } else {
    const int gw = wave - 2 - (wave >> 2);
    {
      const __amdgpu_buffer_rsrc_t rsQ = __builtin_amdgcn_make_buffer_rsrc((void*)wq, 0, DM * NH * DH * 2, 0x00020000);
      const int wvo = (8 * g * (NH * DH) + 8 * c) * 2, wso = (128 * gw * (NH * DH) + h * DH) * 2;
      const int xrow = ((c < 8 ? bp : bp + 8) * SQ + (c & 7));
      const GAS char* xb = (const GAS char*)x + ((size_t)xrow * DM + 128 * gw + 8 * g) * 2;
#define WQ_ROW(t, jj) ((32 * (((t) + (bp >> 1)) & 3) + ((jj) ^ (4 * (bp & 1)))) * (NH * DH * 2))
      v4u xf[4], w[4][8];
      {
#pragma unroll
        for (int jj = 0; jj < 4; ++jj) w[0][jj] = __builtin_amdgcn_raw_buffer_load_b128(rsQ, wvo, wso + WQ_ROW(0, jj), AUX_NT);
        const unsigned pf = *(const GAS unsigned*)(xb + (hq & 1) * 128);
        __builtin_amdgcn_sched_barrier(0);
        __builtin_amdgcn_s_barrier();
        __builtin_amdgcn_sched_barrier(0);
        asm volatile("" ::"v"(pf));
        vm_wait<0>();
        __builtin_amdgcn_s_sleep(Q_GAP);
        __builtin_amdgcn_sched_barrier(0);
      }
#pragma unroll
      for (int jj = 4; jj < 8; ++jj) w[0][jj] = __builtin_amdgcn_raw_buffer_load_b128(rsQ, wvo, wso + WQ_ROW(0, jj), AUX_NT);
#pragma unroll
      for (int tt = 0; tt < 4; ++tt) xf[tt] = *(const GAS v4u*)(xb + ((tt + (bp >> 1)) & 3) * 64);
#pragma unroll
      for (int t = 1; t < 3; ++t)
#pragma unroll
        for (int jj = 0; jj < 8; ++jj) w[t][jj] = __builtin_amdgcn_raw_buffer_load_b128(rsQ, wvo, wso + WQ_ROW(t, jj), AUX_NT);
      __builtin_amdgcn_sched_barrier(0);
      ctl_add(ctl + 4 * C_QI);
      if (bp & 1) {
#pragma unroll
        for (int t = 0; t < 4; ++t) xf[t] = v4u{xf[t][2], xf[t][3], xf[t][0], xf[t][1]};
      }
      v4f qa[8];
#pragma unroll
      for (int d = 0; d < 8; ++d) qa[d] = v4f{0.f, 0.f, 0.f, 0.f};
#pragma unroll
      for (int t = 0; t < 4; ++t) {
#pragma unroll
        for (int dd = 0; dd < 8; ++dd) {
          v4u f;
#pragma unroll
          for (int v = 0; v < 4; ++v) f[v] = __builtin_amdgcn_perm(w[t][2 * v + 1][dd >> 1], w[t][2 * v][dd >> 1], (dd & 1) ? 0x07060302u : 0x05040100u);
          qa[dd] = mfma(f, xf[t], qa[dd]);
          if (dd & 1) __builtin_amdgcn_sched_barrier(0);
        }
        if (t == 0) {
#pragma unroll
          for (int jj = 0; jj < 8; ++jj) {
            w[3][jj] = __builtin_amdgcn_raw_buffer_load_b128(rsQ, wvo, wso + WQ_ROW(3, jj), AUX_NT);
            __builtin_amdgcn_sched_barrier(0);
          }
        }
      }
      STAMP(16 + gw);
      LAS char* part = L + L_PS + (gw & 3) * 8192 + lane * 16;
      if (gw >= 4) {
        ctl_wait<0>(ctl + 4 * C_Q1, 4);
        CFENCE();
      }
#pragma unroll
      for (int i = 0; i < 4; ++i) {
        v4f lo = {qa[0][i], qa[1][i], qa[2][i], qa[3][i]}, hi = {qa[4][i], qa[5][i], qa[6][i], qa[7][i]};
        if (gw >= 4) { lo += *(LAS v4f*)(part + i * 2048); hi += *(LAS v4f*)(part + i * 2048 + 1024); }
        *(LAS v4f*)(part + i * 2048) = lo;
        *(LAS v4f*)(part + i * 2048 + 1024) = hi;
      }
      LDS_WAIT();
      ctl_add(ctl + 4 * (gw >= 4 ? C_Q2 : C_Q1));
      if (gw == 0) STAMP(7);
      if (gw == 7) STAMP(8);
    }
    const int xc = j & 7, idx = j >> 3, ct = (idx + CT_ROT) & 7, rg = idx >> 3;
    unsigned hb = 0;
#define HBAR(SLP) do { hb += NHELP; ctl_add(ctl + 4 * C_HB); ctl_wait<SLP>(ctl + 4 * C_HB, hb); CFENCE(); } while (0)
    const int pdst = W_P + (idx * 128 + gw * 8 + xc) * 1024;
    v4u wf[16];
    {
      const __amdgpu_buffer_rsrc_t rsW = __builtin_amdgcn_make_buffer_rsrc((void*)wo, 0, DM * NH * DH * 2, 0x00020000);
      ctl_wait<32>(ctl + 4 * C_FULL, WO_AT + 2 * gw);
#pragma unroll
      for (int ks = 0; ks < 16; ++ks) {
        wf[ks] = __builtin_amdgcn_raw_buffer_load_b128(rsW, g * 2048 + c * 16, ((4 * xc + (ks >> 2)) * 128 + (ks & 3) * 32 + 4 * gw) * 2048 + ct * 256, AUX_NT);
        __builtin_amdgcn_s_sleep(WF_SLEEP);
        __builtin_amdgcn_sched_barrier(0);
      }
      LAS char* sw = L + L_STG + gw * PIECE + lane * 16;
      LAS char* sr = L + L_STG + 2 * g * PIECE + (c >> 2) * 256 + gw * 32 + (c & 3) * 8;
#pragma unroll
      for (int r = 0; r < 16; ++r) {
        *(LAS v4u*)(sw) = wf[r];
        LDS_WAIT();
        HBAR(2);
        const v2u a0 = trread(sr), b0 = trread(sr + PIECE);
        wf[r] = v4u{a0[0], a0[1], b0[0], b0[1]};
        asm volatile("" : "+v"(wf[r])::"memory");
        HBAR(2);
      }
    }
    const v2u tagv = *(LAS v2u*)(L + L_CTRL + 4 * C_EP);
    const unsigned long long tag = ((unsigned long long)tagv[1] << 32) | tagv[0];
    unsigned tb[4];
    tag_bytes(tagv, tb);
    if (gw == 7) STAMP(9);
    ctl_wait<16>(ctl + 4 * C_FULL, NUNIT - 16);
    ctl_wait<2>(ctl + 4 * C_LDONE, 1);
    if (gw == 7) STAMP(10);
    {
      const int src = (2 * rg + (lane & 1)) * 32 + (gw >> 1) * 8 + xc;
      GAS unsigned long long* fl = (GAS unsigned long long*)(ws + W_CF) + FSTR * src;
      for (unsigned sp = 0; sp < (1u << 16); ++sp) {
        if (__all(__hip_atomic_load(fl, __ATOMIC_RELAXED, __HIP_MEMORY_SCOPE_AGENT) == tag)) break;
        __builtin_amdgcn_s_sleep(G1_SLEEP);
      }
      __builtin_amdgcn_fence(__ATOMIC_ACQUIRE, "wavefront");
    }
    if (gw == 7) STAMP(11);
#pragma unroll 1
    for (int ph = 0; ph < 2; ++ph) {
      v4u f0, f1;
      for (unsigned sp = 0; sp < (1u << 16); ++sp) {
        CFENCE();
        f0 = __builtin_amdgcn_raw_buffer_load_b128(rsO, lane * 16, W_O + ((ph * 4 + rg) * 8 + xc) * 16384 + gw * 2048, AUX_SC1);
        f1 = __builtin_amdgcn_raw_buffer_load_b128(rsO, lane * 16, W_O + ((ph * 4 + rg) * 8 + xc) * 16384 + gw * 2048 + 1024, AUX_SC1);
        const unsigned mxv = max(max(max(f0[0], f0[1]), max(f0[2], f0[3])), max(max(f1[0], f1[1]), max(f1[2], f1[3])));
        if (__all(mxv != SENT)) break;
        __builtin_amdgcn_s_sleep(G1_SLEEP);
      }
      *(LAS v4u*)(L + L_OB + gw * 2048 + lane * 16) = f0;
      *(LAS v4u*)(L + L_OB + gw * 2048 + 1024 + lane * 16) = f1;
      LDS_WAIT();
      HBAR(0);
      v4f oa[4];
#pragma unroll
      for (int k = 0; k < 4; ++k) oa[k] = v4f{0.f, 0.f, 0.f, 0.f};
#pragma unroll
      for (int f = 0; f < 16; ++f) oa[f & 3] = mfma(wf[f], *(LAS v4u*)(L + L_OB + f * 1024 + lane * 16), oa[f & 3]);
      v4u pt = __builtin_bit_cast(v4u, (oa[0] + oa[1]) + (oa[2] + oa[3]));
#pragma unroll
      for (int k = 0; k < 4; ++k) pt[k] = ((pt[k] + ((pt[k] & 0x7F800000u) == 0x7F800000u ? 0u : 0x80u)) & ~0xFFu) | tb[k];
      __builtin_amdgcn_raw_buffer_store_b128(pt, rsO, lane * 16, pdst + ph * 65536, AUX_SC1);
      if (gw == 7) STAMP(24 + ph);
      if (ph == 0) HBAR(0);
    }
    if (gw < 2) {
      GAS v2u* dst = (GAS v2u*)((GAS char*)out + ((size_t)(64 * gw + 16 * rg + c) * DM + 128 * ct + 16 * xc + 4 * g) * 2);
      asm volatile("" : "+v"(dst));
      const v4u tv4 = {tb[0], tb[1], tb[2], tb[3]};
      v2u res;
      for (unsigned sp = 0; sp < (1u << 16); ++sp) {
        CFENCE();
        v4u q[8];
#pragma unroll
        for (int k = 0; k < 8; ++k) q[k] = __builtin_amdgcn_raw_buffer_load_b128(rsO, lane * 16, W_P + ((idx * 2 + gw) * 64 + xc * 8 + k) * 1024, AUX_SC1);
        v4u bad = q[0] ^ tv4;
        v4f s = __builtin_bit_cast(v4f, bad);
#pragma unroll
        for (int k = 1; k < 8; ++k) { const v4u y = q[k] ^ tv4; bad |= y; s += __builtin_bit_cast(v4f, y); }
        res = v2u{pk2(s[0], s[1]), pk2(s[2], s[3])};
        asm volatile("" : "+v"(res));
        if (__all((((bad[0] | bad[1]) | (bad[2] | bad[3])) & 0xFFu) == 0u)) break;
        __builtin_amdgcn_s_sleep(X2_SLEEP);
      }
      *dst = res;
      if (gw == 0 && (j & 63) == 0 && lane == 0) __hip_atomic_store((GAS unsigned long long*)(ws + W_EPOCH) + FSTR * rg, tag, __ATOMIC_RELAXED, __HIP_MEMORY_SCOPE_AGENT);
      STAMP(12 + gw);
    }
  }
}
}

extern "C" void kernel_launch(void* const* d_in, const int* in_sizes, int n_in, void* d_out, int out_size, void* d_ws, size_t ws_size,
                              hipStream_t stream) {
  (void)in_sizes; (void)n_in; (void)out_size; (void)ws_size;
  (void)hipFuncSetAttribute((const void*)mha_block_kernel, hipFuncAttributeMaxDynamicSharedMemorySize, LDS_BYTES);
  mha_block_kernel<<<NWG, NTHR, LDS_BYTES, stream>>>((const unsigned short*)d_in[0], (const unsigned short*)d_in[1], (const unsigned short*)d_in[2],
                                                     (const unsigned short*)d_in[3], (const unsigned short*)d_in[4], (unsigned short*)d_out,
                                                     (unsigned char*)d_ws);
}
```

```cpp
#define SHIFT (-75)
#include <hip/hip_runtime.h>
#include <hip/hip_bf16.h>
#include <stdint.h>

#define GAS __attribute__((address_space(1)))
#define LAS __attribute__((address_space(3)))
typedef unsigned v4u __attribute__((ext_vector_type(4)));
typedef unsigned v2u __attribute__((ext_vector_type(2)));
typedef float v4f __attribute__((ext_vector_type(4)));
typedef float v2f __attribute__((ext_vector_type(2)));
typedef short v4s __attribute__((ext_vector_type(4)));
typedef __bf16 v8bf __attribute__((ext_vector_type(8)));
typedef __bf16 v2bf __attribute__((ext_vector_type(2)));

namespace {
constexpr int NB = 16, SQ = 8, DM = 1024, NH = 32, SK = 2048, DH = 128;
constexpr int NWG = 256, NTHR = 768, NATT = 3, NHELP = 8;
constexpr int PIECE = 1056;
constexpr int UNIT = 8 * PIECE;
constexpr int NR = 14;
constexpr int NUNIT = 256;
constexpr int AUX_NT = 2, AUX_SC1 = 16;
constexpr int WIN = 20;
constexpr int WIN0 = 24, U0 = 24;
constexpr int WINE = 32, UE = 16;
constexpr int WINB = 28;
constexpr int CK_U0 = 24, CK_T0 = 1080 - SHIFT, CK_U1 = 108, CK_T1 = 3530 - SHIFT, CK_U2 = 200, CK_T2 = 6030 - SHIFT;
constexpr int CK_G = 160;
constexpr int UI = 3, QI_GO = 5, Q_GAP = 4;
constexpr int WO_AT = 68, WF_SLEEP = 16;
constexpr int OA_AT = 57;
constexpr int G1_SLEEP = 1, X2_SLEEP = 1, CT_ROT = 2;
constexpr int L_CTRL = NR * UNIT;
constexpr int PREC = 544;
constexpr int L_PS = L_CTRL + 256;
constexpr int L_OB = L_PS;
constexpr int L_MRG = L_PS + 32 * PREC;
constexpr int L_ST = L_PS + 64 * PREC;
constexpr int L_STG = L_ST + 1024;
constexpr int LDS_BYTES = 160 * 1024;
static_assert(L_STG + 8 * PIECE <= LDS_BYTES && 16384 <= 32 * PREC && 32 * PREC + 8192 <= 48 * PREC && NR <= 14, "LDS map");
enum { C_FULL = 0, C_REL = 4, C_Q1 = 8, C_Q2 = 9, C_QI = 10, C_HB = 11, C_QR = 12, C_EP = 14, C_ML = 18, C_MRG = 20, C_LDONE = 24 };
constexpr int W_EPOCH = 0, W_CF = 65536, FSTR = 16;
constexpr int W_O = 1 << 20;
constexpr int W_P = 2 << 20;
constexpr int WS_USED = 6 << 20;
constexpr unsigned SENT = 0xFFFFFFFFu;
constexpr float CSC = 0.08838834764831845f * 1.4426950408889634f;
constexpr unsigned SPIN_CAP = 1u << 22;

__device__ __forceinline__ unsigned rfl(unsigned x) { return __builtin_amdgcn_readfirstlane(x); }
__device__ __forceinline__ v4f mfma(v4u a, v4u b, v4f c) {
  return __builtin_amdgcn_mfma_f32_16x16x32_bf16(__builtin_bit_cast(v8bf, a), __builtin_bit_cast(v8bf, b), c, 0, 0, 0);
}
__device__ __forceinline__ unsigned pk2(float lo, float hi) {
  v2f x = {lo, hi};
  return __builtin_bit_cast(unsigned, __builtin_convertvector(x, v2bf));
}
__device__ __forceinline__ v2u trread(LAS char* p) {
  return __builtin_bit_cast(v2u, __builtin_amdgcn_ds_read_tr16_b64_v4i16((LAS v4s*)p));
}
__device__ __forceinline__ float xg_max(float x) {
  auto s = __builtin_amdgcn_permlane16_swap(__float_as_uint(x), __float_as_uint(x), false, false);
  x = fmaxf(__uint_as_float(s[0]), __uint_as_float(s[1]));
  auto t = __builtin_amdgcn_permlane32_swap(__float_as_uint(x), __float_as_uint(x), false, false);
  return fmaxf(__uint_as_float(t[0]), __uint_as_float(t[1]));
}
__device__ __forceinline__ float xg_sum(float x) {
  auto s = __builtin_amdgcn_permlane16_swap(__float_as_uint(x), __float_as_uint(x), false, false);
  x = __uint_as_float(s[0]) + __uint_as_float(s[1]);
  auto t = __builtin_amdgcn_permlane32_swap(__float_as_uint(x), __float_as_uint(x), false, false);
  return __uint_as_float(t[0]) + __uint_as_float(t[1]);
}
#define LDS_WAIT() asm volatile("s_waitcnt lgkmcnt(0)" ::: "memory")
#define CFENCE() asm volatile("" ::: "memory")
#define PIN8(a) asm volatile("" : "+v"(a[0]), "+v"(a[1]), "+v"(a[2]), "+v"(a[3]), "+v"(a[4]), "+v"(a[5]), "+v"(a[6]), "+v"(a[7])::"memory")
template <int N> __device__ __forceinline__ void vm_wait() { asm volatile("s_waitcnt vmcnt(%c0)" ::"n"(N) : "memory"); }
__device__ __forceinline__ unsigned ctl_rd(unsigned addr) {
  unsigned v;
  asm volatile("ds_read_b32 %0, %1\n\ts_waitcnt lgkmcnt(0)" : "=v"(v) : "v"(addr) : "memory");
  return rfl(v);
}
__device__ __forceinline__ void ctl_wr(unsigned addr, unsigned val) {
  asm volatile("s_mov_b64 exec, 1\n\tds_write_b32 %0, %1\n\ts_mov_b64 exec, -1" ::"v"(addr), "v"(val) : "memory");
}
__device__ __forceinline__ unsigned ctl_add(unsigned addr) {
  unsigned v;
  asm volatile("s_mov_b64 exec, 1\n\tds_add_rtn_u32 %0, %1, %2\n\ts_mov_b64 exec, -1\n\ts_waitcnt lgkmcnt(0)" : "=&v"(v) : "v"(addr), "v"(1u) : "memory");
  return rfl(v);
}
template <int SLP> __device__ __forceinline__ unsigned ctl_wait(unsigned addr, unsigned need) {
  unsigned v = ctl_rd(addr);
  for (unsigned sp = 0; v < need && sp < SPIN_CAP; ++sp) {
    __builtin_amdgcn_s_sleep(SLP);
    v = ctl_rd(addr);
  }
  return v;
}

__device__ __forceinline__ void tag_bytes(v2u tagv, unsigned (&tb)[4]) {
  const unsigned long long e = (((unsigned long long)tagv[1] << 32) | tagv[0]) - 1ull;
  const unsigned hs = (unsigned)(e >> 8) * 0x9E3779B1u ^ (unsigned)(e >> 40) * 0x85EBCA77u;
#pragma unroll
  for (int k = 0; k < 4; ++k) tb[k] = rfl((tagv[0] + ((hs >> (8 * k)) & 0xFFu) % 254u) & 0xFFu);
}

#define PUBL(k) ".if %c[b] == " #k "\n\ts_mov_b64 exec, 1\n\tds_write_b32 %[fa], v23\n\ts_mov_b64 exec, -1\n\t.endif\n\t"
#define PC(k, VO, IMM) "s_waitcnt vmcnt(%c[w])\n\t" PUBL(k) "buffer_load_dwordx4 " VO ", %[rs], %[so] offen offset:" IMM " nt lds\n\t"
template <int W>
__device__ __forceinline__ void dma_units(__amdgpu_buffer_rsrc_t rs, unsigned soff, unsigned uend, unsigned& m0b, unsigned& u, unsigned& lim, unsigned vo0,
                                          unsigned vo1, unsigned vctl, unsigned faddr) {
  unsigned sp;
  asm volatile(
      "0:\n\t"
      "s_cmp_lt_u32 %[u], %[lim]\n\t"
      "s_cbranch_scc1 2f\n\t"
      "s_mov_b32 %[sp], 0\n"
      "1:\n\t"
      "ds_read_b32 v20, %[vc] offset:16\n\tds_read_b32 v21, %[vc] offset:20\n\tds_read_b32 v22, %[vc] offset:24\n\t"
      "s_waitcnt lgkmcnt(0)\n\t"
      "v_min3_u32 v20, v20, v21, v22\n\t"
      "s_add_u32 %[sp], %[sp], 1\n\t"
      "v_readfirstlane_b32 %[lim], v20\n\t"
      "s_add_u32 %[lim], %[lim], %c[nr]\n\t"
      "s_cmp_lt_u32 %[u], %[lim]\n\t"
      "s_cbranch_scc1 2f\n\t"
      "s_sleep 1\n\t"
      "s_cmp_lt_u32 %[sp], %c[cap]\n\t"
      "s_cbranch_scc1 1b\n"
      "2:\n\t"
      "s_mov_b32 m0, %[m0b]\n\t"
      "v_mov_b32 v23, %[u]\n\t"
      "v_subrev_u32 v23, %c[a], v23\n\t"
      PC(0, "%[vo0]", "0")
      "ds_read_b32 v20, %[vc] offset:16\n\tds_read_b32 v21, %[vc] offset:20\n\tds_read_b32 v22, %[vc] offset:24\n\t"
      "s_add_u32 m0, m0, 32\n\t"
      PC(1, "%[vo0]", "1024")
      "s_add_u32 m0, m0, 32\n\t"
      PC(2, "%[vo0]", "2048")
      "s_add_u32 m0, m0, 32\n\t"
      PC(3, "%[vo0]", "3072")
      "s_add_u32 m0, m0, 0x1020\n\t"
      PC(4, "%[vo1]", "0")
      "s_waitcnt lgkmcnt(0)\n\t"
      "v_min3_u32 v20, v20, v21, v22\n\t"
      "s_add_u32 m0, m0, 32\n\t"
      PC(5, "%[vo1]", "1024")
      "v_readfirstlane_b32 %[lim], v20\n\t"
      "s_add_u32 %[m0b], %[m0b], %c[unit]\n\t"
      "s_add_u32 m0, m0, 32\n\t"
      PC(6, "%[vo1]", "2048")
      "s_add_u32 %[lim], %[lim], %c[nr]\n\t"
      "s_cmp_lt_u32 %[m0b], %c[ring]\n\t"
      "s_cselect_b32 %[m0b], %[m0b], 0\n\t"
      "s_add_u32 %[u], %[u], 1\n\t"
      "s_add_u32 m0, m0, 32\n\t"
      PC(7, "%[vo1]", "3072")
      "s_add_u32 %[so], %[so], 0x2000\n\t"
      "s_cmp_lt_u32 %[u], %[ue]\n\t"
      "s_cbranch_scc1 0b"
      : [m0b] "+&s"(m0b), [u] "+&s"(u), [lim] "+&s"(lim), [so] "+&s"(soff), [sp] "=&s"(sp)
      : [ue] "s"(uend), [w] "n"(W - 1), [a] "n"((W - 1) / 8), [b] "n"((W - 1) % 8), [nr] "n"(NR), [cap] "n"(SPIN_CAP), [unit] "n"(UNIT), [ring] "n"(NR * UNIT), [rs] "s"(rs),
        [vo0] "v"(vo0), [vo1] "v"(vo1), [vc] "v"(vctl), [fa] "v"(faddr)
      : "memory", "scc", "v20", "v21", "v22", "v23");
}

__global__ void __launch_bounds__(NTHR, 3)
mha_block_kernel(const unsigned short* __restrict__ x, const unsigned short* __restrict__ kc, const unsigned short* __restrict__ vc,
                 const unsigned short* __restrict__ wq, const unsigned short* __restrict__ wo, unsigned short* __restrict__ out,
                 unsigned char* __restrict__ ws) {
  extern __shared__ __attribute__((aligned(16))) char smem[];
  LAS char* L = (LAS char*)smem;
  const unsigned lbase = (unsigned)(size_t)L;
  const int lane = threadIdx.x & 63, wave = rfl(threadIdx.x >> 6);
  const int g = lane >> 4, c = lane & 15;
  const int j = blockIdx.x, hq = (j >> 3) & 3, h = 4 * (j & 7) + hq, bp = j >> 5;
  const unsigned ctl = lbase + L_CTRL;
  const __amdgpu_buffer_rsrc_t rsO = __builtin_amdgcn_make_buffer_rsrc((void*)ws, 0, WS_USED, 0x00020000);

  if (wave == 0) {
    __builtin_amdgcn_s_setprio(3);
    __builtin_amdgcn_s_waitcnt(0x0F70);
    const unsigned long long tl0 = __builtin_amdgcn_s_memrealtime();
    const unsigned vo0 = lane * 16, vo1 = lane * 16 + 4096;
    const unsigned sbase = (unsigned)(bp * NH + h) * (SK * DH * 2);
    unsigned m0b = 0, u = 0, lim = NR;
    unsigned ub = 0;
    static_assert(UI >= 2 && UI + (WIN0 - 1) / 8 < U0 && U0 <= CK_U0 && CK_U0 < CK_U1 && CK_U1 < CK_U2 && CK_U2 < NUNIT - UE, "segment order");
#pragma unroll 1
    while (u < NUNIT) {
      const __amdgpu_buffer_rsrc_t rs = __builtin_amdgcn_make_buffer_rsrc((void*)((u & 64) ? vc : kc), 0, NB * NH * SK * DH * 2, 0x00020000);
      const unsigned soff = sbase + (u >> 7) * (8u * NH * SK * DH * 2) + (u & 63) * 8192u;
      unsigned ue = (u | 63) + 1;
      if (u < U0) {
        if (u == 2) __builtin_amdgcn_s_barrier();
        if (u == UI) {
          vm_wait<0>(); ctl_wr(ctl + 4 * C_FULL, UI);
          ctl_wait<1>(ctl + 4 * C_QI, QI_GO);
        }
        const bool early = u < UI + (WIN0 - 1) / 8;
        dma_units<WIN0>(rs, soff, min(ue, early ? u + 1 : (unsigned)U0), m0b, u, lim, vo0, vo1, ctl, early ? ctl + 252 : ctl + 4 * C_FULL);
        if (u <= 2) lim = NR;
      } else if (u >= NUNIT - UE) {
        dma_units<WINE>(rs, soff, ue, m0b, u, lim, vo0, vo1, ctl, ctl + 4 * C_FULL);
      } else {
        if (u == CK_U0 || u == CK_U1 || u == CK_U2) {
          const int late = (int)(unsigned)(__builtin_amdgcn_s_memrealtime() - tl0) - (u == CK_U0 ? CK_T0 : (u == CK_U1 ? CK_T1 : CK_T2));
          ub = u + (late > 0 ? min((late * CK_G) >> 10, NUNIT) : 0);
        }
        ue = min(ue, u < CK_U0 ? (unsigned)CK_U0 : (u < CK_U1 ? (unsigned)CK_U1 : (u < CK_U2 ? (unsigned)CK_U2 : (unsigned)(NUNIT - UE))));
        if (u < ub) dma_units<WINB>(rs, soff, min(ue, ub), m0b, u, lim, vo0, vo1, ctl, ctl + 4 * C_FULL);
        else dma_units<WIN>(rs, soff, ue, m0b, u, lim, vo0, vo1, ctl, ctl + 4 * C_FULL);
      }
    }
    ctl_wr(ctl + 4 * C_LDONE, 1u);
    if (WINE > 48) { vm_wait<48>(); ctl_wr(ctl + 4 * C_FULL, NUNIT - 6); }
    if (WINE > 40) { vm_wait<40>(); ctl_wr(ctl + 4 * C_FULL, NUNIT - 5); }
    if (WINE > 32) { vm_wait<32>(); ctl_wr(ctl + 4 * C_FULL, NUNIT - 4); }
    if (WINE > 24) { vm_wait<24>(); ctl_wr(ctl + 4 * C_FULL, NUNIT - 3); }
    vm_wait<16>(); ctl_wr(ctl + 4 * C_FULL, NUNIT - 2);
    vm_wait<8>(); ctl_wr(ctl + 4 * C_FULL, NUNIT - 1);
    vm_wait<0>(); ctl_wr(ctl + 4 * C_FULL, NUNIT);
  } else if ((wave & 3) == 0 || wave == 1) {
    const int aw = wave == 1 ? 2 : (wave >> 2) - 1;
    if (aw == 0) { *(LAS unsigned*)(L + L_CTRL + 4 * lane) = 0u; LDS_WAIT(); }
    __builtin_amdgcn_s_barrier();
    const unsigned koff = (c & 7) * PIECE + (c >> 3) * 256 + g * 16;
    const unsigned voff = (4 * (g & 1) + (c >> 2)) * PIECE + (g >> 1) * 256 + (c & 3) * 16;
    const int obase = W_O + ((bp >> 1) * 8 + (j & 7)) * 16384 + hq * 4096 + g * 256 + ((bp & 1) * 8 + (c & 7)) * 16;
    v4u oA[4];
#pragma unroll
    for (int s = 0; s < 4; ++s) oA[s] = v4u{0u, 0u, 0u, 0u};
    {
      const v4u sv = {SENT, SENT, SENT, SENT};
      if (aw == 0) {
        const unsigned long long tag = __hip_atomic_load((GAS unsigned long long*)(ws + W_EPOCH) + FSTR * (j >> 6), __ATOMIC_RELAXED, __HIP_MEMORY_SCOPE_AGENT) + 1ull;
#pragma unroll
        for (int s = 0; s < 4; ++s) __builtin_amdgcn_raw_buffer_store_b128(sv, rsO, obase + (c >> 3) * (4 * 8 * 16384), s * 1024, AUX_SC1);
        if (lane == 0) *(LAS v2u*)(L + L_CTRL + 4 * C_EP) = v2u{(unsigned)tag, (unsigned)(tag >> 32)};
      }
    }
    v4u kf[8], vf[8];
    unsigned full = ctl_wait<1>(ctl + 4 * C_FULL, aw + 1);
    CFENCE();
#pragma unroll
    for (int i = 0; i < 8; ++i) kf[i] = *(LAS v4u*)(L + aw * UNIT + koff + (i >> 2) * 512 + (i & 3) * 64);
    PIN8(kf);
    ctl_wr(ctl + 4 * (C_REL + aw), aw + NATT);
    full = ctl_wait<1>(ctl + 4 * C_FULL, aw + NATT + 1);
    CFENCE();
#pragma unroll
    for (int i = 0; i < 8; ++i) vf[i] = *(LAS v4u*)(L + (aw + NATT) * UNIT + koff + (i >> 2) * 512 + (i & 3) * 64);
    PIN8(vf);
    ctl_wr(ctl + 4 * (C_REL + aw), aw + 2 * NATT);
    ctl_wait<1>(ctl + 4 * C_Q2, 4);
    CFENCE();
    v4u qf[4];
#pragma unroll
    for (int s = 0; s < 4; ++s) {
      LAS char* qp = L + L_PS + g * 2048 + (s * 16 + c) * 16;
      const v4f lo = (*(LAS v4f*)(qp) + *(LAS v4f*)(qp + 8192)) + (*(LAS v4f*)(qp + 16384) + *(LAS v4f*)(qp + 24576));
      const v4f hi = (*(LAS v4f*)(qp + 1024) + *(LAS v4f*)(qp + 9216)) + (*(LAS v4f*)(qp + 17408) + *(LAS v4f*)(qp + 25600));
      qf[s] = v4u{pk2(lo[0], lo[1]), pk2(lo[2], lo[3]), pk2(hi[0], hi[1]), pk2(hi[2], hi[3])};
      if (s & 1) asm volatile("" : "+v"(qf[s - 1]), "+v"(qf[s])::"memory");
    }
    ctl_add(ctl + 4 * C_QR);
    const unsigned poff = L_PS + (g * 8 + (c & 7)) * 16, aoff = L_PS + 512 + (c & 7) * 4;
#pragma unroll 1
    for (int ph = 0; ph < 2; ++ph) {
      const bool mine = (c >> 3) == ph;
      float m = -INFINITY, l = 0.f;
#pragma unroll 1
      for (int t = aw; t < 64; t += NATT) {
        const unsigned uk = 128 * ph + t;
        if (uk >= 2 * NATT) {
          if (full < uk + 1) full = ctl_wait<0>(ctl + 4 * C_FULL, uk + 1);
          CFENCE();
          LAS char* kb = L + (uk % NR) * UNIT + koff;
#pragma unroll
          for (int i = 0; i < 8; ++i) kf[i] = *(LAS v4u*)(kb + (i >> 2) * 512 + (i & 3) * 64);
          PIN8(kf);
          ctl_wr(ctl + 4 * (C_REL + aw), t + NATT < 64 ? uk + NATT : 128 * ph + 64 + aw);
          __builtin_amdgcn_sched_barrier(0);
        } else if (uk >= NATT) {
#pragma unroll
          for (int i = 0; i < 8; ++i) kf[i] = vf[i];
        }
        v4f s0 = {0.f, 0.f, 0.f, 0.f}, s1 = {0.f, 0.f, 0.f, 0.f};
#pragma unroll
        for (int s = 0; s < 4; ++s) { s0 = mfma(kf[s], qf[s], s0); s1 = mfma(kf[4 + s], qf[s], s1); }
        float mx = fmaxf(fmaxf(fmaxf(s0[0], s0[1]), fmaxf(s0[2], s0[3])), fmaxf(fmaxf(s1[0], s1[1]), fmaxf(s1[2], s1[3])));
        mx = xg_max(mx);
        const float mn = fmaxf(m, mx);
        const float alpha = __builtin_amdgcn_exp2f((m - mn) * CSC);
        const float mc = mn * CSC;
        m = mn;
        float p[8];
#pragma unroll
        for (int i = 0; i < 4; ++i) { p[i] = __builtin_amdgcn_exp2f(fmaf(s0[i], CSC, -mc)); p[4 + i] = __builtin_amdgcn_exp2f(fmaf(s1[i], CSC, -mc)); }
        l = fmaf(l, alpha, ((p[0] + p[1]) + (p[2] + p[3])) + ((p[4] + p[5]) + (p[6] + p[7])));
        if (uk < NATT) ctl_wait<0>(ctl + 4 * C_QR, NATT);
        if (mine) {
          *(LAS v4u*)(L + poff + t * PREC) = v4u{pk2(p[0], p[1]), pk2(p[2], p[3]), pk2(p[4], p[5]), pk2(p[6], p[7])};
          if (g == 0) *(LAS float*)(L + aoff + t * PREC) = alpha;
        }
      }
      float f0 = 0.f, f1 = 0.f;
      if (aw == 0) {
        ctl_wait<0>(ctl + 4 * (C_ML + ph), 2);
        CFENCE();
        const v2f a0 = *(LAS v2f*)(L + L_ST + lane * 8), a1 = *(LAS v2f*)(L + L_ST + 512 + lane * 8);
        const float M = fmaxf(m, fmaxf(a0[0], a1[0]));
        const float w = __builtin_amdgcn_exp2f((m - M) * CSC), w0 = __builtin_amdgcn_exp2f((a0[0] - M) * CSC), w1 = __builtin_amdgcn_exp2f((a1[0] - M) * CSC);
        const float inv = 1.0f / xg_sum(fmaf(w, l, fmaf(w0, a0[1], w1 * a1[1])));
        l = w * inv; f0 = w0 * inv; f1 = w1 * inv;
      } else {
        if (mine) *(LAS v2f*)(L + L_ST + (aw - 1) * 512 + lane * 8) = v2f{m, l};
        LDS_WAIT();
        ctl_add(ctl + 4 * (C_ML + ph));
      }
      v4f acc[8];
#pragma unroll
      for (int d = 0; d < 8; ++d) acc[d] = v4f{0.f, 0.f, 0.f, 0.f};
      LAS char* rec = L + L_MRG + (g * 8 + (c & 7)) * 16;
#pragma unroll 1
      for (int t = aw; t < 64; t += NATT) {
        const unsigned uv = 128 * ph + 64 + t;
        v4u pf = *(LAS v4u*)(L + poff + t * PREC);
        const float alpha = *(LAS float*)(L + aoff + t * PREC);
#pragma unroll
        for (int d = 0; d < 8; ++d) acc[d] *= alpha;
        if (t == 63) {
          ctl_wait<0>(ctl + 4 * (C_MRG + 2 * ph), 1);
          CFENCE();
#pragma unroll
          for (int d = 0; d < 8; ++d) kf[d] = __builtin_bit_cast(v4u, *(LAS v4f*)(rec + d * 512) * f0);
          PIN8(kf);
        }
        asm volatile("" : "+v"(pf));
        PIN8(acc);
        if (full < uv + 1) full = ctl_wait<0>(ctl + 4 * C_FULL, uv + 1);
        CFENCE();
        LAS char* vb = L + (uv % NR) * UNIT + voff;
#pragma unroll
        for (int d = 0; d < 8; ++d) {
          const v2u a = trread(vb + (d >> 1) * 64 + (d & 1) * 8), b = trread(vb + 512 + (d >> 1) * 64 + (d & 1) * 8);
          vf[d] = v4u{a[0], a[1], b[0], b[1]};
        }
        PIN8(vf);
        ctl_wr(ctl + 4 * (C_REL + aw), t + NATT < 64 ? uv + NATT : 128 * (ph + 1) + aw);
        __builtin_amdgcn_sched_barrier(0);
#pragma unroll
        for (int d = 0; d < 8; ++d) acc[d] = mfma(vf[d], pf, acc[d]);
        if (ph == 1 && t == OA_AT) {
          vm_wait<0>();
          const v2u tg = *(LAS v2u*)(L + L_CTRL + 4 * C_EP);
          if (lane == 0) __hip_atomic_store((GAS unsigned long long*)(ws + W_CF) + FSTR * j, ((unsigned long long)tg[1] << 32) | tg[0], __ATOMIC_RELAXED, __HIP_MEMORY_SCOPE_AGENT);
          if (c < 8) {
#pragma unroll
            for (int s = 0; s < 4; ++s) __builtin_amdgcn_raw_buffer_store_b128(oA[s], rsO, obase + s * 1024, 0, AUX_SC1);
          }
        }
      }
      if (aw != 0) {
        if (c < 8) {
#pragma unroll
          for (int d = 0; d < 8; ++d) *(LAS v4f*)(rec + (aw - 1) * 4096 + d * 512) = acc[d];
        }
        LDS_WAIT();
        ctl_wr(ctl + 4 * (C_MRG + 2 * ph + aw - 1), 1u);
      } else {
        ctl_wait<0>(ctl + 4 * (C_MRG + 2 * ph + 1), 1);
        CFENCE();
#pragma unroll
        for (int d = 0; d < 8; ++d) vf[d] = *(LAS v4u*)(rec + 4096 + d * 512);
#pragma unroll
        for (int s = 0; s < 4; ++s) {
          const v4f x0 = __builtin_elementwise_fma(__builtin_bit_cast(v4f, vf[2 * s]), v4f{f1, f1, f1, f1}, __builtin_elementwise_fma(acc[2 * s], v4f{l, l, l, l}, __builtin_bit_cast(v4f, kf[2 * s])));
          const v4f x1 = __builtin_elementwise_fma(__builtin_bit_cast(v4f, vf[2 * s + 1]), v4f{f1, f1, f1, f1}, __builtin_elementwise_fma(acc[2 * s + 1], v4f{l, l, l, l}, __builtin_bit_cast(v4f, kf[2 * s + 1])));
          v4u o = {pk2(x0[0], x0[1]), pk2(x0[2], x0[3]), pk2(x1[0], x1[1]), pk2(x1[2], x1[3])};
#pragma unroll
          for (int k = 0; k < 4; ++k) o[k] = min(o[k], SENT - 1u);
          if (ph == 0) oA[s] = o;
          else if (mine) __builtin_amdgcn_raw_buffer_store_b128(o, rsO, obase + s * 1024, 4 * 8 * 16384, AUX_SC1);
        }
      }
    }
  } else {
    const int gw = wave - 2 - (wave >> 2);
    {
      const __amdgpu_buffer_rsrc_t rsQ = __builtin_amdgcn_make_buffer_rsrc((void*)wq, 0, DM * NH * DH * 2, 0x00020000);
      const int wvo = (8 * g * (NH * DH) + 8 * c) * 2, wso = (128 * gw * (NH * DH) + h * DH) * 2;
      const int xrow = ((c < 8 ? bp : bp + 8) * SQ + (c & 7));
      const GAS char* xb = (const GAS char*)x + ((size_t)xrow * DM + 128 * gw + 8 * g) * 2;
#define WQ_ROW(t, jj) ((32 * (((t) + (bp >> 1)) & 3) + ((jj) ^ (4 * (bp & 1)))) * (NH * DH * 2))
      v4u xf[4], w[4][8];
      {
#pragma unroll
        for (int jj = 0; jj < 4; ++jj) w[0][jj] = __builtin_amdgcn_raw_buffer_load_b128(rsQ, wvo, wso + WQ_ROW(0, jj), AUX_NT);
        const unsigned pf = *(const GAS unsigned*)(xb + (hq & 1) * 128);
        __builtin_amdgcn_sched_barrier(0);
        __builtin_amdgcn_s_barrier();
        __builtin_amdgcn_sched_barrier(0);
        asm volatile("" ::"v"(pf));
        vm_wait<0>();
        __builtin_amdgcn_s_sleep(Q_GAP);
        __builtin_amdgcn_sched_barrier(0);
      }
#pragma unroll
      for (int jj = 4; jj < 8; ++jj) w[0][jj] = __builtin_amdgcn_raw_buffer_load_b128(rsQ, wvo, wso + WQ_ROW(0, jj), AUX_NT);
#pragma unroll
      for (int tt = 0; tt < 4; ++tt) xf[tt] = *(const GAS v4u*)(xb + ((tt + (bp >> 1)) & 3) * 64);
#pragma unroll
      for (int t = 1; t < 3; ++t)
#pragma unroll
        for (int jj = 0; jj < 8; ++jj) w[t][jj] = __builtin_amdgcn_raw_buffer_load_b128(rsQ, wvo, wso + WQ_ROW(t, jj), AUX_NT);
      __builtin_amdgcn_sched_barrier(0);
      ctl_add(ctl + 4 * C_QI);
      if (bp & 1) {
#pragma unroll
        for (int t = 0; t < 4; ++t) xf[t] = v4u{xf[t][2], xf[t][3], xf[t][0], xf[t][1]};
      }
      v4f qa[8];
#pragma unroll
      for (int d = 0; d < 8; ++d) qa[d] = v4f{0.f, 0.f, 0.f, 0.f};
#pragma unroll
      for (int t = 0; t < 4; ++t) {
#pragma unroll
        for (int dd = 0; dd < 8; ++dd) {
          v4u f;
#pragma unroll
          for (int v = 0; v < 4; ++v) f[v] = __builtin_amdgcn_perm(w[t][2 * v + 1][dd >> 1], w[t][2 * v][dd >> 1], (dd & 1) ? 0x07060302u : 0x05040100u);
          qa[dd] = mfma(f, xf[t], qa[dd]);
          if (dd & 1) __builtin_amdgcn_sched_barrier(0);
        }
        if (t == 0) {
#pragma unroll
          for (int jj = 0; jj < 8; ++jj) {
            w[3][jj] = __builtin_amdgcn_raw_buffer_load_b128(rsQ, wvo, wso + WQ_ROW(3, jj), AUX_NT);
            __builtin_amdgcn_sched_barrier(0);
          }
        }
      }
      LAS char* part = L + L_PS + (gw & 3) * 8192 + lane * 16;
      if (gw >= 4) {
        ctl_wait<0>(ctl + 4 * C_Q1, 4);
        CFENCE();
      }
#pragma unroll
      for (int i = 0; i < 4; ++i) {
        v4f lo = {qa[0][i], qa[1][i], qa[2][i], qa[3][i]}, hi = {qa[4][i], qa[5][i], qa[6][i], qa[7][i]};
        if (gw >= 4) { lo += *(LAS v4f*)(part + i * 2048); hi += *(LAS v4f*)(part + i * 2048 + 1024); }
        *(LAS v4f*)(part + i * 2048) = lo;
        *(LAS v4f*)(part + i * 2048 + 1024) = hi;
      }
      LDS_WAIT();
      ctl_add(ctl + 4 * (gw >= 4 ? C_Q2 : C_Q1));
    }
    const int xc = j & 7, idx = j >> 3, ct = (idx + CT_ROT) & 7, rg = idx >> 3;
    unsigned hb = 0;
#define HBAR(SLP) do { hb += NHELP; ctl_add(ctl + 4 * C_HB); ctl_wait<SLP>(ctl + 4 * C_HB, hb); CFENCE(); } while (0)
    const int pdst = W_P + (idx * 128 + gw * 8 + xc) * 1024;
    v4u wf[16];
    {
      const __amdgpu_buffer_rsrc_t rsW = __builtin_amdgcn_make_buffer_rsrc((void*)wo, 0, DM * NH * DH * 2, 0x00020000);
      ctl_wait<32>(ctl + 4 * C_FULL, WO_AT + 2 * gw);
#pragma unroll
      for (int ks = 0; ks < 16; ++ks) {
        wf[ks] = __builtin_amdgcn_raw_buffer_load_b128(rsW, g * 2048 + c * 16, ((4 * xc + (ks >> 2)) * 128 + (ks & 3) * 32 + 4 * gw) * 2048 + ct * 256, AUX_NT);
        __builtin_amdgcn_s_sleep(WF_SLEEP);
        __builtin_amdgcn_sched_barrier(0);
      }
      LAS char* sw = L + L_STG + gw * PIECE + lane * 16;
      LAS char* sr = L + L_STG + 2 * g * PIECE + (c >> 2) * 256 + gw * 32 + (c & 3) * 8;
#pragma unroll
      for (int r = 0; r < 16; ++r) {
        *(LAS v4u*)(sw) = wf[r];
        LDS_WAIT();
        HBAR(2);
        const v2u a0 = trread(sr), b0 = trread(sr + PIECE);
        wf[r] = v4u{a0[0], a0[1], b0[0], b0[1]};
        asm volatile("" : "+v"(wf[r])::"memory");
        HBAR(2);
      }
    }
    const v2u tagv = *(LAS v2u*)(L + L_CTRL + 4 * C_EP);
    const unsigned long long tag = ((unsigned long long)tagv[1] << 32) | tagv[0];
    unsigned tb[4];
    tag_bytes(tagv, tb);
    ctl_wait<16>(ctl + 4 * C_FULL, NUNIT - 16);
    ctl_wait<2>(ctl + 4 * C_LDONE, 1);
    {
      const int src = (2 * rg + (lane & 1)) * 32 + (gw >> 1) * 8 + xc;
      GAS unsigned long long* fl = (GAS unsigned long long*)(ws + W_CF) + FSTR * src;
      for (unsigned sp = 0; sp < (1u << 16); ++sp) {
        if (__all(__hip_atomic_load(fl, __ATOMIC_RELAXED, __HIP_MEMORY_SCOPE_AGENT) == tag)) break;
        __builtin_amdgcn_s_sleep(G1_SLEEP);
      }
      __builtin_amdgcn_fence(__ATOMIC_ACQUIRE, "wavefront");
    }
#pragma unroll 1
    for (int ph = 0; ph < 2; ++ph) {
      v4u f0, f1;
      for (unsigned sp = 0; sp < (1u << 16); ++sp) {
        CFENCE();
        f0 = __builtin_amdgcn_raw_buffer_load_b128(rsO, lane * 16, W_O + ((ph * 4 + rg) * 8 + xc) * 16384 + gw * 2048, AUX_SC1);
        f1 = __builtin_amdgcn_raw_buffer_load_b128(rsO, lane * 16, W_O + ((ph * 4 + rg) * 8 + xc) * 16384 + gw * 2048 + 1024, AUX_SC1);
        const unsigned mxv = max(max(max(f0[0], f0[1]), max(f0[2], f0[3])), max(max(f1[0], f1[1]), max(f1[2], f1[3])));
        if (__all(mxv != SENT)) break;
        __builtin_amdgcn_s_sleep(G1_SLEEP);
      }
      *(LAS v4u*)(L + L_OB + gw * 2048 + lane * 16) = f0;
      *(LAS v4u*)(L + L_OB + gw * 2048 + 1024 + lane * 16) = f1;
      LDS_WAIT();
      HBAR(0);
      v4f oa[4];
#pragma unroll
      for (int k = 0; k < 4; ++k) oa[k] = v4f{0.f, 0.f, 0.f, 0.f};
#pragma unroll
      for (int f = 0; f < 16; ++f) oa[f & 3] = mfma(wf[f], *(LAS v4u*)(L + L_OB + f * 1024 + lane * 16), oa[f & 3]);
      v4u pt = __builtin_bit_cast(v4u, (oa[0] + oa[1]) + (oa[2] + oa[3]));
#pragma unroll
      for (int k = 0; k < 4; ++k) pt[k] = ((pt[k] + ((pt[k] & 0x7F800000u) == 0x7F800000u ? 0u : 0x80u)) & ~0xFFu) | tb[k];
      __builtin_amdgcn_raw_buffer_store_b128(pt, rsO, lane * 16, pdst + ph * 65536, AUX_SC1);
      if (ph == 0) HBAR(0);
    }
    if (gw < 2) {
      GAS v2u* dst = (GAS v2u*)((GAS char*)out + ((size_t)(64 * gw + 16 * rg + c) * DM + 128 * ct + 16 * xc + 4 * g) * 2);
      asm volatile("" : "+v"(dst));
      const v4u tv4 = {tb[0], tb[1], tb[2], tb[3]};
      v2u res;
      for (unsigned sp = 0; sp < (1u << 16); ++sp) {
        CFENCE();
        v4u q[8];
#pragma unroll
        for (int k = 0; k < 8; ++k) q[k] = __builtin_amdgcn_raw_buffer_load_b128(rsO, lane * 16, W_P + ((idx * 2 + gw) * 64 + xc * 8 + k) * 1024, AUX_SC1);
        v4u bad = q[0] ^ tv4;
        v4f s = __builtin_bit_cast(v4f, bad);
#pragma unroll
        for (int k = 1; k < 8; ++k) { const v4u y = q[k] ^ tv4; bad |= y; s += __builtin_bit_cast(v4f, y); }
        res = v2u{pk2(s[0], s[1]), pk2(s[2], s[3])};
        asm volatile("" : "+v"(res));
        if (__all((((bad[0] | bad[1]) | (bad[2] | bad[3])) & 0xFFu) == 0u)) break;
        __builtin_amdgcn_s_sleep(X2_SLEEP);
      }
      *dst = res;
      if (gw == 0 && (j & 63) == 0 && lane == 0) __hip_atomic_store((GAS unsigned long long*)(ws + W_EPOCH) + FSTR * rg, tag, __ATOMIC_RELAXED, __HIP_MEMORY_SCOPE_AGENT);
    }
  }
}
}

extern "C" void kernel_launch(void* const* d_in, const int* in_sizes, int n_in, void* d_out, int out_size, void* d_ws, size_t ws_size,
                              hipStream_t stream) {
  (void)in_sizes; (void)n_in; (void)out_size; (void)ws_size;
  (void)hipFuncSetAttribute((const void*)mha_block_kernel, hipFuncAttributeMaxDynamicSharedMemorySize, LDS_BYTES);
  mha_block_kernel<<<NWG, NTHR, LDS_BYTES, stream>>>((const unsigned short*)d_in[0], (const unsigned short*)d_in[1], (const unsigned short*)d_in[2],
                                                     (const unsigned short*)d_in[3], (const unsigned short*)d_in[4], (unsigned short*)d_out,
                                                     (unsigned char*)d_ws);
}
```

```cpp
#define STAMPS 1
#define WHAT 5
#define CK_T0 1230
#define CK_T1 3680
#define CK_T2 6180
#include <hip/hip_runtime.h>
#include <hip/hip_bf16.h>
#include <stdint.h>

#define GAS __attribute__((address_space(1)))
#define LAS __attribute__((address_space(3)))
typedef unsigned v4u __attribute__((ext_vector_type(4)));
typedef unsigned v2u __attribute__((ext_vector_type(2)));
typedef float v4f __attribute__((ext_vector_type(4)));
typedef float v2f __attribute__((ext_vector_type(2)));
typedef short v4s __attribute__((ext_vector_type(4)));
typedef __bf16 v8bf __attribute__((ext_vector_type(8)));
typedef __bf16 v2bf __attribute__((ext_vector_type(2)));

#define WIN 20
#define WIN0 24
#define U0 24
#define UI 3
#define X2_SLEEP 1
#define APRIO 0
#define QR_LATE 0
#define CK_TE 0
#define UE2 6
#define QI_GO 5
#define Q_GAP 4
#define WINB 28
#define CK_U0 24
#define CK_U1 108
#define CK_U2 200
#define CT_ROT 2
#define WO_AT 68
#define CK_G 160
#define WINE 32
#define UE 16
#define WF_SLEEP 16
#define G1_SLEEP 1
#define OA_AT 57

namespace {
constexpr int NB = 16, SQ = 8, DM = 1024, NH = 32, SK = 2048, DH = 128;
constexpr int NWG = 256, NTHR = 768, NATT = 3, NHELP = 8;
constexpr int PIECE = 1056;
constexpr int UNIT = 8 * PIECE;
constexpr int NR = 14;
constexpr int NUNIT = 256;
constexpr int AUX_NT = 2, AUX_SC1 = 16;
constexpr int L_CTRL = NR * UNIT;
constexpr int PREC = 544;
constexpr int L_PS = L_CTRL + 256;
constexpr int L_OB = L_PS;
constexpr int L_MRG = L_PS + 32 * PREC;
constexpr int L_ST = L_PS + 64 * PREC;
constexpr int L_STG = L_ST + 1024;
constexpr int LDS_BYTES = 160 * 1024;
static_assert(L_STG + 8 * PIECE <= LDS_BYTES && 16384 <= 32 * PREC && 32 * PREC + 8192 <= 48 * PREC && NR <= 14, "LDS map");
enum { C_FULL = 0, C_REL = 4, C_Q1 = 8, C_Q2 = 9, C_QI = 10, C_HB = 11, C_QR = 12, C_EP = 14, C_ML = 18, C_MRG = 20, C_LDONE = 24 };
constexpr int W_EPOCH = 0, W_CF = 65536, FSTR = 16;
constexpr int W_O = 1 << 20;
constexpr int W_P = 2 << 20;
constexpr int WS_USED = 6 << 20;
constexpr unsigned SENT = 0xFFFFFFFFu;
constexpr float CSC = 0.08838834764831845f * 1.4426950408889634f;
constexpr unsigned SPIN_CAP = 1u << 22;

__device__ __forceinline__ unsigned rfl(unsigned x) { return __builtin_amdgcn_readfirstlane(x); }
__device__ __forceinline__ v4f mfma(v4u a, v4u b, v4f c) {
  return __builtin_amdgcn_mfma_f32_16x16x32_bf16(__builtin_bit_cast(v8bf, a), __builtin_bit_cast(v8bf, b), c, 0, 0, 0);
}
__device__ __forceinline__ unsigned pk2(float lo, float hi) {
  v2f x = {lo, hi};
  return __builtin_bit_cast(unsigned, __builtin_convertvector(x, v2bf));
}
__device__ __forceinline__ v2u trread(LAS char* p) {
  return __builtin_bit_cast(v2u, __builtin_amdgcn_ds_read_tr16_b64_v4i16((LAS v4s*)p));
}
__device__ __forceinline__ float xg_max(float x) {
  auto s = __builtin_amdgcn_permlane16_swap(__float_as_uint(x), __float_as_uint(x), false, false);
  x = fmaxf(__uint_as_float(s[0]), __uint_as_float(s[1]));
  auto t = __builtin_amdgcn_permlane32_swap(__float_as_uint(x), __float_as_uint(x), false, false);
  return fmaxf(__uint_as_float(t[0]), __uint_as_float(t[1]));
}
__device__ __forceinline__ float xg_sum(float x) {
  auto s = __builtin_amdgcn_permlane16_swap(__float_as_uint(x), __float_as_uint(x), false, false);
  x = __uint_as_float(s[0]) + __uint_as_float(s[1]);
  auto t = __builtin_amdgcn_permlane32_swap(__float_as_uint(x), __float_as_uint(x), false, false);
  return __uint_as_float(t[0]) + __uint_as_float(t[1]);
}
#define STAMP(k) do { if (lane == 0) ((GAS unsigned long long*)(ws + (16u << 20)))[blockIdx.x * 64 + (k)] = __builtin_amdgcn_s_memrealtime(); } while (0)
#define STAMPV(k, v) do { if (lane == 0) ((GAS unsigned long long*)(ws + (16u << 20)))[blockIdx.x * 64 + (k)] = (v); } while (0)
#define LDS_WAIT() asm volatile("s_waitcnt lgkmcnt(0)" ::: "memory")
#define CFENCE() asm volatile("" ::: "memory")
#define PIN8(a) asm volatile("" : "+v"(a[0]), "+v"(a[1]), "+v"(a[2]), "+v"(a[3]), "+v"(a[4]), "+v"(a[5]), "+v"(a[6]), "+v"(a[7])::"memory")
template <int N> __device__ __forceinline__ void vm_wait() { asm volatile("s_waitcnt vmcnt(%c0)" ::"n"(N) : "memory"); }
__device__ __forceinline__ unsigned ctl_rd(unsigned addr) {
  unsigned v;
  asm volatile("ds_read_b32 %0, %1\n\ts_waitcnt lgkmcnt(0)" : "=v"(v) : "v"(addr) : "memory");
  return rfl(v);
}
__device__ __forceinline__ void ctl_wr(unsigned addr, unsigned val) {
  asm volatile("s_mov_b64 exec, 1\n\tds_write_b32 %0, %1\n\ts_mov_b64 exec, -1" ::"v"(addr), "v"(val) : "memory");
}
__device__ __forceinline__ unsigned ctl_add(unsigned addr) {
  unsigned v;
  asm volatile("s_mov_b64 exec, 1\n\tds_add_rtn_u32 %0, %1, %2\n\ts_mov_b64 exec, -1\n\ts_waitcnt lgkmcnt(0)" : "=&v"(v) : "v"(addr), "v"(1u) : "memory");
  return rfl(v);
}
template <int SLP> __device__ __forceinline__ unsigned ctl_wait(unsigned addr, unsigned need) {
  unsigned v = ctl_rd(addr);
  for (unsigned sp = 0; v < need && sp < SPIN_CAP; ++sp) {
    __builtin_amdgcn_s_sleep(SLP);
    v = ctl_rd(addr);
  }
  return v;
}

__device__ __forceinline__ void tag_bytes(v2u tagv, unsigned (&tb)[4]) {
  const unsigned long long e = (((unsigned long long)tagv[1] << 32) | tagv[0]) - 1ull;
  const unsigned hs = (unsigned)(e >> 8) * 0x9E3779B1u ^ (unsigned)(e >> 40) * 0x85EBCA77u;
#pragma unroll
  for (int k = 0; k < 4; ++k) tb[k] = rfl((tagv[0] + ((hs >> (8 * k)) & 0xFFu) % 254u) & 0xFFu);
}

#define PUBL(k) ".if %c[b] == " #k "\n\ts_mov_b64 exec, 1\n\tds_write_b32 %[fa], v23\n\ts_mov_b64 exec, -1\n\t.endif\n\t"
#define PC(k, VO, IMM) "s_waitcnt vmcnt(%c[w])\n\t" PUBL(k) "buffer_load_dwordx4 " VO ", %[rs], %[so] offen offset:" IMM " nt lds\n\t"
template <int W>
__device__ __forceinline__ void dma_units(__amdgpu_buffer_rsrc_t rs, unsigned soff, unsigned uend, unsigned& m0b, unsigned& u, unsigned& lim, unsigned vo0,
                                          unsigned vo1, unsigned vctl, unsigned faddr) {
  unsigned sp;
  asm volatile(
      "0:\n\t"
      "s_cmp_lt_u32 %[u], %[lim]\n\t"
      "s_cbranch_scc1 2f\n\t"
      "s_mov_b32 %[sp], 0\n"
      "1:\n\t"
      "ds_read_b32 v20, %[vc] offset:16\n\tds_read_b32 v21, %[vc] offset:20\n\tds_read_b32 v22, %[vc] offset:24\n\t"
      "s_waitcnt lgkmcnt(0)\n\t"
      "v_min3_u32 v20, v20, v21, v22\n\t"
      "s_add_u32 %[sp], %[sp], 1\n\t"
      "v_readfirstlane_b32 %[lim], v20\n\t"
      "s_add_u32 %[lim], %[lim], %c[nr]\n\t"
      "s_cmp_lt_u32 %[u], %[lim]\n\t"
      "s_cbranch_scc1 2f\n\t"
      "s_sleep 1\n\t"
      "s_cmp_lt_u32 %[sp], %c[cap]\n\t"
      "s_cbranch_scc1 1b\n"
      "2:\n\t"
      "s_mov_b32 m0, %[m0b]\n\t"
      "v_mov_b32 v23, %[u]\n\t"
      "v_subrev_u32 v23, %c[a], v23\n\t"
      PC(0, "%[vo0]", "0")
      "ds_read_b32 v20, %[vc] offset:16\n\tds_read_b32 v21, %[vc] offset:20\n\tds_read_b32 v22, %[vc] offset:24\n\t"
      "s_add_u32 m0, m0, 32\n\t"
      PC(1, "%[vo0]", "1024")
      "s_add_u32 m0, m0, 32\n\t"
      PC(2, "%[vo0]", "2048")
      "s_add_u32 m0, m0, 32\n\t"
      PC(3, "%[vo0]", "3072")
      "s_add_u32 m0, m0, 0x1020\n\t"
      PC(4, "%[vo1]", "0")
      "s_waitcnt lgkmcnt(0)\n\t"
      "v_min3_u32 v20, v20, v21, v22\n\t"
      "s_add_u32 m0, m0, 32\n\t"
      PC(5, "%[vo1]", "1024")
      "v_readfirstlane_b32 %[lim], v20\n\t"
      "s_add_u32 %[m0b], %[m0b], %c[unit]\n\t"
      "s_add_u32 m0, m0, 32\n\t"
      PC(6, "%[vo1]", "2048")
      "s_add_u32 %[lim], %[lim], %c[nr]\n\t"
      "s_cmp_lt_u32 %[m0b], %c[ring]\n\t"
      "s_cselect_b32 %[m0b], %[m0b], 0\n\t"
      "s_add_u32 %[u], %[u], 1\n\t"
      "s_add_u32 m0, m0, 32\n\t"
      PC(7, "%[vo1]", "3072")
      "s_add_u32 %[so], %[so], 0x2000\n\t"
      "s_cmp_lt_u32 %[u], %[ue]\n\t"
      "s_cbranch_scc1 0b"
      : [m0b] "+s"(m0b), [u] "+s"(u), [lim] "+s"(lim), [so] "+s"(soff), [sp] "=&s"(sp)
      : [ue] "s"(uend), [w] "n"(W - 1), [a] "n"((W - 1) / 8), [b] "n"((W - 1) % 8), [nr] "n"(NR), [cap] "n"(SPIN_CAP), [unit] "n"(UNIT), [ring] "n"(NR * UNIT), [rs] "s"(rs),
        [vo0] "v"(vo0), [vo1] "v"(vo1), [vc] "v"(vctl), [fa] "v"(faddr)
      : "memory", "scc", "v20", "v21", "v22", "v23");
}

__global__ void __launch_bounds__(NTHR, 3)
mha_block_kernel(const unsigned short* __restrict__ x, const unsigned short* __restrict__ kc, const unsigned short* __restrict__ vc,
                 const unsigned short* __restrict__ wq, const unsigned short* __restrict__ wo, unsigned short* __restrict__ out,
                 unsigned char* __restrict__ ws) {
  extern __shared__ __attribute__((aligned(16))) char smem[];
  LAS char* L = (LAS char*)smem;
  const unsigned lbase = (unsigned)(size_t)L;
  const int lane = threadIdx.x & 63, wave = rfl(threadIdx.x >> 6);
  const int g = lane >> 4, c = lane & 15;
  const int j = blockIdx.x, hq = (j >> 3) & 3, h = 4 * (j & 7) + hq, bp = j >> 5;
  const unsigned ctl = lbase + L_CTRL;
  const __amdgpu_buffer_rsrc_t rsO = __builtin_amdgcn_make_buffer_rsrc((void*)ws, 0, WS_USED, 0x00020000);

  if (wave == 0) {
    __builtin_amdgcn_s_setprio(3);
    __builtin_amdgcn_s_waitcnt(0x0F70);
    const unsigned long long tl0 = __builtin_amdgcn_s_memrealtime();
    unsigned tv = 0, nstall = 0;
    const unsigned long long tc0 = __builtin_amdgcn_s_memtime();
    const unsigned vo0 = lane * 16, vo1 = lane * 16 + 4096;
    const unsigned sbase = (unsigned)(bp * NH + h) * (SK * DH * 2);
    unsigned m0b = 0, u = 0, lim = NR;
    unsigned ub = 0;
    static_assert(UI >= 2 && UI + (WIN0 - 1) / 8 < U0 && U0 <= CK_U0 && CK_U0 < CK_U1 && CK_U1 < CK_U2 && CK_U2 < NUNIT - UE, "segment order");
#pragma unroll 1
    while (u < NUNIT) {
      const __amdgpu_buffer_rsrc_t rs = __builtin_amdgcn_make_buffer_rsrc((void*)((u & 64) ? vc : kc), 0, NB * NH * SK * DH * 2, 0x00020000);
      const unsigned soff = sbase + (u >> 7) * (8u * NH * SK * DH * 2) + (u & 63) * 8192u;
      unsigned ue = (u | 63) + 1;
      ue = u < 24 ? u + 1 : (u | 7) + 1;
      if ((u & 7) == 0 || u < 24) { const unsigned tn = (unsigned)(__builtin_amdgcn_s_memrealtime() - tl0); tv = lane == (u < 24 ? 32 + u : (u >> 3)) ? tn : tv; }
      if (u < U0) {
        if (u == 2) __builtin_amdgcn_s_barrier();
        if (u == UI) {
          vm_wait<0>(); ctl_wr(ctl + 4 * C_FULL, UI);
          ctl_wait<1>(ctl + 4 * C_QI, QI_GO);
        }
        const bool early = u < UI + (WIN0 - 1) / 8;
        dma_units<WIN0>(rs, soff, min(ue, early ? u + 1 : (unsigned)U0), m0b, u, lim, vo0, vo1, ctl, early ? ctl + 252 : ctl + 4 * C_FULL);
        if (u <= 2) lim = NR;
      } else if (u >= NUNIT - UE) {
        if (CK_TE && (int)(unsigned)(__builtin_amdgcn_s_memrealtime() - tl0) < CK_TE && u < NUNIT - UE2) dma_units<WIN>(rs, soff, NUNIT - UE2, m0b, u, lim, vo0, vo1, ctl, ctl + 4 * C_FULL);
        else dma_units<WINE>(rs, soff, ue, m0b, u, lim, vo0, vo1, ctl, ctl + 4 * C_FULL);
      } else {
        if (u == CK_U0 || u == CK_U1 || u == CK_U2) {
          const int late = (int)(unsigned)(__builtin_amdgcn_s_memrealtime() - tl0) - (u == CK_U0 ? CK_T0 : (u == CK_U1 ? CK_T1 : CK_T2));
          ub = u + (late > 0 ? min((late * CK_G) >> 10, NUNIT) : 0);
        }
        ue = min(ue, u < CK_U0 ? (unsigned)CK_U0 : (u < CK_U1 ? (unsigned)CK_U1 : (u < CK_U2 ? (unsigned)CK_U2 : (unsigned)(NUNIT - UE))));
        if (u < ub) dma_units<WINB>(rs, soff, min(ue, ub), m0b, u, lim, vo0, vo1, ctl, ctl + 4 * C_FULL);
        else dma_units<WIN>(rs, soff, ue, m0b, u, lim, vo0, vo1, ctl, ctl + 4 * C_FULL);
      }
    }
    ctl_wr(ctl + 4 * C_LDONE, 1u);
    if (WINE > 48) { vm_wait<48>(); ctl_wr(ctl + 4 * C_FULL, NUNIT - 6); }
    if (WINE > 40) { vm_wait<40>(); ctl_wr(ctl + 4 * C_FULL, NUNIT - 5); }
    if (WINE > 32) { vm_wait<32>(); ctl_wr(ctl + 4 * C_FULL, NUNIT - 4); }
    if (WINE > 24) { vm_wait<24>(); ctl_wr(ctl + 4 * C_FULL, NUNIT - 3); }
    vm_wait<16>(); ctl_wr(ctl + 4 * C_FULL, NUNIT - 2);
    vm_wait<8>(); ctl_wr(ctl + 4 * C_FULL, NUNIT - 1);
    vm_wait<0>(); ctl_wr(ctl + 4 * C_FULL, NUNIT);
    nstall = (unsigned)((__builtin_amdgcn_s_memtime() - tc0) * 100 / (__builtin_amdgcn_s_memrealtime() - tl0));
    STAMPV(0, tl0); STAMP(1); STAMPV(2, nstall);
    ((GAS unsigned long long*)(ws + (17u << 20)))[blockIdx.x * 64 + lane] = tv;
    { unsigned xcc; asm volatile("s_getreg_b32 %0, hwreg(HW_REG_XCC_ID)" : "=s"(xcc)); STAMPV(3, xcc & 15); }
  } else if ((wave & 3) == 0 || wave == 1) {
    const int aw = wave == 1 ? 2 : (wave >> 2) - 1;
    if (APRIO) __builtin_amdgcn_s_setprio(APRIO);
    if (aw == 0) { *(LAS unsigned*)(L + L_CTRL + 4 * lane) = 0u; LDS_WAIT(); }
    __builtin_amdgcn_s_barrier();
    const unsigned koff = (c & 7) * PIECE + (c >> 3) * 256 + g * 16;
    const unsigned voff = (4 * (g & 1) + (c >> 2)) * PIECE + (g >> 1) * 256 + (c & 3) * 16;
    const int obase = W_O + ((bp >> 1) * 8 + (j & 7)) * 16384 + hq * 4096 + g * 256 + ((bp & 1) * 8 + (c & 7)) * 16;
    v4u oA[4];
#pragma unroll
    for (int s = 0; s < 4; ++s) oA[s] = v4u{0u, 0u, 0u, 0u};
    {
      const v4u sv = {SENT, SENT, SENT, SENT};
      if (aw == 0) {
        const unsigned long long tag = __hip_atomic_load((GAS unsigned long long*)(ws + W_EPOCH) + FSTR * (j >> 6), __ATOMIC_RELAXED, __HIP_MEMORY_SCOPE_AGENT) + 1ull;
#pragma unroll
        for (int s = 0; s < 4; ++s) __builtin_amdgcn_raw_buffer_store_b128(sv, rsO, obase + (c >> 3) * (4 * 8 * 16384), s * 1024, AUX_SC1);
        if (lane == 0) *(LAS v2u*)(L + L_CTRL + 4 * C_EP) = v2u{(unsigned)tag, (unsigned)(tag >> 32)};
      }
    }
    v4u kf[8], vf[8];
    unsigned full = ctl_wait<1>(ctl + 4 * C_FULL, aw + 1);
    CFENCE();
#pragma unroll
    for (int i = 0; i < 8; ++i) kf[i] = *(LAS v4u*)(L + aw * UNIT + koff + (i >> 2) * 512 + (i & 3) * 64);
    PIN8(kf);
    ctl_wr(ctl + 4 * (C_REL + aw), aw + NATT);
    full = ctl_wait<1>(ctl + 4 * C_FULL, aw + NATT + 1);
    CFENCE();
#pragma unroll
    for (int i = 0; i < 8; ++i) vf[i] = *(LAS v4u*)(L + (aw + NATT) * UNIT + koff + (i >> 2) * 512 + (i & 3) * 64);
    PIN8(vf);
    ctl_wr(ctl + 4 * (C_REL + aw), aw + 2 * NATT);
    if (aw == 0) STAMP(14);
    ctl_wait<1>(ctl + 4 * C_Q2, 4);
    CFENCE();
    v4u qf[4];
#pragma unroll
    for (int s = 0; s < 4; ++s) {
      LAS char* qp = L + L_PS + g * 2048 + (s * 16 + c) * 16;
      const v4f lo = (*(LAS v4f*)(qp) + *(LAS v4f*)(qp + 8192)) + (*(LAS v4f*)(qp + 16384) + *(LAS v4f*)(qp + 24576));
      const v4f hi = (*(LAS v4f*)(qp + 1024) + *(LAS v4f*)(qp + 9216)) + (*(LAS v4f*)(qp + 17408) + *(LAS v4f*)(qp + 25600));
      qf[s] = v4u{pk2(lo[0], lo[1]), pk2(lo[2], lo[3]), pk2(hi[0], hi[1]), pk2(hi[2], hi[3])};
      if (s & 1) asm volatile("" : "+v"(qf[s - 1]), "+v"(qf[s])::"memory");
    }
    ctl_add(ctl + 4 * C_QR);
    if (!QR_LATE) ctl_wait<0>(ctl + 4 * C_QR, NATT);
    if (aw == 0) STAMP(4);
    unsigned ta = 0;
#define TA(k) do { if (ph == 1 && t >= 57) { __builtin_amdgcn_sched_barrier(0); unsigned long long t_; asm volatile("s_memrealtime %0\n\ts_waitcnt lgkmcnt(0)" : "=s"(t_) :: "memory"); ta = lane == 2 * (t - 57) + (k) ? (unsigned)t_ : ta; __builtin_amdgcn_sched_barrier(0); } } while (0)
    const unsigned poff = L_PS + (g * 8 + (c & 7)) * 16, aoff = L_PS + 512 + (c & 7) * 4;
#pragma unroll 1
    for (int ph = 0; ph < 2; ++ph) {
      const bool mine = (c >> 3) == ph;
      float m = -INFINITY, l = 0.f;
#pragma unroll 1
      for (int t = aw; t < 64; t += NATT) {
        const unsigned uk = 128 * ph + t;
        if (uk >= 2 * NATT) {
          if (full < uk + 1) full = ctl_wait<0>(ctl + 4 * C_FULL, uk + 1);
          CFENCE();
          LAS char* kb = L + (uk % NR) * UNIT + koff;
#pragma unroll
          for (int i = 0; i < 8; ++i) kf[i] = *(LAS v4u*)(kb + (i >> 2) * 512 + (i & 3) * 64);
          PIN8(kf);
          ctl_wr(ctl + 4 * (C_REL + aw), t + NATT < 64 ? uk + NATT : 128 * ph + 64 + aw);
          __builtin_amdgcn_sched_barrier(0);
        } else if (uk >= NATT) {
#pragma unroll
          for (int i = 0; i < 8; ++i) kf[i] = vf[i];
        }
        v4f s0 = {0.f, 0.f, 0.f, 0.f}, s1 = {0.f, 0.f, 0.f, 0.f};
#pragma unroll
        for (int s = 0; s < 4; ++s) { s0 = mfma(kf[s], qf[s], s0); s1 = mfma(kf[4 + s], qf[s], s1); }
        float mx = fmaxf(fmaxf(fmaxf(s0[0], s0[1]), fmaxf(s0[2], s0[3])), fmaxf(fmaxf(s1[0], s1[1]), fmaxf(s1[2], s1[3])));
        mx = xg_max(mx);
        const float mn = fmaxf(m, mx);
        const float alpha = __builtin_amdgcn_exp2f((m - mn) * CSC);
        const float mc = mn * CSC;
        m = mn;
        float p[8];
#pragma unroll
        for (int i = 0; i < 4; ++i) { p[i] = __builtin_amdgcn_exp2f(fmaf(s0[i], CSC, -mc)); p[4 + i] = __builtin_amdgcn_exp2f(fmaf(s1[i], CSC, -mc)); }
        l = fmaf(l, alpha, ((p[0] + p[1]) + (p[2] + p[3])) + ((p[4] + p[5]) + (p[6] + p[7])));
        if (QR_LATE && uk < NATT) ctl_wait<0>(ctl + 4 * C_QR, NATT);
        if (mine) {
          *(LAS v4u*)(L + poff + t * PREC) = v4u{pk2(p[0], p[1]), pk2(p[2], p[3]), pk2(p[4], p[5]), pk2(p[6], p[7])};
          if (g == 0) *(LAS float*)(L + aoff + t * PREC) = alpha;
        }
      }
      float f0 = 0.f, f1 = 0.f;
      if (aw == 0) {
        ctl_wait<0>(ctl + 4 * (C_ML + ph), 2);
        CFENCE();
        const v2f a0 = *(LAS v2f*)(L + L_ST + lane * 8), a1 = *(LAS v2f*)(L + L_ST + 512 + lane * 8);
        const float M = fmaxf(m, fmaxf(a0[0], a1[0]));
        const float w = __builtin_amdgcn_exp2f((m - M) * CSC), w0 = __builtin_amdgcn_exp2f((a0[0] - M) * CSC), w1 = __builtin_amdgcn_exp2f((a1[0] - M) * CSC);
        const float inv = 1.0f / xg_sum(fmaf(w, l, fmaf(w0, a0[1], w1 * a1[1])));
        l = w * inv; f0 = w0 * inv; f1 = w1 * inv;
      } else {
        if (mine) *(LAS v2f*)(L + L_ST + (aw - 1) * 512 + lane * 8) = v2f{m, l};
        LDS_WAIT();
        ctl_add(ctl + 4 * (C_ML + ph));
      }
      v4f acc[8];
#pragma unroll
      for (int d = 0; d < 8; ++d) acc[d] = v4f{0.f, 0.f, 0.f, 0.f};
      LAS char* rec = L + L_MRG + (g * 8 + (c & 7)) * 16;
#pragma unroll 1
      for (int t = aw; t < 64; t += NATT) {
        const unsigned uv = 128 * ph + 64 + t;
        v4u pf = *(LAS v4u*)(L + poff + t * PREC);
        const float alpha = *(LAS float*)(L + aoff + t * PREC);
#pragma unroll
        for (int d = 0; d < 8; ++d) acc[d] *= alpha;
        if (t == 63) {
          ctl_wait<0>(ctl + 4 * (C_MRG + 2 * ph), 1);
          CFENCE();
#pragma unroll
          for (int d = 0; d < 8; ++d) kf[d] = __builtin_bit_cast(v4u, *(LAS v4f*)(rec + d * 512) * f0);
          PIN8(kf);
        }
        asm volatile("" : "+v"(pf));
        PIN8(acc);
        TA(0);
        if (full < uv + 1) full = ctl_wait<0>(ctl + 4 * C_FULL, uv + 1);
        CFENCE();
        TA(1);
        LAS char* vb = L + (uv % NR) * UNIT + voff;
#pragma unroll
        for (int d = 0; d < 8; ++d) {
          const v2u a = trread(vb + (d >> 1) * 64 + (d & 1) * 8), b = trread(vb + 512 + (d >> 1) * 64 + (d & 1) * 8);
          vf[d] = v4u{a[0], a[1], b[0], b[1]};
        }
        PIN8(vf);
        ctl_wr(ctl + 4 * (C_REL + aw), t + NATT < 64 ? uv + NATT : 128 * (ph + 1) + aw);
        __builtin_amdgcn_sched_barrier(0);
#pragma unroll
        for (int d = 0; d < 8; ++d) acc[d] = mfma(vf[d], pf, acc[d]);
        TA(2);
        if (ph == 1 && t == OA_AT) {
          vm_wait<0>();
          const v2u tg = *(LAS v2u*)(L + L_CTRL + 4 * C_EP);
          if (lane == 0) __hip_atomic_store((GAS unsigned long long*)(ws + W_CF) + FSTR * j, ((unsigned long long)tg[1] << 32) | tg[0], __ATOMIC_RELAXED, __HIP_MEMORY_SCOPE_AGENT);
          if (c < 8) {
#pragma unroll
            for (int s = 0; s < 4; ++s) __builtin_amdgcn_raw_buffer_store_b128(oA[s], rsO, obase + s * 1024, 0, AUX_SC1);
          }
        }
      }
      if (aw != 0) {
        if (c < 8) {
#pragma unroll
          for (int d = 0; d < 8; ++d) *(LAS v4f*)(rec + (aw - 1) * 4096 + d * 512) = acc[d];
        }
        LDS_WAIT();
        ctl_wr(ctl + 4 * (C_MRG + 2 * ph + aw - 1), 1u);
      } else {
        ctl_wait<0>(ctl + 4 * (C_MRG + 2 * ph + 1), 1);
        CFENCE();
#pragma unroll
        for (int d = 0; d < 8; ++d) vf[d] = *(LAS v4u*)(rec + 4096 + d * 512);
#pragma unroll
        for (int s = 0; s < 4; ++s) {
          const v4f x0 = __builtin_elementwise_fma(__builtin_bit_cast(v4f, vf[2 * s]), v4f{f1, f1, f1, f1}, __builtin_elementwise_fma(acc[2 * s], v4f{l, l, l, l}, __builtin_bit_cast(v4f, kf[2 * s])));
          const v4f x1 = __builtin_elementwise_fma(__builtin_bit_cast(v4f, vf[2 * s + 1]), v4f{f1, f1, f1, f1}, __builtin_elementwise_fma(acc[2 * s + 1], v4f{l, l, l, l}, __builtin_bit_cast(v4f, kf[2 * s + 1])));
          v4u o = {pk2(x0[0], x0[1]), pk2(x0[2], x0[3]), pk2(x1[0], x1[1]), pk2(x1[2], x1[3])};
#pragma unroll
          for (int k = 0; k < 4; ++k) o[k] = min(o[k], SENT - 1u);
          if (ph == 0) oA[s] = o;
          else if (mine) __builtin_amdgcn_raw_buffer_store_b128(o, rsO, obase + s * 1024, 4 * 8 * 16384, AUX_SC1);
        }
        STAMP(5 + ph);
      }
    }
    if (aw == 0) ((GAS unsigned long long*)(ws + (18u << 20)))[blockIdx.x * 64 + lane] = ta;
  } else {
    const int gw = wave - 2 - (wave >> 2);
    {
      const __amdgpu_buffer_rsrc_t rsQ = __builtin_amdgcn_make_buffer_rsrc((void*)wq, 0, DM * NH * DH * 2, 0x00020000);
      const int wvo = (8 * g * (NH * DH) + 8 * c) * 2, wso = (128 * gw * (NH * DH) + h * DH) * 2;
      const int xrow = ((c < 8 ? bp : bp + 8) * SQ + (c & 7));
      const GAS char* xb = (const GAS char*)x + ((size_t)xrow * DM + 128 * gw + 8 * g) * 2;
#define WQ_ROW(t, jj) ((32 * (((t) + (bp >> 1)) & 3) + ((jj) ^ (4 * (bp & 1)))) * (NH * DH * 2))
      v4u xf[4], w[4][8];
      {
#pragma unroll
        for (int jj = 0; jj < 4; ++jj) w[0][jj] = __builtin_amdgcn_raw_buffer_load_b128(rsQ, wvo, wso + WQ_ROW(0, jj), AUX_NT);
        const unsigned pf = *(const GAS unsigned*)(xb + (hq & 1) * 128);
        __builtin_amdgcn_sched_barrier(0);
        __builtin_amdgcn_s_barrier();
        __builtin_amdgcn_sched_barrier(0);
        asm volatile("" ::"v"(pf));
        vm_wait<0>();
        __builtin_amdgcn_s_sleep(Q_GAP);
        __builtin_amdgcn_sched_barrier(0);
      }
#pragma unroll
      for (int jj = 4; jj < 8; ++jj) w[0][jj] = __builtin_amdgcn_raw_buffer_load_b128(rsQ, wvo, wso + WQ_ROW(0, jj), AUX_NT);
#pragma unroll
      for (int tt = 0; tt < 4; ++tt) xf[tt] = *(const GAS v4u*)(xb + ((tt + (bp >> 1)) & 3) * 64);
#pragma unroll
      for (int t = 1; t < 3; ++t)
#pragma unroll
        for (int jj = 0; jj < 8; ++jj) w[t][jj] = __builtin_amdgcn_raw_buffer_load_b128(rsQ, wvo, wso + WQ_ROW(t, jj), AUX_NT);
      __builtin_amdgcn_sched_barrier(0);
      ctl_add(ctl + 4 * C_QI);
      if (bp & 1) {
#pragma unroll
        for (int t = 0; t < 4; ++t) xf[t] = v4u{xf[t][2], xf[t][3], xf[t][0], xf[t][1]};
      }
      v4f qa[8];
#pragma unroll
      for (int d = 0; d < 8; ++d) qa[d] = v4f{0.f, 0.f, 0.f, 0.f};
#pragma unroll
      for (int t = 0; t < 4; ++t) {
#pragma unroll
        for (int dd = 0; dd < 8; ++dd) {
          v4u f;
#pragma unroll
          for (int v = 0; v < 4; ++v) f[v] = __builtin_amdgcn_perm(w[t][2 * v + 1][dd >> 1], w[t][2 * v][dd >> 1], (dd & 1) ? 0x07060302u : 0x05040100u);
          qa[dd] = mfma(f, xf[t], qa[dd]);
          if (dd & 1) __builtin_amdgcn_sched_barrier(0);
        }
        if (t == 0) {
#pragma unroll
          for (int jj = 0; jj < 8; ++jj) {
            w[3][jj] = __builtin_amdgcn_raw_buffer_load_b128(rsQ, wvo, wso + WQ_ROW(3, jj), AUX_NT);
            __builtin_amdgcn_sched_barrier(0);
          }
        }
      }
      STAMP(16 + gw);
      LAS char* part = L + L_PS + (gw & 3) * 8192 + lane * 16;
      if (gw >= 4) {
        ctl_wait<0>(ctl + 4 * C_Q1, 4);
        CFENCE();
      }
#pragma unroll
      for (int i = 0; i < 4; ++i) {
        v4f lo = {qa[0][i], qa[1][i], qa[2][i], qa[3][i]}, hi = {qa[4][i], qa[5][i], qa[6][i], qa[7][i]};
        if (gw >= 4) { lo += *(LAS v4f*)(part + i * 2048); hi += *(LAS v4f*)(part + i * 2048 + 1024); }
        *(LAS v4f*)(part + i * 2048) = lo;
        *(LAS v4f*)(part + i * 2048 + 1024) = hi;
      }
      LDS_WAIT();
      ctl_add(ctl + 4 * (gw >= 4 ? C_Q2 : C_Q1));
      if (gw == 0) STAMP(7);
      if (gw == 7) STAMP(8);
    }
    const int xc = j & 7, idx = j >> 3, ct = (idx + CT_ROT) & 7, rg = idx >> 3;
    unsigned hb = 0;
#define HBAR(SLP) do { hb += NHELP; ctl_add(ctl + 4 * C_HB); ctl_wait<SLP>(ctl + 4 * C_HB, hb); CFENCE(); } while (0)
    const int pdst = W_P + (idx * 128 + gw * 8 + xc) * 1024;
    v4u wf[16];
    {
      const __amdgpu_buffer_rsrc_t rsW = __builtin_amdgcn_make_buffer_rsrc((void*)wo, 0, DM * NH * DH * 2, 0x00020000);
      ctl_wait<32>(ctl + 4 * C_FULL, WO_AT + 2 * gw);
#pragma unroll
      for (int ks = 0; ks < 16; ++ks) {
        wf[ks] = __builtin_amdgcn_raw_buffer_load_b128(rsW, g * 2048 + c * 16, ((4 * xc + (ks >> 2)) * 128 + (ks & 3) * 32 + 4 * gw) * 2048 + ct * 256, AUX_NT);
        __builtin_amdgcn_s_sleep(WF_SLEEP);
        __builtin_amdgcn_sched_barrier(0);
      }
      LAS char* sw = L + L_STG + gw * PIECE + lane * 16;
      LAS char* sr = L + L_STG + 2 * g * PIECE + (c >> 2) * 256 + gw * 32 + (c & 3) * 8;
#pragma unroll
      for (int r = 0; r < 16; ++r) {
        *(LAS v4u*)(sw) = wf[r];
        LDS_WAIT();
        HBAR(2);
        const v2u a0 = trread(sr), b0 = trread(sr + PIECE);
        wf[r] = v4u{a0[0], a0[1], b0[0], b0[1]};
        asm volatile("" : "+v"(wf[r])::"memory");
        HBAR(2);
      }
    }
    const v2u tagv = *(LAS v2u*)(L + L_CTRL + 4 * C_EP);
    const unsigned long long tag = ((unsigned long long)tagv[1] << 32) | tagv[0];
    unsigned tb[4];
    tag_bytes(tagv, tb);
    if (gw == 7) STAMP(9);
    ctl_wait<16>(ctl + 4 * C_FULL, NUNIT - 16);
    ctl_wait<2>(ctl + 4 * C_LDONE, 1);
    if (gw == 7) STAMP(10);
    {
      const int src = (2 * rg + (lane & 1)) * 32 + (gw >> 1) * 8 + xc;
      GAS unsigned long long* fl = (GAS unsigned long long*)(ws + W_CF) + FSTR * src;
      for (unsigned sp = 0; sp < (1u << 16); ++sp) {
        if (__all(__hip_atomic_load(fl, __ATOMIC_RELAXED, __HIP_MEMORY_SCOPE_AGENT) == tag)) break;
        __builtin_amdgcn_s_sleep(G1_SLEEP);
      }
      __builtin_amdgcn_fence(__ATOMIC_ACQUIRE, "wavefront");
    }
    if (gw == 7) STAMP(11);
#pragma unroll 1
    for (int ph = 0; ph < 2; ++ph) {
      v4u f0, f1;
      for (unsigned sp = 0; sp < (1u << 16); ++sp) {
        CFENCE();
        f0 = __builtin_amdgcn_raw_buffer_load_b128(rsO, lane * 16, W_O + ((ph * 4 + rg) * 8 + xc) * 16384 + gw * 2048, AUX_SC1);
        f1 = __builtin_amdgcn_raw_buffer_load_b128(rsO, lane * 16, W_O + ((ph * 4 + rg) * 8 + xc) * 16384 + gw * 2048 + 1024, AUX_SC1);
        const unsigned mxv = max(max(max(f0[0], f0[1]), max(f0[2], f0[3])), max(max(f1[0], f1[1]), max(f1[2], f1[3])));
        if (__all(mxv != SENT)) break;
        __builtin_amdgcn_s_sleep(G1_SLEEP);
      }
      *(LAS v4u*)(L + L_OB + gw * 2048 + lane * 16) = f0;
      *(LAS v4u*)(L + L_OB + gw * 2048 + 1024 + lane * 16) = f1;
      LDS_WAIT();
      HBAR(0);
      v4f oa[4];
#pragma unroll
      for (int k = 0; k < 4; ++k) oa[k] = v4f{0.f, 0.f, 0.f, 0.f};
#pragma unroll
      for (int f = 0; f < 16; ++f) oa[f & 3] = mfma(wf[f], *(LAS v4u*)(L + L_OB + f * 1024 + lane * 16), oa[f & 3]);
      v4u pt = __builtin_bit_cast(v4u, (oa[0] + oa[1]) + (oa[2] + oa[3]));
#pragma unroll
      for (int k = 0; k < 4; ++k) pt[k] = ((pt[k] + ((pt[k] & 0x7F800000u) == 0x7F800000u ? 0u : 0x80u)) & ~0xFFu) | tb[k];
      __builtin_amdgcn_raw_buffer_store_b128(pt, rsO, lane * 16, pdst + ph * 65536, AUX_SC1);
      if (gw == 7) STAMP(24 + ph);
      if (ph == 0) HBAR(0);
    }
    if (gw < 2) {
      GAS v2u* dst = (GAS v2u*)((GAS char*)out + ((size_t)(64 * gw + 16 * rg + c) * DM + 128 * ct + 16 * xc + 4 * g) * 2);
      asm volatile("" : "+v"(dst));
      const v4u tv4 = {tb[0], tb[1], tb[2], tb[3]};
      v2u res;
      for (unsigned sp = 0; sp < (1u << 16); ++sp) {
        CFENCE();
        v4u q[8];
#pragma unroll
        for (int k = 0; k < 8; ++k) q[k] = __builtin_amdgcn_raw_buffer_load_b128(rsO, lane * 16, W_P + ((idx * 2 + gw) * 64 + xc * 8 + k) * 1024, AUX_SC1);
        v4u bad = q[0] ^ tv4;
        v4f s = __builtin_bit_cast(v4f, bad);
#pragma unroll
        for (int k = 1; k < 8; ++k) { const v4u y = q[k] ^ tv4; bad |= y; s += __builtin_bit_cast(v4f, y); }
        res = v2u{pk2(s[0], s[1]), pk2(s[2], s[3])};
        asm volatile("" : "+v"(res));
        if (__all((((bad[0] | bad[1]) | (bad[2] | bad[3])) & 0xFFu) == 0u)) break;
        __builtin_amdgcn_s_sleep(X2_SLEEP);
      }
      *dst = res;
      if (gw == 0 && (j & 63) == 0 && lane == 0) __hip_atomic_store((GAS unsigned long long*)(ws + W_EPOCH) + FSTR * rg, tag, __ATOMIC_RELAXED, __HIP_MEMORY_SCOPE_AGENT);
      STAMP(12 + gw);
    }
  }
}
}

__global__ void k_delay(unsigned char* ws) {
  const unsigned long long tn = __builtin_amdgcn_s_memrealtime();
  const unsigned long long* h = (const unsigned long long*)(ws + (16u << 20));
  const unsigned long long* p = (const unsigned long long*)(ws + (17u << 20));
  const int t = threadIdx.x;
  __shared__ unsigned long long a0[256], a1[256], a2[256];
  a0[t] = h[t * 64]; a1[t] = h[t * 64 + 13]; a2[t] = WHAT == 1 ? p[t * 64 + 30] : (WHAT == 3 ? p[t * 64 + 3] : (WHAT == 7 ? h[t * 64 + 2] : h[t * 64 + 1]));
  __syncthreads();
  if (t == 0) {
    unsigned long long t0 = ~0ull, t1 = 0, sm = 0, mx = 0;
    for (int j = 0; j < 256; ++j) { t0 = a0[j] < t0 ? a0[j] : t0; t1 = a1[j] > t1 ? a1[j] : t1; sm += a2[j]; mx = a2[j] > mx ? a2[j] : mx; }
    long long S;
    if (WHAT == 0) S = (long long)(t1 - t0) - 7000;
    else if (WHAT == 1) S = (long long)(sm / 256) - 6000;
    else if (WHAT == 3) S = (long long)(sm / 256);
    else if (WHAT == 4) S = (long long)(mx - t0) - 7000;
    else if (WHAT == 7) S = (long long)(sm / 256) - 1500;
    else S = 0;
    if (S < 0) S = 0; if (S > 20000) S = 20000;
    while ((long long)(__builtin_amdgcn_s_memrealtime() - tn) < 10 * S + 1000) __builtin_amdgcn_s_sleep(1);
  }
}
extern "C" void kernel_launch(void* const* d_in, const int* in_sizes, int n_in, void* d_out, int out_size, void* d_ws, size_t ws_size,
                              hipStream_t stream) {
  (void)in_sizes; (void)n_in; (void)out_size; (void)ws_size;
  (void)hipFuncSetAttribute((const void*)mha_block_kernel, hipFuncAttributeMaxDynamicSharedMemorySize, LDS_BYTES);
  mha_block_kernel<<<NWG, NTHR, LDS_BYTES, stream>>>((const unsigned short*)d_in[0], (const unsigned short*)d_in[1], (const unsigned short*)d_in[2],
                                                     (const unsigned short*)d_in[3], (const unsigned short*)d_in[4], (unsigned short*)d_out,
                                                     (unsigned char*)d_ws);
  k_delay<<<1, 256, 0, stream>>>((unsigned char*)d_ws);
}
```

```cpp
#define G_W 56
#define G_QB 12
#define G_SH 170
#define G_T0 1190
#define G_WA 32
#define G_WOAUX 0
#define G_AUX2 2
#define G_U0 32
#define G_QI 6
#define G_R1 8
#define G_AUX 0
#define GRP (((j >> 3) & 3) == 3)
#include <hip/hip_runtime.h>
#include <hip/hip_bf16.h>
#include <stdint.h>

#define GAS __attribute__((address_space(1)))
#define LAS __attribute__((address_space(3)))
typedef unsigned v4u __attribute__((ext_vector_type(4)));
typedef unsigned v2u __attribute__((ext_vector_type(2)));
typedef float v4f __attribute__((ext_vector_type(4)));
typedef float v2f __attribute__((ext_vector_type(2)));
typedef short v4s __attribute__((ext_vector_type(4)));
typedef __bf16 v8bf __attribute__((ext_vector_type(8)));
typedef __bf16 v2bf __attribute__((ext_vector_type(2)));

namespace {
constexpr int NB = 16, SQ = 8, DM = 1024, NH = 32, SK = 2048, DH = 128;
constexpr int NWG = 256, NTHR = 768, NATT = 3, NHELP = 8;
constexpr int PIECE = 1056;
constexpr int UNIT = 8 * PIECE;
constexpr int NR = 14;
constexpr int NUNIT = 256;
constexpr int AUX_NT = 2, AUX_SC1 = 16;
constexpr int WIN = 20;
constexpr int WIN0 = G_W, U0 = G_U0;
constexpr int WINE = 32, UE = 16;
constexpr int WINB = 28;
constexpr int CK_U0 = G_U0, CK_T0 = G_T0, CK_U1 = 108, CK_T1 = 3605 - G_SH, CK_U2 = 200, CK_T2 = 6105 - G_SH;
constexpr int CK_G = 160;
constexpr int UI = 3, QI_GO = G_QI, Q_GAP = 4;
constexpr int WO_AT = 68, WF_SLEEP = 16;
constexpr int OA_AT = 57;
constexpr int G1_SLEEP = 1, X2_SLEEP = 1, CT_ROT = 2;
constexpr int L_CTRL = NR * UNIT;
constexpr int PREC = 544;
constexpr int L_PS = L_CTRL + 256;
constexpr int L_OB = L_PS;
constexpr int L_MRG = L_PS + 32 * PREC;
constexpr int L_ST = L_PS + 64 * PREC;
constexpr int L_STG = L_ST + 1024;
constexpr int LDS_BYTES = 160 * 1024;
static_assert(L_STG + 8 * PIECE <= LDS_BYTES && 16384 <= 32 * PREC && 32 * PREC + 8192 <= 48 * PREC && NR <= 14, "LDS map");
enum { C_FULL = 0, C_REL = 4, C_Q1 = 8, C_Q2 = 9, C_QI = 10, C_HB = 11, C_QR = 12, C_EP = 14, C_ML = 18, C_MRG = 20, C_LDONE = 24, C_R1 = 25 };
constexpr int W_EPOCH = 0, W_CF = 65536, FSTR = 16;
constexpr int W_O = 1 << 20;
constexpr int W_P = 2 << 20;
constexpr int WS_USED = 6 << 20;
constexpr unsigned SENT = 0xFFFFFFFFu;
constexpr float CSC = 0.08838834764831845f * 1.4426950408889634f;
constexpr unsigned SPIN_CAP = 1u << 22;

__device__ __forceinline__ unsigned rfl(unsigned x) { return __builtin_amdgcn_readfirstlane(x); }
__device__ __forceinline__ v4f mfma(v4u a, v4u b, v4f c) {
  return __builtin_amdgcn_mfma_f32_16x16x32_bf16(__builtin_bit_cast(v8bf, a), __builtin_bit_cast(v8bf, b), c, 0, 0, 0);
}
__device__ __forceinline__ unsigned pk2(float lo, float hi) {
  v2f x = {lo, hi};
  return __builtin_bit_cast(unsigned, __builtin_convertvector(x, v2bf));
}
__device__ __forceinline__ v2u trread(LAS char* p) {
  return __builtin_bit_cast(v2u, __builtin_amdgcn_ds_read_tr16_b64_v4i16((LAS v4s*)p));
}
__device__ __forceinline__ float xg_max(float x) {
  auto s = __builtin_amdgcn_permlane16_swap(__float_as_uint(x), __float_as_uint(x), false, false);
  x = fmaxf(__uint_as_float(s[0]), __uint_as_float(s[1]));
  auto t = __builtin_amdgcn_permlane32_swap(__float_as_uint(x), __float_as_uint(x), false, false);
  return fmaxf(__uint_as_float(t[0]), __uint_as_float(t[1]));
}
__device__ __forceinline__ float xg_sum(float x) {
  auto s = __builtin_amdgcn_permlane16_swap(__float_as_uint(x), __float_as_uint(x), false, false);
  x = __uint_as_float(s[0]) + __uint_as_float(s[1]);
  auto t = __builtin_amdgcn_permlane32_swap(__float_as_uint(x), __float_as_uint(x), false, false);
  return __uint_as_float(t[0]) + __uint_as_float(t[1]);
}
#define LDS_WAIT() asm volatile("s_waitcnt lgkmcnt(0)" ::: "memory")
#define CFENCE() asm volatile("" ::: "memory")
#define PIN8(a) asm volatile("" : "+v"(a[0]), "+v"(a[1]), "+v"(a[2]), "+v"(a[3]), "+v"(a[4]), "+v"(a[5]), "+v"(a[6]), "+v"(a[7])::"memory")
template <int N> __device__ __forceinline__ void vm_wait() { asm volatile("s_waitcnt vmcnt(%c0)" ::"n"(N) : "memory"); }
__device__ __forceinline__ unsigned ctl_rd(unsigned addr) {
  unsigned v;
  asm volatile("ds_read_b32 %0, %1\n\ts_waitcnt lgkmcnt(0)" : "=v"(v) : "v"(addr) : "memory");
  return rfl(v);
}
__device__ __forceinline__ void ctl_wr(unsigned addr, unsigned val) {
  asm volatile("s_mov_b64 exec, 1\n\tds_write_b32 %0, %1\n\ts_mov_b64 exec, -1" ::"v"(addr), "v"(val) : "memory");
}
__device__ __forceinline__ unsigned ctl_add(unsigned addr) {
  unsigned v;
  asm volatile("s_mov_b64 exec, 1\n\tds_add_rtn_u32 %0, %1, %2\n\ts_mov_b64 exec, -1\n\ts_waitcnt lgkmcnt(0)" : "=&v"(v) : "v"(addr), "v"(1u) : "memory");
  return rfl(v);
}
template <int SLP> __device__ __forceinline__ unsigned ctl_wait(unsigned addr, unsigned need) {
  unsigned v = ctl_rd(addr);
  for (unsigned sp = 0; v < need && sp < SPIN_CAP; ++sp) {
    __builtin_amdgcn_s_sleep(SLP);
    v = ctl_rd(addr);
  }
  return v;
}

__device__ __forceinline__ void tag_bytes(v2u tagv, unsigned (&tb)[4]) {
  const unsigned long long e = (((unsigned long long)tagv[1] << 32) | tagv[0]) - 1ull;
  const unsigned hs = (unsigned)(e >> 8) * 0x9E3779B1u ^ (unsigned)(e >> 40) * 0x85EBCA77u;
#pragma unroll
  for (int k = 0; k < 4; ++k) tb[k] = rfl((tagv[0] + ((hs >> (8 * k)) & 0xFFu) % 254u) & 0xFFu);
}

#define PUBL(k) ".if %c[b] == " #k "\n\ts_mov_b64 exec, 1\n\tds_write_b32 %[fa], v23\n\ts_mov_b64 exec, -1\n\t.endif\n\t"
#define PC(k, VO, IMM) "s_waitcnt vmcnt(%c[w])\n\t" PUBL(k) "buffer_load_dwordx4 " VO ", %[rs], %[so] offen offset:" IMM " nt lds\n\t"
template <int W>
__device__ __forceinline__ void dma_units(__amdgpu_buffer_rsrc_t rs, unsigned soff, unsigned uend, unsigned& m0b, unsigned& u, unsigned& lim, unsigned vo0,
                                          unsigned vo1, unsigned vctl, unsigned faddr) {
  unsigned sp;
  asm volatile(
      "0:\n\t"
      "s_cmp_lt_u32 %[u], %[lim]\n\t"
      "s_cbranch_scc1 2f\n\t"
      "s_mov_b32 %[sp], 0\n"
      "1:\n\t"
      "ds_read_b32 v20, %[vc] offset:16\n\tds_read_b32 v21, %[vc] offset:20\n\tds_read_b32 v22, %[vc] offset:24\n\t"
      "s_waitcnt lgkmcnt(0)\n\t"
      "v_min3_u32 v20, v20, v21, v22\n\t"
      "s_add_u32 %[sp], %[sp], 1\n\t"
      "v_readfirstlane_b32 %[lim], v20\n\t"
      "s_add_u32 %[lim], %[lim], %c[nr]\n\t"
      "s_cmp_lt_u32 %[u], %[lim]\n\t"
      "s_cbranch_scc1 2f\n\t"
      "s_sleep 1\n\t"
      "s_cmp_lt_u32 %[sp], %c[cap]\n\t"
      "s_cbranch_scc1 1b\n"
      "2:\n\t"
      "s_mov_b32 m0, %[m0b]\n\t"
      "v_mov_b32 v23, %[u]\n\t"
      "v_subrev_u32 v23, %c[a], v23\n\t"
      PC(0, "%[vo0]", "0")
      "ds_read_b32 v20, %[vc] offset:16\n\tds_read_b32 v21, %[vc] offset:20\n\tds_read_b32 v22, %[vc] offset:24\n\t"
      "s_add_u32 m0, m0, 32\n\t"
      PC(1, "%[vo0]", "1024")
      "s_add_u32 m0, m0, 32\n\t"
      PC(2, "%[vo0]", "2048")
      "s_add_u32 m0, m0, 32\n\t"
      PC(3, "%[vo0]", "3072")
      "s_add_u32 m0, m0, 0x1020\n\t"
      PC(4, "%[vo1]", "0")
      "s_waitcnt lgkmcnt(0)\n\t"
      "v_min3_u32 v20, v20, v21, v22\n\t"
      "s_add_u32 m0, m0, 32\n\t"
      PC(5, "%[vo1]", "1024")
      "v_readfirstlane_b32 %[lim], v20\n\t"
      "s_add_u32 %[m0b], %[m0b], %c[unit]\n\t"
      "s_add_u32 m0, m0, 32\n\t"
      PC(6, "%[vo1]", "2048")
      "s_add_u32 %[lim], %[lim], %c[nr]\n\t"
      "s_cmp_lt_u32 %[m0b], %c[ring]\n\t"
      "s_cselect_b32 %[m0b], %[m0b], 0\n\t"
      "s_add_u32 %[u], %[u], 1\n\t"
      "s_add_u32 m0, m0, 32\n\t"
      PC(7, "%[vo1]", "3072")
      "s_add_u32 %[so], %[so], 0x2000\n\t"
      "s_cmp_lt_u32 %[u], %[ue]\n\t"
      "s_cbranch_scc1 0b"
      : [m0b] "+&s"(m0b), [u] "+&s"(u), [lim] "+&s"(lim), [so] "+&s"(soff), [sp] "=&s"(sp)
      : [ue] "s"(uend), [w] "n"(W - 1), [a] "n"((W - 1) / 8), [b] "n"((W - 1) % 8), [nr] "n"(NR), [cap] "n"(SPIN_CAP), [unit] "n"(UNIT), [ring] "n"(NR * UNIT), [rs] "s"(rs),
        [vo0] "v"(vo0), [vo1] "v"(vo1), [vc] "v"(vctl), [fa] "v"(faddr)
      : "memory", "scc", "v20", "v21", "v22", "v23");
}

__global__ void __launch_bounds__(NTHR, 3)
mha_block_kernel(const unsigned short* __restrict__ x, const unsigned short* __restrict__ kc, const unsigned short* __restrict__ vc,
                 const unsigned short* __restrict__ wq, const unsigned short* __restrict__ wo, unsigned short* __restrict__ out,
                 unsigned char* __restrict__ ws) {
  extern __shared__ __attribute__((aligned(16))) char smem[];
  LAS char* L = (LAS char*)smem;
  const unsigned lbase = (unsigned)(size_t)L;
  const int lane = threadIdx.x & 63, wave = rfl(threadIdx.x >> 6);
  const int g = lane >> 4, c = lane & 15;
  const int j = blockIdx.x, hq = (j >> 3) & 3, h = 4 * (j & 7) + hq, bp = j >> 5;
  const unsigned ctl = lbase + L_CTRL;
  const bool grpB = GRP;
  const __amdgpu_buffer_rsrc_t rsO = __builtin_amdgcn_make_buffer_rsrc((void*)ws, 0, WS_USED, 0x00020000);

  if (wave == 0) {
    __builtin_amdgcn_s_setprio(3);
    __builtin_amdgcn_s_waitcnt(0x0F70);
    const unsigned long long tl0 = __builtin_amdgcn_s_memrealtime();
    const unsigned vo0 = lane * 16, vo1 = lane * 16 + 4096;
    const unsigned sbase = (unsigned)(bp * NH + h) * (SK * DH * 2);
    unsigned m0b = 0, u = 0, lim = NR;
    unsigned ub = 0;
    static_assert(UI >= 2 && UI + (WIN0 - 1) / 8 < U0 && U0 <= CK_U0 && CK_U0 < CK_U1 && CK_U1 < CK_U2 && CK_U2 < NUNIT - UE, "segment order");
#pragma unroll 1
    while (u < NUNIT) {
      const __amdgpu_buffer_rsrc_t rs = __builtin_amdgcn_make_buffer_rsrc((void*)((u & 64) ? vc : kc), 0, NB * NH * SK * DH * 2, 0x00020000);
      const unsigned soff = sbase + (u >> 7) * (8u * NH * SK * DH * 2) + (u & 63) * 8192u;
      unsigned ue = (u | 63) + 1;
      if (u < U0) {
        if (u == 2) __builtin_amdgcn_s_barrier();
        if (u == UI) {
          vm_wait<0>(); ctl_wr(ctl + 4 * C_FULL, UI);
          if (grpB) ctl_wait<1>(ctl + 4 * C_R1, G_R1); else ctl_wait<1>(ctl + 4 * C_QI, QI_GO);
        }
        const bool early = u < UI + (WIN0 - 1) / 8;
        if (!grpB && u >= UI) dma_units<G_WA>(rs, soff, u < UI + (G_WA - 1) / 8 ? UI + (G_WA - 1) / 8 : U0, m0b, u, lim, vo0, vo1, ctl, u < UI + (G_WA - 1) / 8 ? ctl + 252 : ctl + 4 * C_FULL);
        else dma_units<WIN0>(rs, soff, u < UI ? u + 1 : (early ? UI + (WIN0 - 1) / 8 : U0), m0b, u, lim, vo0, vo1, ctl, early ? ctl + 252 : ctl + 4 * C_FULL);
        if (u <= 2) lim = NR;
      } else if (u >= NUNIT - UE) {
        dma_units<WINE>(rs, soff, ue, m0b, u, lim, vo0, vo1, ctl, ctl + 4 * C_FULL);
      } else {
        if (u == CK_U0 || u == CK_U1 || u == CK_U2) {
          const int late = (int)(unsigned)(__builtin_amdgcn_s_memrealtime() - tl0) - (u == CK_U0 ? CK_T0 : (u == CK_U1 ? CK_T1 : CK_T2));
          ub = u + (late > 0 ? min((late * CK_G) >> 10, NUNIT) : 0);
        }
        ue = min(ue, u < CK_U0 ? (unsigned)CK_U0 : (u < CK_U1 ? (unsigned)CK_U1 : (u < CK_U2 ? (unsigned)CK_U2 : (unsigned)(NUNIT - UE))));
        if (u < ub) dma_units<WINB>(rs, soff, min(ue, ub), m0b, u, lim, vo0, vo1, ctl, ctl + 4 * C_FULL);
        else dma_units<WIN>(rs, soff, ue, m0b, u, lim, vo0, vo1, ctl, ctl + 4 * C_FULL);
      }
    }
    ctl_wr(ctl + 4 * C_LDONE, 1u);
    if (WINE > 48) { vm_wait<48>(); ctl_wr(ctl + 4 * C_FULL, NUNIT - 6); }
    if (WINE > 40) { vm_wait<40>(); ctl_wr(ctl + 4 * C_FULL, NUNIT - 5); }
    if (WINE > 32) { vm_wait<32>(); ctl_wr(ctl + 4 * C_FULL, NUNIT - 4); }
    if (WINE > 24) { vm_wait<24>(); ctl_wr(ctl + 4 * C_FULL, NUNIT - 3); }
    vm_wait<16>(); ctl_wr(ctl + 4 * C_FULL, NUNIT - 2);
    vm_wait<8>(); ctl_wr(ctl + 4 * C_FULL, NUNIT - 1);
    vm_wait<0>(); ctl_wr(ctl + 4 * C_FULL, NUNIT);
  } else if ((wave & 3) == 0 || wave == 1) {
    const int aw = wave == 1 ? 2 : (wave >> 2) - 1;
    if (aw == 0) { *(LAS unsigned*)(L + L_CTRL + 4 * lane) = 0u; LDS_WAIT(); }
    __builtin_amdgcn_s_barrier();
    const unsigned koff = (c & 7) * PIECE + (c >> 3) * 256 + g * 16;
    const unsigned voff = (4 * (g & 1) + (c >> 2)) * PIECE + (g >> 1) * 256 + (c & 3) * 16;
    const int obase = W_O + ((bp >> 1) * 8 + (j & 7)) * 16384 + hq * 4096 + g * 256 + ((bp & 1) * 8 + (c & 7)) * 16;
    v4u oA[4];
#pragma unroll
    for (int s = 0; s < 4; ++s) oA[s] = v4u{0u, 0u, 0u, 0u};
    {
      const v4u sv = {SENT, SENT, SENT, SENT};
      if (aw == 0) {
        const unsigned long long tag = __hip_atomic_load((GAS unsigned long long*)(ws + W_EPOCH) + FSTR * (j >> 6), __ATOMIC_RELAXED, __HIP_MEMORY_SCOPE_AGENT) + 1ull;
#pragma unroll
        for (int s = 0; s < 4; ++s) __builtin_amdgcn_raw_buffer_store_b128(sv, rsO, obase + (c >> 3) * (4 * 8 * 16384), s * 1024, AUX_SC1);
        if (lane == 0) *(LAS v2u*)(L + L_CTRL + 4 * C_EP) = v2u{(unsigned)tag, (unsigned)(tag >> 32)};
      }
    }
    v4u kf[8], vf[8];
    unsigned full = ctl_wait<1>(ctl + 4 * C_FULL, aw + 1);
    CFENCE();
#pragma unroll
    for (int i = 0; i < 8; ++i) kf[i] = *(LAS v4u*)(L + aw * UNIT + koff + (i >> 2) * 512 + (i & 3) * 64);
    PIN8(kf);
    ctl_wr(ctl + 4 * (C_REL + aw), aw + NATT);
    full = ctl_wait<1>(ctl + 4 * C_FULL, aw + NATT + 1);
    CFENCE();
#pragma unroll
    for (int i = 0; i < 8; ++i) vf[i] = *(LAS v4u*)(L + (aw + NATT) * UNIT + koff + (i >> 2) * 512 + (i & 3) * 64);
    PIN8(vf);
    ctl_wr(ctl + 4 * (C_REL + aw), aw + 2 * NATT);
    ctl_wait<1>(ctl + 4 * C_Q2, 4);
    CFENCE();
    v4u qf[4];
#pragma unroll
    for (int s = 0; s < 4; ++s) {
      LAS char* qp = L + L_PS + g * 2048 + (s * 16 + c) * 16;
      const v4f lo = (*(LAS v4f*)(qp) + *(LAS v4f*)(qp + 8192)) + (*(LAS v4f*)(qp + 16384) + *(LAS v4f*)(qp + 24576));
      const v4f hi = (*(LAS v4f*)(qp + 1024) + *(LAS v4f*)(qp + 9216)) + (*(LAS v4f*)(qp + 17408) + *(LAS v4f*)(qp + 25600));
      qf[s] = v4u{pk2(lo[0], lo[1]), pk2(lo[2], lo[3]), pk2(hi[0], hi[1]), pk2(hi[2], hi[3])};
      if (s & 1) asm volatile("" : "+v"(qf[s - 1]), "+v"(qf[s])::"memory");
    }
    ctl_add(ctl + 4 * C_QR);
    const unsigned poff = L_PS + (g * 8 + (c & 7)) * 16, aoff = L_PS + 512 + (c & 7) * 4;
#pragma unroll 1
    for (int ph = 0; ph < 2; ++ph) {
      const bool mine = (c >> 3) == ph;
      float m = -INFINITY, l = 0.f;
#pragma unroll 1
      for (int t = aw; t < 64; t += NATT) {
        const unsigned uk = 128 * ph + t;
        if (uk >= 2 * NATT) {
          if (full < uk + 1) full = ctl_wait<0>(ctl + 4 * C_FULL, uk + 1);
          CFENCE();
          LAS char* kb = L + (uk % NR) * UNIT + koff;
#pragma unroll
          for (int i = 0; i < 8; ++i) kf[i] = *(LAS v4u*)(kb + (i >> 2) * 512 + (i & 3) * 64);
          PIN8(kf);
          ctl_wr(ctl + 4 * (C_REL + aw), t + NATT < 64 ? uk + NATT : 128 * ph + 64 + aw);
          __builtin_amdgcn_sched_barrier(0);
        } else if (uk >= NATT) {
#pragma unroll
          for (int i = 0; i < 8; ++i) kf[i] = vf[i];
        }
        v4f s0 = {0.f, 0.f, 0.f, 0.f}, s1 = {0.f, 0.f, 0.f, 0.f};
#pragma unroll
        for (int s = 0; s < 4; ++s) { s0 = mfma(kf[s], qf[s], s0); s1 = mfma(kf[4 + s], qf[s], s1); }
        float mx = fmaxf(fmaxf(fmaxf(s0[0], s0[1]), fmaxf(s0[2], s0[3])), fmaxf(fmaxf(s1[0], s1[1]), fmaxf(s1[2], s1[3])));
        mx = xg_max(mx);
        const float mn = fmaxf(m, mx);
        const float alpha = __builtin_amdgcn_exp2f((m - mn) * CSC);
        const float mc = mn * CSC;
        m = mn;
        float p[8];
#pragma unroll
        for (int i = 0; i < 4; ++i) { p[i] = __builtin_amdgcn_exp2f(fmaf(s0[i], CSC, -mc)); p[4 + i] = __builtin_amdgcn_exp2f(fmaf(s1[i], CSC, -mc)); }
        l = fmaf(l, alpha, ((p[0] + p[1]) + (p[2] + p[3])) + ((p[4] + p[5]) + (p[6] + p[7])));
        if (uk < NATT) ctl_wait<0>(ctl + 4 * C_QR, NATT);
        if (mine) {
          *(LAS v4u*)(L + poff + t * PREC) = v4u{pk2(p[0], p[1]), pk2(p[2], p[3]), pk2(p[4], p[5]), pk2(p[6], p[7])};
          if (g == 0) *(LAS float*)(L + aoff + t * PREC) = alpha;
        }
      }
      float f0 = 0.f, f1 = 0.f;
      if (aw == 0) {
        ctl_wait<0>(ctl + 4 * (C_ML + ph), 2);
        CFENCE();
        const v2f a0 = *(LAS v2f*)(L + L_ST + lane * 8), a1 = *(LAS v2f*)(L + L_ST + 512 + lane * 8);
        const float M = fmaxf(m, fmaxf(a0[0], a1[0]));
        const float w = __builtin_amdgcn_exp2f((m - M) * CSC), w0 = __builtin_amdgcn_exp2f((a0[0] - M) * CSC), w1 = __builtin_amdgcn_exp2f((a1[0] - M) * CSC);
        const float inv = 1.0f / xg_sum(fmaf(w, l, fmaf(w0, a0[1], w1 * a1[1])));
        l = w * inv; f0 = w0 * inv; f1 = w1 * inv;
      } else {
        if (mine) *(LAS v2f*)(L + L_ST + (aw - 1) * 512 + lane * 8) = v2f{m, l};
        LDS_WAIT();
        ctl_add(ctl + 4 * (C_ML + ph));
      }
      v4f acc[8];
#pragma unroll
      for (int d = 0; d < 8; ++d) acc[d] = v4f{0.f, 0.f, 0.f, 0.f};
      LAS char* rec = L + L_MRG + (g * 8 + (c & 7)) * 16;
#pragma unroll 1
      for (int t = aw; t < 64; t += NATT) {
        const unsigned uv = 128 * ph + 64 + t;
        v4u pf = *(LAS v4u*)(L + poff + t * PREC);
        const float alpha = *(LAS float*)(L + aoff + t * PREC);
#pragma unroll
        for (int d = 0; d < 8; ++d) acc[d] *= alpha;
        if (t == 63) {
          ctl_wait<0>(ctl + 4 * (C_MRG + 2 * ph), 1);
          CFENCE();
#pragma unroll
          for (int d = 0; d < 8; ++d) kf[d] = __builtin_bit_cast(v4u, *(LAS v4f*)(rec + d * 512) * f0);
          PIN8(kf);
        }
        asm volatile("" : "+v"(pf));
        PIN8(acc);
        if (full < uv + 1) full = ctl_wait<0>(ctl + 4 * C_FULL, uv + 1);
        CFENCE();
        LAS char* vb = L + (uv % NR) * UNIT + voff;
#pragma unroll
        for (int d = 0; d < 8; ++d) {
          const v2u a = trread(vb + (d >> 1) * 64 + (d & 1) * 8), b = trread(vb + 512 + (d >> 1) * 64 + (d & 1) * 8);
          vf[d] = v4u{a[0], a[1], b[0], b[1]};
        }
        PIN8(vf);
        ctl_wr(ctl + 4 * (C_REL + aw), t + NATT < 64 ? uv + NATT : 128 * (ph + 1) + aw);
        __builtin_amdgcn_sched_barrier(0);
#pragma unroll
        for (int d = 0; d < 8; ++d) acc[d] = mfma(vf[d], pf, acc[d]);
        if (ph == 1 && t == OA_AT) {
          vm_wait<0>();
          const v2u tg = *(LAS v2u*)(L + L_CTRL + 4 * C_EP);
          if (lane == 0) __hip_atomic_store((GAS unsigned long long*)(ws + W_CF) + FSTR * j, ((unsigned long long)tg[1] << 32) | tg[0], __ATOMIC_RELAXED, __HIP_MEMORY_SCOPE_AGENT);
          if (c < 8) {
#pragma unroll
            for (int s = 0; s < 4; ++s) __builtin_amdgcn_raw_buffer_store_b128(oA[s], rsO, obase + s * 1024, 0, AUX_SC1);
          }
        }
      }
      if (aw != 0) {
        if (c < 8) {
#pragma unroll
          for (int d = 0; d < 8; ++d) *(LAS v4f*)(rec + (aw - 1) * 4096 + d * 512) = acc[d];
        }
        LDS_WAIT();
        ctl_wr(ctl + 4 * (C_MRG + 2 * ph + aw - 1), 1u);
      } else {
        ctl_wait<0>(ctl + 4 * (C_MRG + 2 * ph + 1), 1);
        CFENCE();
#pragma unroll
        for (int d = 0; d < 8; ++d) vf[d] = *(LAS v4u*)(rec + 4096 + d * 512);
#pragma unroll
        for (int s = 0; s < 4; ++s) {
          const v4f x0 = __builtin_elementwise_fma(__builtin_bit_cast(v4f, vf[2 * s]), v4f{f1, f1, f1, f1}, __builtin_elementwise_fma(acc[2 * s], v4f{l, l, l, l}, __builtin_bit_cast(v4f, kf[2 * s])));
          const v4f x1 = __builtin_elementwise_fma(__builtin_bit_cast(v4f, vf[2 * s + 1]), v4f{f1, f1, f1, f1}, __builtin_elementwise_fma(acc[2 * s + 1], v4f{l, l, l, l}, __builtin_bit_cast(v4f, kf[2 * s + 1])));
          v4u o = {pk2(x0[0], x0[1]), pk2(x0[2], x0[3]), pk2(x1[0], x1[1]), pk2(x1[2], x1[3])};
#pragma unroll
          for (int k = 0; k < 4; ++k) o[k] = min(o[k], SENT - 1u);
          if (ph == 0) oA[s] = o;
          else if (mine) __builtin_amdgcn_raw_buffer_store_b128(o, rsO, obase + s * 1024, 4 * 8 * 16384, AUX_SC1);
        }
      }
    }
  } else {
    const int gw = wave - 2 - (wave >> 2);
    {
      const __amdgpu_buffer_rsrc_t rsQ = __builtin_amdgcn_make_buffer_rsrc((void*)wq, 0, DM * NH * DH * 2, 0x00020000);
      const int wvo = (8 * g * (NH * DH) + 8 * c) * 2, wso = (128 * gw * (NH * DH) + h * DH) * 2;
      const int xrow = ((c < 8 ? bp : bp + 8) * SQ + (c & 7));
      const GAS char* xb = (const GAS char*)x + ((size_t)xrow * DM + 128 * gw + 8 * g) * 2;
#define WQ_ROW(t, jj) ((32 * (((t) + (bp >> 1)) & 3) + ((jj) ^ (4 * (bp & 1)))) * (NH * DH * 2))
      v4u xf[4], w[4][8];
      {
#pragma unroll
        for (int jj = 0; jj < 4; ++jj) w[0][jj] = __builtin_amdgcn_raw_buffer_load_b128(rsQ, wvo, wso + WQ_ROW(0, jj), G_AUX);
        const unsigned pf = *(const GAS unsigned*)(xb + (hq & 1) * 128);
        __builtin_amdgcn_sched_barrier(0);
        __builtin_amdgcn_s_barrier();
        __builtin_amdgcn_sched_barrier(0);
        asm volatile("" ::"v"(pf));
        vm_wait<0>();
        if (grpB) { ctl_add(ctl + 4 * C_R1); ctl_wait<2>(ctl + 4 * C_FULL, G_QB); }
        else __builtin_amdgcn_s_sleep(Q_GAP);
        __builtin_amdgcn_sched_barrier(0);
      }
#pragma unroll
      for (int jj = 4; jj < 8; ++jj) w[0][jj] = __builtin_amdgcn_raw_buffer_load_b128(rsQ, wvo, wso + WQ_ROW(0, jj), G_AUX2);
#pragma unroll
      for (int tt = 0; tt < 4; ++tt) xf[tt] = *(const GAS v4u*)(xb + ((tt + (bp >> 1)) & 3) * 64);
#pragma unroll
      for (int t = 1; t < 3; ++t)
#pragma unroll
        for (int jj = 0; jj < 8; ++jj) w[t][jj] = __builtin_amdgcn_raw_buffer_load_b128(rsQ, wvo, wso + WQ_ROW(t, jj), G_AUX2);
      __builtin_amdgcn_sched_barrier(0);
      ctl_add(ctl + 4 * C_QI);
      if (bp & 1) {
#pragma unroll
        for (int t = 0; t < 4; ++t) xf[t] = v4u{xf[t][2], xf[t][3], xf[t][0], xf[t][1]};
      }
      v4f qa[8];
#pragma unroll
      for (int d = 0; d < 8; ++d) qa[d] = v4f{0.f, 0.f, 0.f, 0.f};
#pragma unroll
      for (int t = 0; t < 4; ++t) {
#pragma unroll
        for (int dd = 0; dd < 8; ++dd) {
          v4u f;
#pragma unroll
          for (int v = 0; v < 4; ++v) f[v] = __builtin_amdgcn_perm(w[t][2 * v + 1][dd >> 1], w[t][2 * v][dd >> 1], (dd & 1) ? 0x07060302u : 0x05040100u);
          qa[dd] = mfma(f, xf[t], qa[dd]);
          if (dd & 1) __builtin_amdgcn_sched_barrier(0);
        }
        if (t == 0) {
#pragma unroll
          for (int jj = 0; jj < 8; ++jj) {
            w[3][jj] = __builtin_amdgcn_raw_buffer_load_b128(rsQ, wvo, wso + WQ_ROW(3, jj), G_AUX2);
            __builtin_amdgcn_sched_barrier(0);
          }
        }
      }
      LAS char* part = L + L_PS + (gw & 3) * 8192 + lane * 16;
      if (gw >= 4) {
        ctl_wait<0>(ctl + 4 * C_Q1, 4);
        CFENCE();
      }
#pragma unroll
      for (int i = 0; i < 4; ++i) {
        v4f lo = {qa[0][i], qa[1][i], qa[2][i], qa[3][i]}, hi = {qa[4][i], qa[5][i], qa[6][i], qa[7][i]};
        if (gw >= 4) { lo += *(LAS v4f*)(part + i * 2048); hi += *(LAS v4f*)(part + i * 2048 + 1024); }
        *(LAS v4f*)(part + i * 2048) = lo;
        *(LAS v4f*)(part + i * 2048 + 1024) = hi;
      }
      LDS_WAIT();
      ctl_add(ctl + 4 * (gw >= 4 ? C_Q2 : C_Q1));
    }
    const int xc = j & 7, idx = j >> 3, ct = (idx + CT_ROT) & 7, rg = idx >> 3;
    unsigned hb = 0;
#define HBAR(SLP) do { hb += NHELP; ctl_add(ctl + 4 * C_HB); ctl_wait<SLP>(ctl + 4 * C_HB, hb); CFENCE(); } while (0)
    const int pdst = W_P + (idx * 128 + gw * 8 + xc) * 1024;
    v4u wf[16];
    {
      const __amdgpu_buffer_rsrc_t rsW = __builtin_amdgcn_make_buffer_rsrc((void*)wo, 0, DM * NH * DH * 2, 0x00020000);
      ctl_wait<32>(ctl + 4 * C_FULL, WO_AT + 2 * gw);
#pragma unroll
      for (int ks = 0; ks < 16; ++ks) {
        wf[ks] = __builtin_amdgcn_raw_buffer_load_b128(rsW, g * 2048 + c * 16, ((4 * xc + (ks >> 2)) * 128 + (ks & 3) * 32 + 4 * gw) * 2048 + ct * 256, G_WOAUX);
        __builtin_amdgcn_s_sleep(WF_SLEEP);
        __builtin_amdgcn_sched_barrier(0);
      }
      LAS char* sw = L + L_STG + gw * PIECE + lane * 16;
      LAS char* sr = L + L_STG + 2 * g * PIECE + (c >> 2) * 256 + gw * 32 + (c & 3) * 8;
#pragma unroll
      for (int r = 0; r < 16; ++r) {
        *(LAS v4u*)(sw) = wf[r];
        LDS_WAIT();
        HBAR(2);
        const v2u a0 = trread(sr), b0 = trread(sr + PIECE);
        wf[r] = v4u{a0[0], a0[1], b0[0], b0[1]};
        asm volatile("" : "+v"(wf[r])::"memory");
        HBAR(2);
      }
    }
    const v2u tagv = *(LAS v2u*)(L + L_CTRL + 4 * C_EP);
    const unsigned long long tag = ((unsigned long long)tagv[1] << 32) | tagv[0];
    unsigned tb[4];
    tag_bytes(tagv, tb);
    ctl_wait<16>(ctl + 4 * C_FULL, NUNIT - 16);
    ctl_wait<2>(ctl + 4 * C_LDONE, 1);
    {
      const int src = (2 * rg + (lane & 1)) * 32 + (gw >> 1) * 8 + xc;
      GAS unsigned long long* fl = (GAS unsigned long long*)(ws + W_CF) + FSTR * src;
      for (unsigned sp = 0; sp < (1u << 16); ++sp) {
        if (__all(__hip_atomic_load(fl, __ATOMIC_RELAXED, __HIP_MEMORY_SCOPE_AGENT) == tag)) break;
        __builtin_amdgcn_s_sleep(G1_SLEEP);
      }
      __builtin_amdgcn_fence(__ATOMIC_ACQUIRE, "wavefront");
    }
#pragma unroll 1
    for (int ph = 0; ph < 2; ++ph) {
      v4u f0, f1;
      for (unsigned sp = 0; sp < (1u << 16); ++sp) {
        CFENCE();
        f0 = __builtin_amdgcn_raw_buffer_load_b128(rsO, lane * 16, W_O + ((ph * 4 + rg) * 8 + xc) * 16384 + gw * 2048, AUX_SC1);
        f1 = __builtin_amdgcn_raw_buffer_load_b128(rsO, lane * 16, W_O + ((ph * 4 + rg) * 8 + xc) * 16384 + gw * 2048 + 1024, AUX_SC1);
        const unsigned mxv = max(max(max(f0[0], f0[1]), max(f0[2], f0[3])), max(max(f1[0], f1[1]), max(f1[2], f1[3])));
        if (__all(mxv != SENT)) break;
        __builtin_amdgcn_s_sleep(G1_SLEEP);
      }
      *(LAS v4u*)(L + L_OB + gw * 2048 + lane * 16) = f0;
      *(LAS v4u*)(L + L_OB + gw * 2048 + 1024 + lane * 16) = f1;
      LDS_WAIT();
      HBAR(0);
      v4f oa[4];
#pragma unroll
      for (int k = 0; k < 4; ++k) oa[k] = v4f{0.f, 0.f, 0.f, 0.f};
#pragma unroll
      for (int f = 0; f < 16; ++f) oa[f & 3] = mfma(wf[f], *(LAS v4u*)(L + L_OB + f * 1024 + lane * 16), oa[f & 3]);
      v4u pt = __builtin_bit_cast(v4u, (oa[0] + oa[1]) + (oa[2] + oa[3]));
#pragma unroll
      for (int k = 0; k < 4; ++k) pt[k] = ((pt[k] + ((pt[k] & 0x7F800000u) == 0x7F800000u ? 0u : 0x80u)) & ~0xFFu) | tb[k];
      __builtin_amdgcn_raw_buffer_store_b128(pt, rsO, lane * 16, pdst + ph * 65536, AUX_SC1);
      if (ph == 0) HBAR(0);
    }
    if (gw < 2) {
      GAS v2u* dst = (GAS v2u*)((GAS char*)out + ((size_t)(64 * gw + 16 * rg + c) * DM + 128 * ct + 16 * xc + 4 * g) * 2);
      asm volatile("" : "+v"(dst));
      const v4u tv4 = {tb[0], tb[1], tb[2], tb[3]};
      v2u res;
      for (unsigned sp = 0; sp < (1u << 16); ++sp) {
        CFENCE();
        v4u q[8];
#pragma unroll
        for (int k = 0; k < 8; ++k) q[k] = __builtin_amdgcn_raw_buffer_load_b128(rsO, lane * 16, W_P + ((idx * 2 + gw) * 64 + xc * 8 + k) * 1024, AUX_SC1);
        v4u bad = q[0] ^ tv4;
        v4f s = __builtin_bit_cast(v4f, bad);
#pragma unroll
        for (int k = 1; k < 8; ++k) { const v4u y = q[k] ^ tv4; bad |= y; s += __builtin_bit_cast(v4f, y); }
        res = v2u{pk2(s[0], s[1]), pk2(s[2], s[3])};
        asm volatile("" : "+v"(res));
        if (__all((((bad[0] | bad[1]) | (bad[2] | bad[3])) & 0xFFu) == 0u)) break;
        __builtin_amdgcn_s_sleep(X2_SLEEP);
      }
      *dst = res;
      if (gw == 0 && (j & 63) == 0 && lane == 0) __hip_atomic_store((GAS unsigned long long*)(ws + W_EPOCH) + FSTR * rg, tag, __ATOMIC_RELAXED, __HIP_MEMORY_SCOPE_AGENT);
    }
  }
}
}

extern "C" void kernel_launch(void* const* d_in, const int* in_sizes, int n_in, void* d_out, int out_size, void* d_ws, size_t ws_size,
                              hipStream_t stream) {
  (void)in_sizes; (void)n_in; (void)out_size; (void)ws_size;
  (void)hipFuncSetAttribute((const void*)mha_block_kernel, hipFuncAttributeMaxDynamicSharedMemorySize, LDS_BYTES);
  mha_block_kernel<<<NWG, NTHR, LDS_BYTES, stream>>>((const unsigned short*)d_in[0], (const unsigned short*)d_in[1], (const unsigned short*)d_in[2],
                                                     (const unsigned short*)d_in[3], (const unsigned short*)d_in[4], (unsigned short*)d_out,
                                                     (unsigned char*)d_ws);
}
```

```cpp
#include <hip/hip_runtime.h>
#include <hip/hip_bf16.h>
#include <stdint.h>

#define GAS __attribute__((address_space(1)))
#define LAS __attribute__((address_space(3)))
typedef unsigned v4u __attribute__((ext_vector_type(4)));
typedef unsigned v2u __attribute__((ext_vector_type(2)));
typedef float v4f __attribute__((ext_vector_type(4)));
typedef float v2f __attribute__((ext_vector_type(2)));
typedef short v4s __attribute__((ext_vector_type(4)));
typedef __bf16 v8bf __attribute__((ext_vector_type(8)));
typedef __bf16 v2bf __attribute__((ext_vector_type(2)));

namespace {
constexpr int NB = 16, SQ = 8, DM = 1024, NH = 32, SK = 2048, DH = 128;
constexpr int NWG = 256, NTHR = 768, NATT = 3, NHELP = 8;
constexpr int PIECE = 1056;
constexpr int UNIT = 8 * PIECE;
constexpr int NR = 14;
constexpr int NUNIT = 256;
constexpr int AUX_NT = 2, AUX_SC1 = 16;
constexpr int WIN = 20;
constexpr int WINA = 32, WIN0 = 56, U0 = 32;
constexpr int WINE = 32, UE = 16;
constexpr int WINB = 28;
constexpr int CK_U0 = 32, CK_T0 = 1200, CK_U1 = 108, CK_T1 = 3455, CK_U2 = 200, CK_T2 = 5955;
constexpr int CK_G = 160;
constexpr int UI = 2, QI_GO = 6, Q_GAP = 4;
constexpr int NPRE = 3, QB_AT = 15;
constexpr int WO_AT = 68, WF_SLEEP = 8;
constexpr int OA_AT = 57;
constexpr int G1_SLEEP = 1, X2_SLEEP = 1, CT_ROT = 2;
constexpr int L_CTRL = NR * UNIT;
constexpr int PREC = 544;
constexpr int L_PS = L_CTRL + 256;
constexpr int L_OB = L_PS;
constexpr int L_MRG = L_PS + 32 * PREC;
constexpr int L_ST = L_PS + 64 * PREC;
constexpr int L_STG = L_ST + 1024;
constexpr int LDS_BYTES = 160 * 1024;
static_assert(L_STG + 8 * PIECE <= LDS_BYTES && 16384 <= 32 * PREC && 32 * PREC + 8192 <= 48 * PREC && NR <= 14, "LDS map");
enum { C_FULL = 0, C_REL = 4, C_Q1 = 8, C_Q2 = 9, C_QI = 10, C_HB = 11, C_QR = 12, C_EP = 14, C_ML = 18, C_MRG = 20, C_LDONE = 24, C_R1 = 25 };
constexpr int W_EPOCH = 0, W_CF = 65536, FSTR = 16;
constexpr int W_O = 1 << 20;
constexpr int W_P = 2 << 20;
constexpr int WS_USED = 6 << 20;
constexpr unsigned SENT = 0xFFFFFFFFu;
constexpr float CSC = 0.08838834764831845f * 1.4426950408889634f;
constexpr unsigned SPIN_CAP = 1u << 22;

__device__ __forceinline__ unsigned rfl(unsigned x) { return __builtin_amdgcn_readfirstlane(x); }
__device__ __forceinline__ v4f mfma(v4u a, v4u b, v4f c) {
  return __builtin_amdgcn_mfma_f32_16x16x32_bf16(__builtin_bit_cast(v8bf, a), __builtin_bit_cast(v8bf, b), c, 0, 0, 0);
}
__device__ __forceinline__ unsigned pk2(float lo, float hi) {
  v2f x = {lo, hi};
  return __builtin_bit_cast(unsigned, __builtin_convertvector(x, v2bf));
}
__device__ __forceinline__ v2u trread(LAS char* p) {
  return __builtin_bit_cast(v2u, __builtin_amdgcn_ds_read_tr16_b64_v4i16((LAS v4s*)p));
}
__device__ __forceinline__ float xg_max(float x) {
  auto s = __builtin_amdgcn_permlane16_swap(__float_as_uint(x), __float_as_uint(x), false, false);
  x = fmaxf(__uint_as_float(s[0]), __uint_as_float(s[1]));
  auto t = __builtin_amdgcn_permlane32_swap(__float_as_uint(x), __float_as_uint(x), false, false);
  return fmaxf(__uint_as_float(t[0]), __uint_as_float(t[1]));
}
__device__ __forceinline__ float xg_sum(float x) {
  auto s = __builtin_amdgcn_permlane16_swap(__float_as_uint(x), __float_as_uint(x), false, false);
  x = __uint_as_float(s[0]) + __uint_as_float(s[1]);
  auto t = __builtin_amdgcn_permlane32_swap(__float_as_uint(x), __float_as_uint(x), false, false);
  return __uint_as_float(t[0]) + __uint_as_float(t[1]);
}
#define LDS_WAIT() asm volatile("s_waitcnt lgkmcnt(0)" ::: "memory")
#define CFENCE() asm volatile("" ::: "memory")
#define PIN8(a) asm volatile("" : "+v"(a[0]), "+v"(a[1]), "+v"(a[2]), "+v"(a[3]), "+v"(a[4]), "+v"(a[5]), "+v"(a[6]), "+v"(a[7])::"memory")
template <int N> __device__ __forceinline__ void vm_wait() { asm volatile("s_waitcnt vmcnt(%c0)" ::"n"(N) : "memory"); }
__device__ __forceinline__ unsigned ctl_rd(unsigned addr) {
  unsigned v;
  asm volatile("ds_read_b32 %0, %1\n\ts_waitcnt lgkmcnt(0)" : "=v"(v) : "v"(addr) : "memory");
  return rfl(v);
}
__device__ __forceinline__ void ctl_wr(unsigned addr, unsigned val) {
  asm volatile("s_mov_b64 exec, 1\n\tds_write_b32 %0, %1\n\ts_mov_b64 exec, -1" ::"v"(addr), "v"(val) : "memory");
}
__device__ __forceinline__ unsigned ctl_add(unsigned addr) {
  unsigned v;
  asm volatile("s_mov_b64 exec, 1\n\tds_add_rtn_u32 %0, %1, %2\n\ts_mov_b64 exec, -1\n\ts_waitcnt lgkmcnt(0)" : "=&v"(v) : "v"(addr), "v"(1u) : "memory");
  return rfl(v);
}
template <int SLP> __device__ __forceinline__ unsigned ctl_wait(unsigned addr, unsigned need) {
  unsigned v = ctl_rd(addr);
  for (unsigned sp = 0; v < need && sp < SPIN_CAP; ++sp) {
    __builtin_amdgcn_s_sleep(SLP);
    v = ctl_rd(addr);
  }
  return v;
}

__device__ __forceinline__ void tag_bytes(v2u tagv, unsigned (&tb)[4]) {
  const unsigned long long e = (((unsigned long long)tagv[1] << 32) | tagv[0]) - 1ull;
  const unsigned hs = (unsigned)(e >> 8) * 0x9E3779B1u ^ (unsigned)(e >> 40) * 0x85EBCA77u;
#pragma unroll
  for (int k = 0; k < 4; ++k) tb[k] = rfl((tagv[0] + ((hs >> (8 * k)) & 0xFFu) % 254u) & 0xFFu);
}

#define PUBL(k) ".if %c[b] == " #k "\n\ts_mov_b64 exec, 1\n\tds_write_b32 %[fa], v23\n\ts_mov_b64 exec, -1\n\t.endif\n\t"
#define PC(k, VO, IMM) "s_waitcnt vmcnt(%c[w])\n\t" PUBL(k) "buffer_load_dwordx4 " VO ", %[rs], %[so] offen offset:" IMM " nt lds\n\t"
template <int W>
__device__ __forceinline__ void dma_units(__amdgpu_buffer_rsrc_t rs, unsigned soff, unsigned uend, unsigned& m0b, unsigned& u, unsigned& lim, unsigned vo0,
                                          unsigned vo1, unsigned vctl, unsigned faddr) {
  unsigned sp;
  asm volatile(
      "0:\n\t"
      "s_cmp_lt_u32 %[u], %[lim]\n\t"
      "s_cbranch_scc1 2f\n\t"
      "s_mov_b32 %[sp], 0\n"
      "1:\n\t"
      "ds_read_b32 v20, %[vc] offset:16\n\tds_read_b32 v21, %[vc] offset:20\n\tds_read_b32 v22, %[vc] offset:24\n\t"
      "s_waitcnt lgkmcnt(0)\n\t"
      "v_min3_u32 v20, v20, v21, v22\n\t"
      "s_add_u32 %[sp], %[sp], 1\n\t"
      "v_readfirstlane_b32 %[lim], v20\n\t"
      "s_add_u32 %[lim], %[lim], %c[nr]\n\t"
      "s_cmp_lt_u32 %[u], %[lim]\n\t"
      "s_cbranch_scc1 2f\n\t"
      "s_sleep 1\n\t"
      "s_cmp_lt_u32 %[sp], %c[cap]\n\t"
      "s_cbranch_scc1 1b\n"
      "2:\n\t"
      "s_mov_b32 m0, %[m0b]\n\t"
      "v_mov_b32 v23, %[u]\n\t"
      "v_subrev_u32 v23, %c[a], v23\n\t"
      PC(0, "%[vo0]", "0")
      "ds_read_b32 v20, %[vc] offset:16\n\tds_read_b32 v21, %[vc] offset:20\n\tds_read_b32 v22, %[vc] offset:24\n\t"
      "s_add_u32 m0, m0, 32\n\t"
      PC(1, "%[vo0]", "1024")
      "s_add_u32 m0, m0, 32\n\t"
      PC(2, "%[vo0]", "2048")
      "s_add_u32 m0, m0, 32\n\t"
      PC(3, "%[vo0]", "3072")
      "s_add_u32 m0, m0, 0x1020\n\t"
      PC(4, "%[vo1]", "0")
      "s_waitcnt lgkmcnt(0)\n\t"
      "v_min3_u32 v20, v20, v21, v22\n\t"
      "s_add_u32 m0, m0, 32\n\t"
      PC(5, "%[vo1]", "1024")
      "v_readfirstlane_b32 %[lim], v20\n\t"
      "s_add_u32 %[m0b], %[m0b], %c[unit]\n\t"
      "s_add_u32 m0, m0, 32\n\t"
      PC(6, "%[vo1]", "2048")
      "s_add_u32 %[lim], %[lim], %c[nr]\n\t"
      "s_cmp_lt_u32 %[m0b], %c[ring]\n\t"
      "s_cselect_b32 %[m0b], %[m0b], 0\n\t"
      "s_add_u32 %[u], %[u], 1\n\t"
      "s_add_u32 m0, m0, 32\n\t"
      PC(7, "%[vo1]", "3072")
      "s_add_u32 %[so], %[so], 0x2000\n\t"
      "s_cmp_lt_u32 %[u], %[ue]\n\t"
      "s_cbranch_scc1 0b"
      : [m0b] "+&s"(m0b), [u] "+&s"(u), [lim] "+&s"(lim), [so] "+&s"(soff), [sp] "=&s"(sp)
      : [ue] "s"(uend), [w] "n"(W - 1), [a] "n"((W - 1) / 8), [b] "n"((W - 1) % 8), [nr] "n"(NR), [cap] "n"(SPIN_CAP), [unit] "n"(UNIT), [ring] "n"(NR * UNIT), [rs] "s"(rs),
        [vo0] "v"(vo0), [vo1] "v"(vo1), [vc] "v"(vctl), [fa] "v"(faddr)
      : "memory", "scc", "v20", "v21", "v22", "v23");
}

__global__ void __launch_bounds__(NTHR, 3)
mha_block_kernel(const unsigned short* __restrict__ x, const unsigned short* __restrict__ kc, const unsigned short* __restrict__ vc,
                 const unsigned short* __restrict__ wq, const unsigned short* __restrict__ wo, unsigned short* __restrict__ out,
                 unsigned char* __restrict__ ws) {
  extern __shared__ __attribute__((aligned(16))) char smem[];
  LAS char* L = (LAS char*)smem;
  const unsigned lbase = (unsigned)(size_t)L;
  const int lane = threadIdx.x & 63, wave = rfl(threadIdx.x >> 6);
  const int g = lane >> 4, c = lane & 15;
  const int j = blockIdx.x, hq = (j >> 3) & 3, h = 4 * (j & 7) + hq, bp = j >> 5;
  const unsigned ctl = lbase + L_CTRL;
  const bool grpB = hq == 3;
  const __amdgpu_buffer_rsrc_t rsO = __builtin_amdgcn_make_buffer_rsrc((void*)ws, 0, WS_USED, 0x00020000);

  if (wave == 0) {
    __builtin_amdgcn_s_setprio(3);
    __builtin_amdgcn_s_waitcnt(0x0F70);
    const unsigned long long tl0 = __builtin_amdgcn_s_memrealtime();
    const unsigned vo0 = lane * 16, vo1 = lane * 16 + 4096;
    const unsigned sbase = (unsigned)(bp * NH + h) * (SK * DH * 2);
    unsigned m0b = 0, u = 0, lim = NR;
    unsigned ub = 0;
    static_assert(QB_AT <= NR + NPRE * NATT - 1 - (WIN0 - 1) / 8 && NPRE * NATT <= NR - 1 - (WIN0 - 1) / 8 + 2 * NATT, "published before the ring is full");
    static_assert(UI >= 2 && UI + (WIN0 - 1) / 8 < U0 && U0 <= CK_U0 && CK_U0 < CK_U1 && CK_U1 < CK_U2 && CK_U2 < NUNIT - UE, "segment order");
#pragma unroll 1
    while (u < NUNIT) {
      const __amdgpu_buffer_rsrc_t rs = __builtin_amdgcn_make_buffer_rsrc((void*)((u & 64) ? vc : kc), 0, NB * NH * SK * DH * 2, 0x00020000);
      const unsigned soff = sbase + (u >> 7) * (8u * NH * SK * DH * 2) + (u & 63) * 8192u;
      unsigned ue = (u | 63) + 1;
      if (u < U0) {
        if (u == 2) __builtin_amdgcn_s_barrier();
        if (u == UI) {
          vm_wait<0>(); ctl_wr(ctl + 4 * C_FULL, UI);
          if (grpB) ctl_wait<1>(ctl + 4 * C_R1, NHELP); else ctl_wait<1>(ctl + 4 * C_QI, QI_GO);
        }
        const bool early = u < UI + (WIN0 - 1) / 8;
        if (!grpB && u >= UI) dma_units<WINA>(rs, soff, u < UI + (WINA - 1) / 8 ? UI + (WINA - 1) / 8 : U0, m0b, u, lim, vo0, vo1, ctl, u < UI + (WINA - 1) / 8 ? ctl + 252 : ctl + 4 * C_FULL);
        else dma_units<WIN0>(rs, soff, u < UI ? u + 1 : (early ? UI + (WIN0 - 1) / 8 : U0), m0b, u, lim, vo0, vo1, ctl, early ? ctl + 252 : ctl + 4 * C_FULL);
        if (u <= 2) lim = NR;
      } else if (u >= NUNIT - UE) {
        dma_units<WINE>(rs, soff, ue, m0b, u, lim, vo0, vo1, ctl, ctl + 4 * C_FULL);
      } else {
        if (u == CK_U0 || u == CK_U1 || u == CK_U2) {
          const int late = (int)(unsigned)(__builtin_amdgcn_s_memrealtime() - tl0) - (u == CK_U0 ? CK_T0 : (u == CK_U1 ? CK_T1 : CK_T2));
          ub = u + (late > 0 ? min((late * CK_G) >> 10, NUNIT) : 0);
        }
        ue = min(ue, u < CK_U0 ? (unsigned)CK_U0 : (u < CK_U1 ? (unsigned)CK_U1 : (u < CK_U2 ? (unsigned)CK_U2 : (unsigned)(NUNIT - UE))));
        if (u < ub) dma_units<WINB>(rs, soff, min(ue, ub), m0b, u, lim, vo0, vo1, ctl, ctl + 4 * C_FULL);
        else dma_units<WIN>(rs, soff, ue, m0b, u, lim, vo0, vo1, ctl, ctl + 4 * C_FULL);
      }
    }
    ctl_wr(ctl + 4 * C_LDONE, 1u);
    if (WINE > 48) { vm_wait<48>(); ctl_wr(ctl + 4 * C_FULL, NUNIT - 6); }
    if (WINE > 40) { vm_wait<40>(); ctl_wr(ctl + 4 * C_FULL, NUNIT - 5); }
    if (WINE > 32) { vm_wait<32>(); ctl_wr(ctl + 4 * C_FULL, NUNIT - 4); }
    if (WINE > 24) { vm_wait<24>(); ctl_wr(ctl + 4 * C_FULL, NUNIT - 3); }
    vm_wait<16>(); ctl_wr(ctl + 4 * C_FULL, NUNIT - 2);
    vm_wait<8>(); ctl_wr(ctl + 4 * C_FULL, NUNIT - 1);
    vm_wait<0>(); ctl_wr(ctl + 4 * C_FULL, NUNIT);
  } else if ((wave & 3) == 0 || wave == 1) {
    const int aw = wave == 1 ? 2 : (wave >> 2) - 1;
    if (aw == 0) { *(LAS unsigned*)(L + L_CTRL + 4 * lane) = 0u; LDS_WAIT(); }
    __builtin_amdgcn_s_barrier();
    const unsigned koff = (c & 7) * PIECE + (c >> 3) * 256 + g * 16;
    const unsigned voff = (4 * (g & 1) + (c >> 2)) * PIECE + (g >> 1) * 256 + (c & 3) * 16;
    const int obase = W_O + ((bp >> 1) * 8 + (j & 7)) * 16384 + hq * 4096 + g * 256 + ((bp & 1) * 8 + (c & 7)) * 16;
    {
      const v4u sv = {SENT, SENT, SENT, SENT};
      if (aw == 0) {
        const unsigned long long tag = __hip_atomic_load((GAS unsigned long long*)(ws + W_EPOCH) + FSTR * (j >> 6), __ATOMIC_RELAXED, __HIP_MEMORY_SCOPE_AGENT) + 1ull;
#pragma unroll
        for (int s = 0; s < 4; ++s) __builtin_amdgcn_raw_buffer_store_b128(sv, rsO, obase + (c >> 3) * (4 * 8 * 16384), s * 1024, AUX_SC1);
        if (lane == 0) *(LAS v2u*)(L + L_CTRL + 4 * C_EP) = v2u{(unsigned)tag, (unsigned)(tag >> 32)};
      }
    }
    v4u kf[8], vf[8];
    unsigned full = ctl_wait<1>(ctl + 4 * C_FULL, aw + 1);
    CFENCE();
#pragma unroll
    for (int i = 0; i < 8; ++i) kf[i] = *(LAS v4u*)(L + aw * UNIT + koff + (i >> 2) * 512 + (i & 3) * 64);
    PIN8(kf);
    ctl_wr(ctl + 4 * (C_REL + aw), aw + NATT);
    full = ctl_wait<1>(ctl + 4 * C_FULL, aw + NATT + 1);
    CFENCE();
#pragma unroll
    for (int i = 0; i < 8; ++i) vf[i] = *(LAS v4u*)(L + (aw + NATT) * UNIT + koff + (i >> 2) * 512 + (i & 3) * 64);
    PIN8(vf);
    ctl_wr(ctl + 4 * (C_REL + aw), aw + 2 * NATT);
    v4u k3[8];
    full = ctl_wait<1>(ctl + 4 * C_FULL, aw + 2 * NATT + 1);
    CFENCE();
#pragma unroll
    for (int i = 0; i < 8; ++i) k3[i] = *(LAS v4u*)(L + (aw + 2 * NATT) * UNIT + koff + (i >> 2) * 512 + (i & 3) * 64);
    PIN8(k3);
    ctl_wr(ctl + 4 * (C_REL + aw), aw + 3 * NATT);
    ctl_wait<1>(ctl + 4 * C_Q2, 4);
    CFENCE();
    v4u qf[4];
#pragma unroll
    for (int s = 0; s < 4; ++s) {
      LAS char* qp = L + L_PS + g * 2048 + (s * 16 + c) * 16;
      const v4f lo = (*(LAS v4f*)(qp) + *(LAS v4f*)(qp + 8192)) + (*(LAS v4f*)(qp + 16384) + *(LAS v4f*)(qp + 24576));
      const v4f hi = (*(LAS v4f*)(qp + 1024) + *(LAS v4f*)(qp + 9216)) + (*(LAS v4f*)(qp + 17408) + *(LAS v4f*)(qp + 25600));
      qf[s] = v4u{pk2(lo[0], lo[1]), pk2(lo[2], lo[3]), pk2(hi[0], hi[1]), pk2(hi[2], hi[3])};
      asm volatile("" : "+v"(qf[s])::"memory");
    }
    ctl_add(ctl + 4 * C_QR);
    const unsigned poff = L_PS + (g * 8 + (c & 7)) * 16, aoff = L_PS + 512 + (c & 7) * 4;
    float m = -INFINITY, l = 0.f;
    auto ktile = [&](int t, bool mine, bool first) {
      v4f s0 = {0.f, 0.f, 0.f, 0.f}, s1 = {0.f, 0.f, 0.f, 0.f};
#pragma unroll
      for (int s = 0; s < 4; ++s) { s0 = mfma(kf[s], qf[s], s0); s1 = mfma(kf[4 + s], qf[s], s1); }
      float mx = fmaxf(fmaxf(fmaxf(s0[0], s0[1]), fmaxf(s0[2], s0[3])), fmaxf(fmaxf(s1[0], s1[1]), fmaxf(s1[2], s1[3])));
      mx = xg_max(mx);
      const float mn = fmaxf(m, mx);
      const float alpha = __builtin_amdgcn_exp2f((m - mn) * CSC);
      const float mc = mn * CSC;
      m = mn;
      float p[8];
#pragma unroll
      for (int i = 0; i < 4; ++i) { p[i] = __builtin_amdgcn_exp2f(fmaf(s0[i], CSC, -mc)); p[4 + i] = __builtin_amdgcn_exp2f(fmaf(s1[i], CSC, -mc)); }
      l = fmaf(l, alpha, ((p[0] + p[1]) + (p[2] + p[3])) + ((p[4] + p[5]) + (p[6] + p[7])));
      if (first) ctl_wait<0>(ctl + 4 * C_QR, NATT);
      if (mine) {
        *(LAS v4u*)(L + poff + t * PREC) = v4u{pk2(p[0], p[1]), pk2(p[2], p[3]), pk2(p[4], p[5]), pk2(p[6], p[7])};
        if (g == 0) *(LAS float*)(L + aoff + t * PREC) = alpha;
      }
    };
#pragma unroll 1
    for (int i = 0; i < NPRE; ++i) {
      ktile(aw + NATT * i, c < 8, i == 0);
#pragma unroll
      for (int k = 0; k < 8; ++k) { kf[k] = vf[k]; vf[k] = k3[k]; }
    }
    v4u oA[4];
#pragma unroll
    for (int s = 0; s < 4; ++s) oA[s] = v4u{0u, 0u, 0u, 0u};
#pragma unroll 1
    for (int ph = 0; ph < 2; ++ph) {
      const bool mine = (c >> 3) == ph;
#pragma unroll 1
      for (int t = ph ? aw : aw + NPRE * NATT; t < 64; t += NATT) {
        const unsigned uk = 128 * ph + t;
        if (full < uk + 1) full = ctl_wait<0>(ctl + 4 * C_FULL, uk + 1);
        CFENCE();
        LAS char* kb = L + (uk % NR) * UNIT + koff;
#pragma unroll
        for (int i = 0; i < 8; ++i) kf[i] = *(LAS v4u*)(kb + (i >> 2) * 512 + (i & 3) * 64);
        PIN8(kf);
        ctl_wr(ctl + 4 * (C_REL + aw), t + NATT < 64 ? uk + NATT : 128 * ph + 64 + aw);
        __builtin_amdgcn_sched_barrier(0);
        ktile(t, mine, false);
      }
      float f0 = 0.f, f1 = 0.f;
      if (aw == 0) {
        ctl_wait<0>(ctl + 4 * (C_ML + ph), 2);
        CFENCE();
        const v2f a0 = *(LAS v2f*)(L + L_ST + lane * 8), a1 = *(LAS v2f*)(L + L_ST + 512 + lane * 8);
        const float M = fmaxf(m, fmaxf(a0[0], a1[0]));
        const float w = __builtin_amdgcn_exp2f((m - M) * CSC), w0 = __builtin_amdgcn_exp2f((a0[0] - M) * CSC), w1 = __builtin_amdgcn_exp2f((a1[0] - M) * CSC);
        const float inv = 1.0f / xg_sum(fmaf(w, l, fmaf(w0, a0[1], w1 * a1[1])));
        l = w * inv; f0 = w0 * inv; f1 = w1 * inv;
      } else {
        if (mine) *(LAS v2f*)(L + L_ST + (aw - 1) * 512 + lane * 8) = v2f{m, l};
        LDS_WAIT();
        ctl_add(ctl + 4 * (C_ML + ph));
      }
      v4f acc[8];
#pragma unroll
      for (int d = 0; d < 8; ++d) acc[d] = v4f{0.f, 0.f, 0.f, 0.f};
      LAS char* rec = L + L_MRG + (g * 8 + (c & 7)) * 16;
#pragma unroll 1
      for (int t = aw; t < 64; t += NATT) {
        const unsigned uv = 128 * ph + 64 + t;
        v4u pf = *(LAS v4u*)(L + poff + t * PREC);
        const float alpha = *(LAS float*)(L + aoff + t * PREC);
#pragma unroll
        for (int d = 0; d < 8; ++d) acc[d] *= alpha;
        if (t == 63) {
          ctl_wait<0>(ctl + 4 * (C_MRG + 2 * ph), 1);
          CFENCE();
#pragma unroll
          for (int d = 0; d < 8; ++d) kf[d] = __builtin_bit_cast(v4u, *(LAS v4f*)(rec + d * 512) * f0);
          PIN8(kf);
        }
        asm volatile("" : "+v"(pf));
        PIN8(acc);
        if (full < uv + 1) full = ctl_wait<0>(ctl + 4 * C_FULL, uv + 1);
        CFENCE();
        LAS char* vb = L + (uv % NR) * UNIT + voff;
#pragma unroll
        for (int d = 0; d < 8; ++d) {
          const v2u a = trread(vb + (d >> 1) * 64 + (d & 1) * 8), b = trread(vb + 512 + (d >> 1) * 64 + (d & 1) * 8);
          vf[d] = v4u{a[0], a[1], b[0], b[1]};
        }
        PIN8(vf);
        ctl_wr(ctl + 4 * (C_REL + aw), t + NATT < 64 ? uv + NATT : 128 * (ph + 1) + aw);
        __builtin_amdgcn_sched_barrier(0);
#pragma unroll
        for (int d = 0; d < 8; ++d) acc[d] = mfma(vf[d], pf, acc[d]);
        if (ph == 1 && t == OA_AT) {
          vm_wait<0>();
          const v2u tg = *(LAS v2u*)(L + L_CTRL + 4 * C_EP);
          if (lane == 0) __hip_atomic_store((GAS unsigned long long*)(ws + W_CF) + FSTR * j, ((unsigned long long)tg[1] << 32) | tg[0], __ATOMIC_RELAXED, __HIP_MEMORY_SCOPE_AGENT);
          if (c < 8) {
#pragma unroll
            for (int s = 0; s < 4; ++s) __builtin_amdgcn_raw_buffer_store_b128(oA[s], rsO, obase + s * 1024, 0, AUX_SC1);
          }
        }
      }
      if (aw != 0) {
        if (c < 8) {
#pragma unroll
          for (int d = 0; d < 8; ++d) *(LAS v4f*)(rec + (aw - 1) * 4096 + d * 512) = acc[d];
        }
        LDS_WAIT();
        ctl_wr(ctl + 4 * (C_MRG + 2 * ph + aw - 1), 1u);
      } else {
        ctl_wait<0>(ctl + 4 * (C_MRG + 2 * ph + 1), 1);
        CFENCE();
#pragma unroll
        for (int d = 0; d < 8; ++d) vf[d] = *(LAS v4u*)(rec + 4096 + d * 512);
#pragma unroll
        for (int s = 0; s < 4; ++s) {
          const v4f x0 = __builtin_elementwise_fma(__builtin_bit_cast(v4f, vf[2 * s]), v4f{f1, f1, f1, f1}, __builtin_elementwise_fma(acc[2 * s], v4f{l, l, l, l}, __builtin_bit_cast(v4f, kf[2 * s])));
          const v4f x1 = __builtin_elementwise_fma(__builtin_bit_cast(v4f, vf[2 * s + 1]), v4f{f1, f1, f1, f1}, __builtin_elementwise_fma(acc[2 * s + 1], v4f{l, l, l, l}, __builtin_bit_cast(v4f, kf[2 * s + 1])));
          v4u o = {pk2(x0[0], x0[1]), pk2(x0[2], x0[3]), pk2(x1[0], x1[1]), pk2(x1[2], x1[3])};
#pragma unroll
          for (int k = 0; k < 4; ++k) o[k] = min(o[k], SENT - 1u);
          if (ph == 0) oA[s] = o;
          else if (mine) __builtin_amdgcn_raw_buffer_store_b128(o, rsO, obase + s * 1024, 4 * 8 * 16384, AUX_SC1);
        }
      }
      m = -INFINITY; l = 0.f;
    }
  } else {
    const int gw = wave - 2 - (wave >> 2);
    {
      const __amdgpu_buffer_rsrc_t rsQ = __builtin_amdgcn_make_buffer_rsrc((void*)wq, 0, DM * NH * DH * 2, 0x00020000);
      const int wvo = (8 * g * (NH * DH) + 8 * c) * 2, wso = (128 * gw * (NH * DH) + h * DH) * 2;
      const int xrow = ((c < 8 ? bp : bp + 8) * SQ + (c & 7));
      const GAS char* xb = (const GAS char*)x + ((size_t)xrow * DM + 128 * gw + 8 * g) * 2;
#define WQ_ROW(t, jj) ((32 * (((t) + (bp >> 1)) & 3) + ((jj) ^ (4 * (bp & 1)))) * (NH * DH * 2))
      v4u xf[4], w[4][8];
      {
#pragma unroll
        for (int jj = 0; jj < 4; ++jj) w[0][jj] = __builtin_amdgcn_raw_buffer_load_b128(rsQ, wvo, wso + WQ_ROW(0, jj), 0);
        const unsigned pf = *(const GAS unsigned*)(xb + (hq & 1) * 128);
        __builtin_amdgcn_sched_barrier(0);
        __builtin_amdgcn_s_barrier();
        __builtin_amdgcn_sched_barrier(0);
        asm volatile("" ::"v"(pf));
        vm_wait<0>();
        if (grpB) { ctl_add(ctl + 4 * C_R1); ctl_wait<2>(ctl + 4 * C_FULL, QB_AT); }
        else __builtin_amdgcn_s_sleep(Q_GAP);
        __builtin_amdgcn_sched_barrier(0);
      }
#pragma unroll
      for (int jj = 4; jj < 8; ++jj) w[0][jj] = __builtin_amdgcn_raw_buffer_load_b128(rsQ, wvo, wso + WQ_ROW(0, jj), AUX_NT);
#pragma unroll
      for (int tt = 0; tt < 4; ++tt) xf[tt] = *(const GAS v4u*)(xb + ((tt + (bp >> 1)) & 3) * 64);
#pragma unroll
      for (int t = 1; t < 3; ++t)
#pragma unroll
        for (int jj = 0; jj < 8; ++jj) w[t][jj] = __builtin_amdgcn_raw_buffer_load_b128(rsQ, wvo, wso + WQ_ROW(t, jj), AUX_NT);
      __builtin_amdgcn_sched_barrier(0);
      ctl_add(ctl + 4 * C_QI);
      if (bp & 1) {
#pragma unroll
        for (int t = 0; t < 4; ++t) xf[t] = v4u{xf[t][2], xf[t][3], xf[t][0], xf[t][1]};
      }
      v4f qa[8];
#pragma unroll
      for (int d = 0; d < 8; ++d) qa[d] = v4f{0.f, 0.f, 0.f, 0.f};
#pragma unroll
      for (int t = 0; t < 4; ++t) {
#pragma unroll
        for (int dd = 0; dd < 8; ++dd) {
          v4u f;
#pragma unroll
          for (int v = 0; v < 4; ++v) f[v] = __builtin_amdgcn_perm(w[t][2 * v + 1][dd >> 1], w[t][2 * v][dd >> 1], (dd & 1) ? 0x07060302u : 0x05040100u);
          qa[dd] = mfma(f, xf[t], qa[dd]);
          if (dd & 1) __builtin_amdgcn_sched_barrier(0);
        }
        if (t == 0) {
#pragma unroll
          for (int jj = 0; jj < 8; ++jj) {
            w[3][jj] = __builtin_amdgcn_raw_buffer_load_b128(rsQ, wvo, wso + WQ_ROW(3, jj), AUX_NT);
            __builtin_amdgcn_sched_barrier(0);
          }
        }
      }
      LAS char* part = L + L_PS + (gw & 3) * 8192 + lane * 16;
      if (gw >= 4) {
        ctl_wait<0>(ctl + 4 * C_Q1, 4);
        CFENCE();
      }
#pragma unroll
      for (int i = 0; i < 4; ++i) {
        v4f lo = {qa[0][i], qa[1][i], qa[2][i], qa[3][i]}, hi = {qa[4][i], qa[5][i], qa[6][i], qa[7][i]};
        if (gw >= 4) { lo += *(LAS v4f*)(part + i * 2048); hi += *(LAS v4f*)(part + i * 2048 + 1024); }
        *(LAS v4f*)(part + i * 2048) = lo;
        *(LAS v4f*)(part + i * 2048 + 1024) = hi;
      }
      LDS_WAIT();
      ctl_add(ctl + 4 * (gw >= 4 ? C_Q2 : C_Q1));
    }
    const int xc = j & 7, idx = j >> 3, ct = (idx + CT_ROT) & 7, rg = idx >> 3;
    unsigned hb = 0;
#define HBAR(SLP) do { hb += NHELP; ctl_add(ctl + 4 * C_HB); ctl_wait<SLP>(ctl + 4 * C_HB, hb); CFENCE(); } while (0)
    const int pdst = W_P + (idx * 128 + gw * 8 + xc) * 1024;
    v4u wf[16];
    {
      const __amdgpu_buffer_rsrc_t rsW = __builtin_amdgcn_make_buffer_rsrc((void*)wo, 0, DM * NH * DH * 2, 0x00020000);
      ctl_wait<32>(ctl + 4 * C_FULL, WO_AT + 2 * ((gw + 2 * rg) & 7));
#pragma unroll
      for (int ks = 0; ks < 16; ++ks) {
        wf[ks] = __builtin_amdgcn_raw_buffer_load_b128(rsW, g * 2048 + c * 16, ((4 * xc + (ks >> 2)) * 128 + (ks & 3) * 32 + 4 * gw) * 2048 + ct * 256, 0);
        __builtin_amdgcn_s_sleep(WF_SLEEP);
        __builtin_amdgcn_sched_barrier(0);
      }
      LAS char* sw = L + L_STG + gw * PIECE + lane * 16;
      LAS char* sr = L + L_STG + 2 * g * PIECE + (c >> 2) * 256 + gw * 32 + (c & 3) * 8;
#pragma unroll
      for (int r = 0; r < 16; ++r) {
        *(LAS v4u*)(sw) = wf[r];
        LDS_WAIT();
        HBAR(2);
        const v2u a0 = trread(sr), b0 = trread(sr + PIECE);
        wf[r] = v4u{a0[0], a0[1], b0[0], b0[1]};
        asm volatile("" : "+v"(wf[r])::"memory");
        HBAR(2);
      }
    }
    const v2u tagv = *(LAS v2u*)(L + L_CTRL + 4 * C_EP);
    const unsigned long long tag = ((unsigned long long)tagv[1] << 32) | tagv[0];
    unsigned tb[4];
    tag_bytes(tagv, tb);
    ctl_wait<16>(ctl + 4 * C_FULL, NUNIT - 16);
    ctl_wait<2>(ctl + 4 * C_LDONE, 1);
    {
      const int src = (2 * rg + (lane & 1)) * 32 + (gw >> 1) * 8 + xc;
      GAS unsigned long long* fl = (GAS unsigned long long*)(ws + W_CF) + FSTR * src;
      for (unsigned sp = 0; sp < (1u << 16); ++sp) {
        if (__all(__hip_atomic_load(fl, __ATOMIC_RELAXED, __HIP_MEMORY_SCOPE_AGENT) == tag)) break;
        __builtin_amdgcn_s_sleep(G1_SLEEP);
      }
      __builtin_amdgcn_fence(__ATOMIC_ACQUIRE, "wavefront");
    }
#pragma unroll 1
    for (int ph = 0; ph < 2; ++ph) {
      v4u f0, f1;
      for (unsigned sp = 0; sp < (1u << 16); ++sp) {
        CFENCE();
        f0 = __builtin_amdgcn_raw_buffer_load_b128(rsO, lane * 16, W_O + ((ph * 4 + rg) * 8 + xc) * 16384 + gw * 2048, AUX_SC1);
        f1 = __builtin_amdgcn_raw_buffer_load_b128(rsO, lane * 16, W_O + ((ph * 4 + rg) * 8 + xc) * 16384 + gw * 2048 + 1024, AUX_SC1);
        const unsigned mxv = max(max(max(f0[0], f0[1]), max(f0[2], f0[3])), max(max(f1[0], f1[1]), max(f1[2], f1[3])));
        if (__all(mxv != SENT)) break;
        __builtin_amdgcn_s_sleep(G1_SLEEP);
      }
      *(LAS v4u*)(L + L_OB + gw * 2048 + lane * 16) = f0;
      *(LAS v4u*)(L + L_OB + gw * 2048 + 1024 + lane * 16) = f1;
      LDS_WAIT();
      HBAR(0);
      v4f oa[4];
#pragma unroll
      for (int k = 0; k < 4; ++k) oa[k] = v4f{0.f, 0.f, 0.f, 0.f};
#pragma unroll
      for (int f = 0; f < 16; ++f) oa[f & 3] = mfma(wf[f], *(LAS v4u*)(L + L_OB + f * 1024 + lane * 16), oa[f & 3]);
      v4u pt = __builtin_bit_cast(v4u, (oa[0] + oa[1]) + (oa[2] + oa[3]));
#pragma unroll
      for (int k = 0; k < 4; ++k) pt[k] = ((pt[k] + ((pt[k] & 0x7F800000u) == 0x7F800000u ? 0u : 0x80u)) & ~0xFFu) | tb[k];
      __builtin_amdgcn_raw_buffer_store_b128(pt, rsO, lane * 16, pdst + ph * 65536, AUX_SC1);
      if (ph == 0) HBAR(0);
    }
    if (gw < 2) {
      GAS v2u* dst = (GAS v2u*)((GAS char*)out + ((size_t)(64 * gw + 16 * rg + c) * DM + 128 * ct + 16 * xc + 4 * g) * 2);
      asm volatile("" : "+v"(dst));
      const v4u tv4 = {tb[0], tb[1], tb[2], tb[3]};
      v2u res;
      for (unsigned sp = 0; sp < (1u << 16); ++sp) {
        CFENCE();
        v4u q[8];
#pragma unroll
        for (int k = 0; k < 8; ++k) q[k] = __builtin_amdgcn_raw_buffer_load_b128(rsO, lane * 16, W_P + ((idx * 2 + gw) * 64 + xc * 8 + k) * 1024, AUX_SC1);
        v4u bad = q[0] ^ tv4;
        v4f s = __builtin_bit_cast(v4f, bad);
#pragma unroll
        for (int k = 1; k < 8; ++k) { const v4u y = q[k] ^ tv4; bad |= y; s += __builtin_bit_cast(v4f, y); }
        res = v2u{pk2(s[0], s[1]), pk2(s[2], s[3])};
        asm volatile("" : "+v"(res));
        if (__all((((bad[0] | bad[1]) | (bad[2] | bad[3])) & 0xFFu) == 0u)) break;
        __builtin_amdgcn_s_sleep(X2_SLEEP);
      }
      *dst = res;
      if (gw == 0 && (j & 63) == 0 && lane == 0) __hip_atomic_store((GAS unsigned long long*)(ws + W_EPOCH) + FSTR * rg, tag, __ATOMIC_RELAXED, __HIP_MEMORY_SCOPE_AGENT);
    }
  }
}
}

extern "C" void kernel_launch(void* const* d_in, const int* in_sizes, int n_in, void* d_out, int out_size, void* d_ws, size_t ws_size,
                              hipStream_t stream) {
  (void)in_sizes; (void)n_in; (void)out_size; (void)ws_size;
  (void)hipFuncSetAttribute((const void*)mha_block_kernel, hipFuncAttributeMaxDynamicSharedMemorySize, LDS_BYTES);
  mha_block_kernel<<<NWG, NTHR, LDS_BYTES, stream>>>((const unsigned short*)d_in[0], (const unsigned short*)d_in[1], (const unsigned short*)d_in[2],
                                                     (const unsigned short*)d_in[3], (const unsigned short*)d_in[4], (unsigned short*)d_out,
                                                     (unsigned char*)d_ws);
}
```

```cpp
#include <hip/hip_runtime.h>
#include <hip/hip_bf16.h>
#include <stdint.h>

#define GAS __attribute__((address_space(1)))
#define LAS __attribute__((address_space(3)))
typedef unsigned v4u __attribute__((ext_vector_type(4)));
typedef unsigned v2u __attribute__((ext_vector_type(2)));
typedef float v4f __attribute__((ext_vector_type(4)));
typedef float v2f __attribute__((ext_vector_type(2)));
typedef short v4s __attribute__((ext_vector_type(4)));
typedef __bf16 v8bf __attribute__((ext_vector_type(8)));
typedef __bf16 v2bf __attribute__((ext_vector_type(2)));

namespace {
constexpr int NB = 16, SQ = 8, DM = 1024, NH = 32, SK = 2048, DH = 128;
constexpr int NWG = 256, NTHR = 768, NATT = 3, NHELP = 8;
constexpr int PIECE = 1056;
constexpr int UNIT = 8 * PIECE;
constexpr int NR = 14;
constexpr int NUNIT = 256;
constexpr int AUX_NT = 2, AUX_SC1 = 16;
constexpr int WIN = 20;
constexpr int WINA = 32, WIN0 = 56, U0 = 32;
constexpr int WINE = 32, UE = 16;
constexpr int WINB = 28;
constexpr int CK_U0 = 32, CK_T0 = 1170, CK_U1 = 108, CK_T1 = 3395, CK_U2 = 200, CK_T2 = 5895;
constexpr int CK_G = 160;
constexpr int UI = 2, QI_GO = 6, Q_GAP = 4;
constexpr int NPRE = 3, QB_AT = 15;
constexpr int WO_AT = 68, WF_SLEEP = 8;
constexpr int OA_AT = 57;
constexpr int G1_SLEEP = 1, X2_SLEEP = 1, CT_ROT = 2;
constexpr int L_CTRL = NR * UNIT;
constexpr int PREC = 544;
constexpr int L_PS = L_CTRL + 256;
constexpr int L_OB = L_PS;
constexpr int L_MRG = L_PS + 32 * PREC;
constexpr int L_ST = L_PS + 64 * PREC;
constexpr int L_STG = L_ST + 1024;
constexpr int LDS_BYTES = 160 * 1024;
static_assert(L_STG + 8 * PIECE <= LDS_BYTES && 16384 <= 32 * PREC && 32 * PREC + 8192 <= 48 * PREC && NR <= 14, "LDS map");
enum { C_FULL = 0, C_REL = 4, C_Q1 = 8, C_Q2 = 9, C_QI = 10, C_HB = 11, C_QR = 12, C_EP = 14, C_ML = 18, C_MRG = 20, C_LDONE = 24, C_R1 = 25 };
constexpr int W_EPOCH = 0, W_CF = 65536, FSTR = 16;
constexpr int W_O = 1 << 20;
constexpr int W_P = 2 << 20;
constexpr int WS_USED = 6 << 20;
constexpr unsigned SENT = 0xFFFFFFFFu;
constexpr float CSC = 0.08838834764831845f * 1.4426950408889634f;
constexpr unsigned SPIN_CAP = 1u << 22;

__device__ __forceinline__ unsigned rfl(unsigned x) { return __builtin_amdgcn_readfirstlane(x); }
__device__ __forceinline__ v4f mfma(v4u a, v4u b, v4f c) {
  return __builtin_amdgcn_mfma_f32_16x16x32_bf16(__builtin_bit_cast(v8bf, a), __builtin_bit_cast(v8bf, b), c, 0, 0, 0);
}
__device__ __forceinline__ unsigned pk2(float lo, float hi) {
  v2f x = {lo, hi};
  return __builtin_bit_cast(unsigned, __builtin_convertvector(x, v2bf));
}
__device__ __forceinline__ v2u trread(LAS char* p) {
  return __builtin_bit_cast(v2u, __builtin_amdgcn_ds_read_tr16_b64_v4i16((LAS v4s*)p));
}
__device__ __forceinline__ float xg_max(float x) {
  auto s = __builtin_amdgcn_permlane16_swap(__float_as_uint(x), __float_as_uint(x), false, false);
  x = fmaxf(__uint_as_float(s[0]), __uint_as_float(s[1]));
  auto t = __builtin_amdgcn_permlane32_swap(__float_as_uint(x), __float_as_uint(x), false, false);
  return fmaxf(__uint_as_float(t[0]), __uint_as_float(t[1]));
}
__device__ __forceinline__ float xg_sum(float x) {
  auto s = __builtin_amdgcn_permlane16_swap(__float_as_uint(x), __float_as_uint(x), false, false);
  x = __uint_as_float(s[0]) + __uint_as_float(s[1]);
  auto t = __builtin_amdgcn_permlane32_swap(__float_as_uint(x), __float_as_uint(x), false, false);
  return __uint_as_float(t[0]) + __uint_as_float(t[1]);
}
#define LDS_WAIT() asm volatile("s_waitcnt lgkmcnt(0)" ::: "memory")
#define CFENCE() asm volatile("" ::: "memory")
#define PIN8(a) asm volatile("" : "+v"(a[0]), "+v"(a[1]), "+v"(a[2]), "+v"(a[3]), "+v"(a[4]), "+v"(a[5]), "+v"(a[6]), "+v"(a[7])::"memory")
template <int N> __device__ __forceinline__ void vm_wait() { asm volatile("s_waitcnt vmcnt(%c0)" ::"n"(N) : "memory"); }
__device__ __forceinline__ unsigned ctl_rd(unsigned addr) {
  unsigned v;
  asm volatile("ds_read_b32 %0, %1\n\ts_waitcnt lgkmcnt(0)" : "=v"(v) : "v"(addr) : "memory");
  return rfl(v);
}
__device__ __forceinline__ void ctl_wr(unsigned addr, unsigned val) {
  asm volatile("s_mov_b64 exec, 1\n\tds_write_b32 %0, %1\n\ts_mov_b64 exec, -1" ::"v"(addr), "v"(val) : "memory");
}
__device__ __forceinline__ unsigned ctl_add(unsigned addr) {
  unsigned v;
  asm volatile("s_mov_b64 exec, 1\n\tds_add_rtn_u32 %0, %1, %2\n\ts_mov_b64 exec, -1\n\ts_waitcnt lgkmcnt(0)" : "=&v"(v) : "v"(addr), "v"(1u) : "memory");
  return rfl(v);
}
template <int SLP> __device__ __forceinline__ unsigned ctl_wait(unsigned addr, unsigned need) {
  unsigned v = ctl_rd(addr);
  for (unsigned sp = 0; v < need && sp < SPIN_CAP; ++sp) {
    __builtin_amdgcn_s_sleep(SLP);
    v = ctl_rd(addr);
  }
  return v;
}

__device__ __forceinline__ void tag_bytes(v2u tagv, unsigned (&tb)[4]) {
  const unsigned long long e = (((unsigned long long)tagv[1] << 32) | tagv[0]) - 1ull;
  const unsigned hs = (unsigned)(e >> 8) * 0x9E3779B1u ^ (unsigned)(e >> 40) * 0x85EBCA77u;
#pragma unroll
  for (int k = 0; k < 4; ++k) tb[k] = rfl((tagv[0] + ((hs >> (8 * k)) & 0xFFu) % 254u) & 0xFFu);
}

#define PUBL(k) ".if %c[b] == " #k "\n\ts_mov_b64 exec, 1\n\tds_write_b32 %[fa], v23\n\ts_mov_b64 exec, -1\n\t.endif\n\t"
#define PC(k, VO, IMM) "s_waitcnt vmcnt(%c[w])\n\t" PUBL(k) "buffer_load_dwordx4 " VO ", %[rs], %[so] offen offset:" IMM " nt lds\n\t"
template <int W>
__device__ __forceinline__ void dma_units(__amdgpu_buffer_rsrc_t rs, unsigned soff, unsigned uend, unsigned& m0b, unsigned& u, unsigned& lim, unsigned vo0,
                                          unsigned vo1, unsigned vctl, unsigned faddr) {
  unsigned sp;
  asm volatile(
      "0:\n\t"
      "s_cmp_lt_u32 %[u], %[lim]\n\t"
      "s_cbranch_scc1 2f\n\t"
      "s_mov_b32 %[sp], 0\n"
      "1:\n\t"
      "ds_read_b32 v20, %[vc] offset:16\n\tds_read_b32 v21, %[vc] offset:20\n\tds_read_b32 v22, %[vc] offset:24\n\t"
      "s_waitcnt lgkmcnt(0)\n\t"
      "v_min3_u32 v20, v20, v21, v22\n\t"
      "s_add_u32 %[sp], %[sp], 1\n\t"
      "v_readfirstlane_b32 %[lim], v20\n\t"
      "s_add_u32 %[lim], %[lim], %c[nr]\n\t"
      "s_cmp_lt_u32 %[u], %[lim]\n\t"
      "s_cbranch_scc1 2f\n\t"
      "s_sleep 1\n\t"
      "s_cmp_lt_u32 %[sp], %c[cap]\n\t"
      "s_cbranch_scc1 1b\n"
      "2:\n\t"
      "s_mov_b32 m0, %[m0b]\n\t"
      "v_mov_b32 v23, %[u]\n\t"
      "v_subrev_u32 v23, %c[a], v23\n\t"
      PC(0, "%[vo0]", "0")
      "ds_read_b32 v20, %[vc] offset:16\n\tds_read_b32 v21, %[vc] offset:20\n\tds_read_b32 v22, %[vc] offset:24\n\t"
      "s_add_u32 m0, m0, 32\n\t"
      PC(1, "%[vo0]", "1024")
      "s_add_u32 m0, m0, 32\n\t"
      PC(2, "%[vo0]", "2048")
      "s_add_u32 m0, m0, 32\n\t"
      PC(3, "%[vo0]", "3072")
      "s_add_u32 m0, m0, 0x1020\n\t"
      PC(4, "%[vo1]", "0")
      "s_waitcnt lgkmcnt(0)\n\t"
      "v_min3_u32 v20, v20, v21, v22\n\t"
      "s_add_u32 m0, m0, 32\n\t"
      PC(5, "%[vo1]", "1024")
      "v_readfirstlane_b32 %[lim], v20\n\t"
      "s_add_u32 %[m0b], %[m0b], %c[unit]\n\t"
      "s_add_u32 m0, m0, 32\n\t"
      PC(6, "%[vo1]", "2048")
      "s_add_u32 %[lim], %[lim], %c[nr]\n\t"
      "s_cmp_lt_u32 %[m0b], %c[ring]\n\t"
      "s_cselect_b32 %[m0b], %[m0b], 0\n\t"
      "s_add_u32 %[u], %[u], 1\n\t"
      "s_add_u32 m0, m0, 32\n\t"
      PC(7, "%[vo1]", "3072")
      "s_add_u32 %[so], %[so], 0x2000\n\t"
      "s_cmp_lt_u32 %[u], %[ue]\n\t"
      "s_cbranch_scc1 0b"
      : [m0b] "+&s"(m0b), [u] "+&s"(u), [lim] "+&s"(lim), [so] "+&s"(soff), [sp] "=&s"(sp)
      : [ue] "s"(uend), [w] "n"(W - 1), [a] "n"((W - 1) / 8), [b] "n"((W - 1) % 8), [nr] "n"(NR), [cap] "n"(SPIN_CAP), [unit] "n"(UNIT), [ring] "n"(NR * UNIT), [rs] "s"(rs),
        [vo0] "v"(vo0), [vo1] "v"(vo1), [vc] "v"(vctl), [fa] "v"(faddr)
      : "memory", "scc", "v20", "v21", "v22", "v23");
}

__global__ void __launch_bounds__(NTHR, 3)
mha_block_kernel(const unsigned short* __restrict__ x, const unsigned short* __restrict__ kc, const unsigned short* __restrict__ vc,
                 const unsigned short* __restrict__ wq, const unsigned short* __restrict__ wo, unsigned short* __restrict__ out,
                 unsigned char* ws) {
  extern __shared__ __attribute__((aligned(16))) char smem[];
  LAS char* L = (LAS char*)smem;
  const unsigned lbase = (unsigned)(size_t)L;
  const int lane = threadIdx.x & 63, wave = rfl(threadIdx.x >> 6);
  const int g = lane >> 4, c = lane & 15;
  const int j = blockIdx.x, hq = (j >> 3) & 3, h = 4 * (j & 7) + hq, bp = j >> 5;
  const unsigned ctl = lbase + L_CTRL;
  const bool grpB = hq == 3;
  const __amdgpu_buffer_rsrc_t rsO = __builtin_amdgcn_make_buffer_rsrc((void*)ws, 0, WS_USED, 0x00020000);

  if (wave == 0) {
    __builtin_amdgcn_s_setprio(3);
    __builtin_amdgcn_s_waitcnt(0x0F70);
    const unsigned long long tl0 = __builtin_amdgcn_s_memrealtime();
    const unsigned vo0 = lane * 16, vo1 = lane * 16 + 4096;
    const unsigned sbase = (unsigned)(bp * NH + h) * (SK * DH * 2);
    unsigned m0b = 0, u = 0, lim = NR;
    unsigned ub = 0;
    static_assert(QB_AT <= NR + NPRE * NATT - 1 - (WIN0 - 1) / 8 && NPRE * NATT <= NR - 1 - (WIN0 - 1) / 8 + 2 * NATT, "published before the ring is full");
    static_assert(UI >= 2 && UI + (WIN0 - 1) / 8 < U0 && U0 <= CK_U0 && CK_U0 < CK_U1 && CK_U1 < CK_U2 && CK_U2 < NUNIT - UE, "segment order");
#pragma unroll 1
    while (u < NUNIT) {
      const __amdgpu_buffer_rsrc_t rs = __builtin_amdgcn_make_buffer_rsrc((void*)((u & 64) ? vc : kc), 0, NB * NH * SK * DH * 2, 0x00020000);
      const unsigned soff = sbase + (u >> 7) * (8u * NH * SK * DH * 2) + (u & 63) * 8192u;
      unsigned ue = (u | 63) + 1;
      if (u < U0) {
        if (u == 2) __builtin_amdgcn_s_barrier();
        if (u == UI) {
          vm_wait<0>(); ctl_wr(ctl + 4 * C_FULL, UI);
          if (grpB) ctl_wait<1>(ctl + 4 * C_R1, NHELP); else ctl_wait<1>(ctl + 4 * C_QI, QI_GO);
        }
        const bool early = u < UI + (WIN0 - 1) / 8;
        if (!grpB && u >= UI) dma_units<WINA>(rs, soff, u < UI + (WINA - 1) / 8 ? UI + (WINA - 1) / 8 : U0, m0b, u, lim, vo0, vo1, ctl, u < UI + (WINA - 1) / 8 ? ctl + 252 : ctl + 4 * C_FULL);
        else dma_units<WIN0>(rs, soff, u < UI ? u + 1 : (early ? UI + (WIN0 - 1) / 8 : U0), m0b, u, lim, vo0, vo1, ctl, early ? ctl + 252 : ctl + 4 * C_FULL);
        if (u <= 2) lim = NR;
      } else if (u >= NUNIT - UE) {
        dma_units<WINE>(rs, soff, ue, m0b, u, lim, vo0, vo1, ctl, ctl + 4 * C_FULL);
      } else {
        if (u == CK_U0 || u == CK_U1 || u == CK_U2) {
          const int late = (int)(unsigned)(__builtin_amdgcn_s_memrealtime() - tl0) - (u == CK_U0 ? CK_T0 : (u == CK_U1 ? CK_T1 : CK_T2));
          ub = u + (late > 0 ? min((late * CK_G) >> 10, NUNIT) : 0);
        }
        ue = min(ue, u < CK_U0 ? (unsigned)CK_U0 : (u < CK_U1 ? (unsigned)CK_U1 : (u < CK_U2 ? (unsigned)CK_U2 : (unsigned)(NUNIT - UE))));
        if (u < ub) dma_units<WINB>(rs, soff, min(ue, ub), m0b, u, lim, vo0, vo1, ctl, ctl + 4 * C_FULL);
        else dma_units<WIN>(rs, soff, ue, m0b, u, lim, vo0, vo1, ctl, ctl + 4 * C_FULL);
      }
    }
    ctl_wr(ctl + 4 * C_LDONE, 1u);
    if (WINE > 48) { vm_wait<48>(); ctl_wr(ctl + 4 * C_FULL, NUNIT - 6); }
    if (WINE > 40) { vm_wait<40>(); ctl_wr(ctl + 4 * C_FULL, NUNIT - 5); }
    if (WINE > 32) { vm_wait<32>(); ctl_wr(ctl + 4 * C_FULL, NUNIT - 4); }
    if (WINE > 24) { vm_wait<24>(); ctl_wr(ctl + 4 * C_FULL, NUNIT - 3); }
    vm_wait<16>(); ctl_wr(ctl + 4 * C_FULL, NUNIT - 2);
    vm_wait<8>(); ctl_wr(ctl + 4 * C_FULL, NUNIT - 1);
    vm_wait<0>(); ctl_wr(ctl + 4 * C_FULL, NUNIT);
  } else if ((wave & 3) == 0 || wave == 1) {
    const int aw = wave == 1 ? 2 : (wave >> 2) - 1;
    if (aw == 0) { *(LAS unsigned*)(L + L_CTRL + 4 * lane) = 0u; LDS_WAIT(); }
    __builtin_amdgcn_s_barrier();
    const unsigned koff = (c & 7) * PIECE + (c >> 3) * 256 + g * 16;
    const unsigned voff = (4 * (g & 1) + (c >> 2)) * PIECE + (g >> 1) * 256 + (c & 3) * 16;
    const int obase = W_O + ((bp >> 1) * 8 + (j & 7)) * 16384 + hq * 4096 + g * 256 + ((bp & 1) * 8 + (c & 7)) * 16;
    {
      const v4u sv = {SENT, SENT, SENT, SENT};
      if (aw == 0) {
        const unsigned long long tag = __hip_atomic_load((GAS unsigned long long*)(ws + W_EPOCH) + FSTR * (j >> 6), __ATOMIC_RELAXED, __HIP_MEMORY_SCOPE_AGENT) + 1ull;
#pragma unroll
        for (int s = 0; s < 4; ++s) __builtin_amdgcn_raw_buffer_store_b128(sv, rsO, obase + (c >> 3) * (4 * 8 * 16384), s * 1024, AUX_SC1);
        if (lane == 0) *(LAS v2u*)(L + L_CTRL + 4 * C_EP) = v2u{(unsigned)tag, (unsigned)(tag >> 32)};
      }
    }
    v4u kf[8], vf[8];
    unsigned full = ctl_wait<1>(ctl + 4 * C_FULL, aw + 1);
    CFENCE();
#pragma unroll
    for (int i = 0; i < 8; ++i) kf[i] = *(LAS v4u*)(L + aw * UNIT + koff + (i >> 2) * 512 + (i & 3) * 64);
    PIN8(kf);
    ctl_wr(ctl + 4 * (C_REL + aw), aw + NATT);
    full = ctl_wait<1>(ctl + 4 * C_FULL, aw + NATT + 1);
    CFENCE();
#pragma unroll
    for (int i = 0; i < 8; ++i) vf[i] = *(LAS v4u*)(L + (aw + NATT) * UNIT + koff + (i >> 2) * 512 + (i & 3) * 64);
    PIN8(vf);
    ctl_wr(ctl + 4 * (C_REL + aw), aw + 2 * NATT);
    v4u k3[8];
    full = ctl_wait<1>(ctl + 4 * C_FULL, aw + 2 * NATT + 1);
    CFENCE();
#pragma unroll
    for (int i = 0; i < 8; ++i) k3[i] = *(LAS v4u*)(L + (aw + 2 * NATT) * UNIT + koff + (i >> 2) * 512 + (i & 3) * 64);
    PIN8(k3);
    ctl_wr(ctl + 4 * (C_REL + aw), aw + 3 * NATT);
    ctl_wait<1>(ctl + 4 * C_Q2, 4);
    CFENCE();
    v4u qf[4];
#pragma unroll
    for (int s = 0; s < 4; ++s) {
      LAS char* qp = L + L_PS + g * 2048 + (s * 16 + c) * 16;
      const v4f lo = (*(LAS v4f*)(qp) + *(LAS v4f*)(qp + 8192)) + (*(LAS v4f*)(qp + 16384) + *(LAS v4f*)(qp + 24576));
      const v4f hi = (*(LAS v4f*)(qp + 1024) + *(LAS v4f*)(qp + 9216)) + (*(LAS v4f*)(qp + 17408) + *(LAS v4f*)(qp + 25600));
      qf[s] = v4u{pk2(lo[0], lo[1]), pk2(lo[2], lo[3]), pk2(hi[0], hi[1]), pk2(hi[2], hi[3])};
      asm volatile("" : "+v"(qf[s])::"memory");
    }
    ctl_add(ctl + 4 * C_QR);
    const unsigned poff = L_PS + (g * 8 + (c & 7)) * 16, aoff = L_PS + 512 + (c & 7) * 4;
    float m = -INFINITY, l = 0.f;
    auto ktile = [&](int t, bool mine, bool first) {
      v4f s0 = {0.f, 0.f, 0.f, 0.f}, s1 = {0.f, 0.f, 0.f, 0.f};
#pragma unroll
      for (int s = 0; s < 4; ++s) { s0 = mfma(kf[s], qf[s], s0); s1 = mfma(kf[4 + s], qf[s], s1); }
      float mx = fmaxf(fmaxf(fmaxf(s0[0], s0[1]), fmaxf(s0[2], s0[3])), fmaxf(fmaxf(s1[0], s1[1]), fmaxf(s1[2], s1[3])));
      mx = xg_max(mx);
      const float mn = fmaxf(m, mx);
      const float alpha = __builtin_amdgcn_exp2f((m - mn) * CSC);
      const float mc = mn * CSC;
      m = mn;
      float p[8];
#pragma unroll
      for (int i = 0; i < 4; ++i) { p[i] = __builtin_amdgcn_exp2f(fmaf(s0[i], CSC, -mc)); p[4 + i] = __builtin_amdgcn_exp2f(fmaf(s1[i], CSC, -mc)); }
      l = fmaf(l, alpha, ((p[0] + p[1]) + (p[2] + p[3])) + ((p[4] + p[5]) + (p[6] + p[7])));
      if (first) ctl_wait<0>(ctl + 4 * C_QR, NATT);
      if (mine) {
        *(LAS v4u*)(L + poff + t * PREC) = v4u{pk2(p[0], p[1]), pk2(p[2], p[3]), pk2(p[4], p[5]), pk2(p[6], p[7])};
        if (g == 0) *(LAS float*)(L + aoff + t * PREC) = alpha;
      }
    };
#pragma unroll 1
    for (int i = 0; i < NPRE; ++i) {
      ktile(aw + NATT * i, c < 8, i == 0);
#pragma unroll
      for (int k = 0; k < 8; ++k) { kf[k] = vf[k]; vf[k] = k3[k]; }
    }
    v4u oA[4];
#pragma unroll
    for (int s = 0; s < 4; ++s) oA[s] = v4u{0u, 0u, 0u, 0u};
#pragma unroll 1
    for (int ph = 0; ph < 2; ++ph) {
      const bool mine = (c >> 3) == ph;
#pragma unroll 1
      for (int t = ph ? aw : aw + NPRE * NATT; t < 64; t += NATT) {
        const unsigned uk = 128 * ph + t;
        if (full < uk + 1) full = ctl_wait<0>(ctl + 4 * C_FULL, uk + 1);
        CFENCE();
        LAS char* kb = L + (uk % NR) * UNIT + koff;
#pragma unroll
        for (int i = 0; i < 8; ++i) kf[i] = *(LAS v4u*)(kb + (i >> 2) * 512 + (i & 3) * 64);
        PIN8(kf);
        ctl_wr(ctl + 4 * (C_REL + aw), t + NATT < 64 ? uk + NATT : 128 * ph + 64 + aw);
        __builtin_amdgcn_sched_barrier(0);
        ktile(t, mine, false);
      }
      float f0 = 0.f, f1 = 0.f;
      if (aw == 0) {
        ctl_wait<0>(ctl + 4 * (C_ML + ph), 2);
        CFENCE();
        const v2f a0 = *(LAS v2f*)(L + L_ST + lane * 8), a1 = *(LAS v2f*)(L + L_ST + 512 + lane * 8);
        const float M = fmaxf(m, fmaxf(a0[0], a1[0]));
        const float w = __builtin_amdgcn_exp2f((m - M) * CSC), w0 = __builtin_amdgcn_exp2f((a0[0] - M) * CSC), w1 = __builtin_amdgcn_exp2f((a1[0] - M) * CSC);
        const float inv = 1.0f / xg_sum(fmaf(w, l, fmaf(w0, a0[1], w1 * a1[1])));
        l = w * inv; f0 = w0 * inv; f1 = w1 * inv;
      } else {
        if (mine) *(LAS v2f*)(L + L_ST + (aw - 1) * 512 + lane * 8) = v2f{m, l};
        LDS_WAIT();
        ctl_add(ctl + 4 * (C_ML + ph));
      }
      v4f acc[8];
#pragma unroll
      for (int d = 0; d < 8; ++d) acc[d] = v4f{0.f, 0.f, 0.f, 0.f};
      LAS char* rec = L + L_MRG + (g * 8 + (c & 7)) * 16;
#pragma unroll 1
      for (int t = aw; t < 64; t += NATT) {
        const unsigned uv = 128 * ph + 64 + t;
        v4u pf = *(LAS v4u*)(L + poff + t * PREC);
        const float alpha = *(LAS float*)(L + aoff + t * PREC);
#pragma unroll
        for (int d = 0; d < 8; ++d) acc[d] *= alpha;
        if (t == 63) {
          ctl_wait<0>(ctl + 4 * (C_MRG + 2 * ph), 1);
          CFENCE();
#pragma unroll
          for (int d = 0; d < 8; ++d) kf[d] = __builtin_bit_cast(v4u, *(LAS v4f*)(rec + d * 512) * f0);
          PIN8(kf);
        }
        asm volatile("" : "+v"(pf));
        PIN8(acc);
        if (full < uv + 1) full = ctl_wait<0>(ctl + 4 * C_FULL, uv + 1);
        CFENCE();
        LAS char* vb = L + (uv % NR) * UNIT + voff;
#pragma unroll
        for (int d = 0; d < 8; ++d) {
          const v2u a = trread(vb + (d >> 1) * 64 + (d & 1) * 8), b = trread(vb + 512 + (d >> 1) * 64 + (d & 1) * 8);
          vf[d] = v4u{a[0], a[1], b[0], b[1]};
        }
        PIN8(vf);
        ctl_wr(ctl + 4 * (C_REL + aw), t + NATT < 64 ? uv + NATT : 128 * (ph + 1) + aw);
        __builtin_amdgcn_sched_barrier(0);
#pragma unroll
        for (int d = 0; d < 8; ++d) acc[d] = mfma(vf[d], pf, acc[d]);
        if (ph == 1 && t == OA_AT) {
          vm_wait<0>();
          const v2u tg = *(LAS v2u*)(L + L_CTRL + 4 * C_EP);
          if (lane == 0) __hip_atomic_store((GAS unsigned long long*)(ws + W_CF) + FSTR * j, ((unsigned long long)tg[1] << 32) | tg[0], __ATOMIC_RELAXED, __HIP_MEMORY_SCOPE_AGENT);
          if (c < 8) {
#pragma unroll
            for (int s = 0; s < 4; ++s) __builtin_amdgcn_raw_buffer_store_b128(oA[s], rsO, obase + s * 1024, 0, AUX_SC1);
          }
        }
      }
      if (aw != 0) {
        if (c < 8) {
#pragma unroll
          for (int d = 0; d < 8; ++d) *(LAS v4f*)(rec + (aw - 1) * 4096 + d * 512) = acc[d];
        }
        LDS_WAIT();
        ctl_wr(ctl + 4 * (C_MRG + 2 * ph + aw - 1), 1u);
      } else {
        ctl_wait<0>(ctl + 4 * (C_MRG + 2 * ph + 1), 1);
        CFENCE();
#pragma unroll
        for (int d = 0; d < 8; ++d) vf[d] = *(LAS v4u*)(rec + 4096 + d * 512);
#pragma unroll
        for (int s = 0; s < 4; ++s) {
          const v4f x0 = __builtin_elementwise_fma(__builtin_bit_cast(v4f, vf[2 * s]), v4f{f1, f1, f1, f1}, __builtin_elementwise_fma(acc[2 * s], v4f{l, l, l, l}, __builtin_bit_cast(v4f, kf[2 * s])));
          const v4f x1 = __builtin_elementwise_fma(__builtin_bit_cast(v4f, vf[2 * s + 1]), v4f{f1, f1, f1, f1}, __builtin_elementwise_fma(acc[2 * s + 1], v4f{l, l, l, l}, __builtin_bit_cast(v4f, kf[2 * s + 1])));
          v4u o = {pk2(x0[0], x0[1]), pk2(x0[2], x0[3]), pk2(x1[0], x1[1]), pk2(x1[2], x1[3])};
#pragma unroll
          for (int k = 0; k < 4; ++k) o[k] = min(o[k], SENT - 1u);
          oA[s] = o;
        }
        if (ph == 1 && mine) {
#pragma unroll
          for (int s = 0; s < 4; ++s) __builtin_amdgcn_raw_buffer_store_b128(oA[s], rsO, obase + s * 1024, 4 * 8 * 16384, AUX_SC1);
        }
      }
      m = -INFINITY; l = 0.f;
    }
  } else {
    const int gw = wave - 2 - (wave >> 2);
    {
      const __amdgpu_buffer_rsrc_t rsQ = __builtin_amdgcn_make_buffer_rsrc((void*)wq, 0, DM * NH * DH * 2, 0x00020000);
      const int wvo = (8 * g * (NH * DH) + 8 * c) * 2, wso = (128 * gw * (NH * DH) + h * DH) * 2;
      const int xrow = ((c < 8 ? bp : bp + 8) * SQ + (c & 7));
      const GAS char* xb = (const GAS char*)x + ((size_t)xrow * DM + 128 * gw + 8 * g) * 2;
#define WQ_ROW(t, jj) ((32 * (((t) + (bp >> 1)) & 3) + ((jj) ^ (4 * (bp & 1)))) * (NH * DH * 2))
      v4u xf[4], w[4][8];
      {
#pragma unroll
        for (int jj = 0; jj < 4; ++jj) w[0][jj] = __builtin_amdgcn_raw_buffer_load_b128(rsQ, wvo, wso + WQ_ROW(0, jj), 0);
        const unsigned pf = *(const GAS unsigned*)(xb + (hq & 1) * 128);
        __builtin_amdgcn_sched_barrier(0);
        __builtin_amdgcn_s_barrier();
        __builtin_amdgcn_sched_barrier(0);
        asm volatile("" ::"v"(pf));
        vm_wait<0>();
        if (grpB) { ctl_add(ctl + 4 * C_R1); ctl_wait<2>(ctl + 4 * C_FULL, QB_AT); }
        else __builtin_amdgcn_s_sleep(Q_GAP);
        __builtin_amdgcn_sched_barrier(0);
      }
#pragma unroll
      for (int jj = 4; jj < 8; ++jj) w[0][jj] = __builtin_amdgcn_raw_buffer_load_b128(rsQ, wvo, wso + WQ_ROW(0, jj), AUX_NT);
#pragma unroll
      for (int tt = 0; tt < 4; ++tt) xf[tt] = *(const GAS v4u*)(xb + ((tt + (bp >> 1)) & 3) * 64);
#pragma unroll
      for (int t = 1; t < 3; ++t)
#pragma unroll
        for (int jj = 0; jj < 8; ++jj) w[t][jj] = __builtin_amdgcn_raw_buffer_load_b128(rsQ, wvo, wso + WQ_ROW(t, jj), AUX_NT);
      __builtin_amdgcn_sched_barrier(0);
      ctl_add(ctl + 4 * C_QI);
      if (bp & 1) {
#pragma unroll
        for (int t = 0; t < 4; ++t) xf[t] = v4u{xf[t][2], xf[t][3], xf[t][0], xf[t][1]};
      }
      v4f qa[8];
#pragma unroll
      for (int d = 0; d < 8; ++d) qa[d] = v4f{0.f, 0.f, 0.f, 0.f};
#pragma unroll
      for (int t = 0; t < 4; ++t) {
#pragma unroll
        for (int dd = 0; dd < 8; ++dd) {
          v4u f;
#pragma unroll
          for (int v = 0; v < 4; ++v) f[v] = __builtin_amdgcn_perm(w[t][2 * v + 1][dd >> 1], w[t][2 * v][dd >> 1], (dd & 1) ? 0x07060302u : 0x05040100u);
          qa[dd] = mfma(f, xf[t], qa[dd]);
          if (dd & 1) __builtin_amdgcn_sched_barrier(0);
        }
        if (t == 0) {
#pragma unroll
          for (int jj = 0; jj < 8; ++jj) {
            w[3][jj] = __builtin_amdgcn_raw_buffer_load_b128(rsQ, wvo, wso + WQ_ROW(3, jj), AUX_NT);
            __builtin_amdgcn_sched_barrier(0);
          }
        }
      }
      LAS char* part = L + L_PS + (gw & 3) * 8192 + lane * 16;
      v4f qs[8];
#pragma unroll
      for (int i = 0; i < 4; ++i) { qs[2 * i] = v4f{qa[0][i], qa[1][i], qa[2][i], qa[3][i]}; qs[2 * i + 1] = v4f{qa[4][i], qa[5][i], qa[6][i], qa[7][i]}; }
      if (gw >= 4) {
        ctl_wait<0>(ctl + 4 * C_Q1, 4);
        CFENCE();
        v4f pr[8];
#pragma unroll
        for (int i = 0; i < 8; ++i) pr[i] = *(LAS v4f*)(part + i * 1024);
        __builtin_amdgcn_sched_barrier(0);
#pragma unroll
        for (int i = 0; i < 8; ++i) qs[i] += pr[i];
      }
#pragma unroll
      for (int i = 0; i < 8; ++i) *(LAS v4f*)(part + i * 1024) = qs[i];
      LDS_WAIT();
      ctl_add(ctl + 4 * (gw >= 4 ? C_Q2 : C_Q1));
    }
    const int xc = j & 7, idx = j >> 3, ct = (idx + CT_ROT) & 7, rg = idx >> 3;
    unsigned hb = 0;
#define HBAR(SLP) do { hb += NHELP; if (ctl_add(ctl + 4 * C_HB) + 1 < hb) ctl_wait<SLP>(ctl + 4 * C_HB, hb); CFENCE(); } while (0)
    const int pdst = W_P + (idx * 128 + gw * 8 + xc) * 1024;
    v4u wf[16];
    {
      const __amdgpu_buffer_rsrc_t rsW = __builtin_amdgcn_make_buffer_rsrc((void*)wo, 0, DM * NH * DH * 2, 0x00020000);
      ctl_wait<32>(ctl + 4 * C_FULL, WO_AT + 2 * ((gw + 2 * rg) & 7));
#pragma unroll
      for (int ks = 0; ks < 16; ++ks) {
        wf[ks] = __builtin_amdgcn_raw_buffer_load_b128(rsW, g * 2048 + c * 16, ((4 * xc + (ks >> 2)) * 128 + (ks & 3) * 32 + 4 * gw) * 2048 + ct * 256, 0);
        __builtin_amdgcn_s_sleep(WF_SLEEP);
        __builtin_amdgcn_sched_barrier(0);
      }
      LAS char* sw = L + L_STG + gw * PIECE + lane * 16;
      LAS char* sr = L + L_STG + 2 * g * PIECE + (c >> 2) * 256 + gw * 32 + (c & 3) * 8;
#pragma unroll
      for (int r = 0; r < 16; ++r) {
        *(LAS v4u*)(sw) = wf[r];
        LDS_WAIT();
        HBAR(2);
        const v2u a0 = trread(sr), b0 = trread(sr + PIECE);
        wf[r] = v4u{a0[0], a0[1], b0[0], b0[1]};
        asm volatile("" : "+v"(wf[r])::"memory");
        HBAR(2);
      }
    }
    const v2u tagv = *(LAS v2u*)(L + L_CTRL + 4 * C_EP);
    const unsigned long long tag = ((unsigned long long)tagv[1] << 32) | tagv[0];
    unsigned tb[4];
    tag_bytes(tagv, tb);
    ctl_wait<16>(ctl + 4 * C_FULL, NUNIT - 16);
    ctl_wait<2>(ctl + 4 * C_LDONE, 1);
    {
      const int src = (2 * rg + (lane & 1)) * 32 + (gw >> 1) * 8 + xc;
      GAS unsigned long long* fl = (GAS unsigned long long*)(ws + W_CF) + FSTR * src;
      for (unsigned sp = 0; sp < (1u << 16); ++sp) {
        if (__all(__hip_atomic_load(fl, __ATOMIC_RELAXED, __HIP_MEMORY_SCOPE_AGENT) == tag)) break;
        __builtin_amdgcn_s_sleep(G1_SLEEP);
      }
      __builtin_amdgcn_fence(__ATOMIC_ACQUIRE, "wavefront");
    }
#pragma unroll 1
    for (int ph = 0; ph < 2; ++ph) {
      v4u f0, f1;
#pragma unroll 1
      for (unsigned sp = 0; sp < (1u << 16); ++sp) {
        int vo = lane * 16;
        asm volatile("" : "+v"(vo)::"memory");
        f0 = __builtin_amdgcn_raw_buffer_load_b128(rsO, vo, W_O + ((ph * 4 + rg) * 8 + xc) * 16384 + gw * 2048, AUX_SC1);
        f1 = __builtin_amdgcn_raw_buffer_load_b128(rsO, vo, W_O + ((ph * 4 + rg) * 8 + xc) * 16384 + gw * 2048 + 1024, AUX_SC1);
        const unsigned mxv = max(max(max(f0[0], f0[1]), max(f0[2], f0[3])), max(max(f1[0], f1[1]), max(f1[2], f1[3])));
        if (__all(mxv != SENT)) break;
        __builtin_amdgcn_s_sleep(G1_SLEEP);
      }
      *(LAS v4u*)(L + L_OB + gw * 2048 + lane * 16) = f0;
      *(LAS v4u*)(L + L_OB + gw * 2048 + 1024 + lane * 16) = f1;
      LDS_WAIT();
      HBAR(0);
      v4f oa[4];
#pragma unroll
      for (int k = 0; k < 4; ++k) oa[k] = v4f{0.f, 0.f, 0.f, 0.f};
      v4u fa[8], fb[8];
#pragma unroll
      for (int f = 0; f < 8; ++f) { fa[f] = *(LAS v4u*)(L + L_OB + f * 1024 + lane * 16); fb[f] = *(LAS v4u*)(L + L_OB + (8 + f) * 1024 + lane * 16); }
      __builtin_amdgcn_sched_barrier(0);
#pragma unroll
      for (int f = 0; f < 8; ++f) { oa[f & 1] = mfma(wf[f], fa[f], oa[f & 1]); oa[2 + (f & 1)] = mfma(wf[8 + f], fb[f], oa[2 + (f & 1)]); }
      v4u pt = __builtin_bit_cast(v4u, (oa[0] + oa[1]) + (oa[2] + oa[3]));
#pragma unroll
      for (int k = 0; k < 4; ++k) pt[k] = ((pt[k] + ((pt[k] & 0x7F800000u) == 0x7F800000u ? 0u : 0x80u)) & ~0xFFu) | tb[k];
      __builtin_amdgcn_raw_buffer_store_b128(pt, rsO, lane * 16, pdst + ph * 65536, AUX_SC1);
      if (ph == 0) HBAR(0);
    }
    if (gw < 2) {
      GAS v2u* dst = (GAS v2u*)((GAS char*)out + ((size_t)(64 * gw + 16 * rg + c) * DM + 128 * ct + 16 * xc + 4 * g) * 2);
      asm volatile("" : "+v"(dst));
      const v4u tv4 = {tb[0], tb[1], tb[2], tb[3]};
      v2u res;
#pragma unroll 1
      for (unsigned sp = 0; sp < (1u << 16); ++sp) {
        int vo = lane * 16;
        asm volatile("" : "+v"(vo)::"memory");
        v4u q[8];
#pragma unroll
        for (int k = 0; k < 8; ++k) q[k] = __builtin_amdgcn_raw_buffer_load_b128(rsO, vo, W_P + ((idx * 2 + gw) * 64 + xc * 8 + k) * 1024, AUX_SC1);
        v4u bad = q[0] ^ tv4;
        v4f s = __builtin_bit_cast(v4f, bad);
#pragma unroll
        for (int k = 1; k < 8; ++k) { const v4u y = q[k] ^ tv4; bad |= y; s += __builtin_bit_cast(v4f, y); }
        res = v2u{pk2(s[0], s[1]), pk2(s[2], s[3])};
        asm volatile("" : "+v"(res));
        if (__all((((bad[0] | bad[1]) | (bad[2] | bad[3])) & 0xFFu) == 0u)) break;
        __builtin_amdgcn_s_sleep(X2_SLEEP);
      }
      *dst = res;
      if (gw == 0 && (j & 63) == 0 && lane == 0) __hip_atomic_store((GAS unsigned long long*)(ws + W_EPOCH) + FSTR * rg, tag, __ATOMIC_RELAXED, __HIP_MEMORY_SCOPE_AGENT);
    }
  }
}
}

extern "C" void kernel_launch(void* const* d_in, const int* in_sizes, int n_in, void* d_out, int out_size, void* d_ws, size_t ws_size,
                              hipStream_t stream) {
  (void)in_sizes; (void)n_in; (void)out_size; (void)ws_size;
  (void)hipFuncSetAttribute((const void*)mha_block_kernel, hipFuncAttributeMaxDynamicSharedMemorySize, LDS_BYTES);
  mha_block_kernel<<<NWG, NTHR, LDS_BYTES, stream>>>((const unsigned short*)d_in[0], (const unsigned short*)d_in[1], (const unsigned short*)d_in[2],
                                                     (const unsigned short*)d_in[3], (const unsigned short*)d_in[4], (unsigned short*)d_out,
                                                     (unsigned char*)d_ws);
}
```
